```python
import torch

def _softmax(x, axis=-1):
    return torch.softmax(x, dim=axis)
def _gelu(x):
    return torch.nn.functional.gelu(x, approximate="tanh")
def _silu(x):
    return torch.nn.functional.silu(x)
def _sigmoid(x):
    return torch.sigmoid(x)
def _layernorm(x, axis=-1, eps=1e-5):
    m = x.mean(dim=axis, keepdim=True)
    v = x.var(dim=axis, keepdim=True, unbiased=False)
    return (x - m) / torch.sqrt(v + eps)
def _scatter_add(tgt, idx, src, axis=0):
    return tgt.clone().index_add_(axis, idx, src)
def _segment_sum(src, seg, n):
    out = torch.zeros((n,) + src.shape[1:], dtype=src.dtype)
    return out.index_add_(0, seg, src)
def _pad(x, pw):
    flat = tuple(v for pair in reversed(pw) for v in pair)
    return torch.nn.functional.pad(x, flat)

kernel_name = 'sequential_preLN__softmax_full_mha__dense_activation_gelu__prompt'

def setup_inputs():

    _w = (lambda a: (lambda t: t.to(torch.bfloat16) if t.is_floating_point() else t)(torch.as_tensor(a)))
    def _A_setup():
        B, S, D = 1, 2048, 640
        return {
            "x": _w((lambda *s: torch.randn(*s, dtype=torch.bfloat16) * 0.05)(B, S, D)),
            "wqkv": _w((lambda *s: torch.randn(*s, dtype=torch.bfloat16) * 0.05)(D, 3 * D)),
            "wo": _w((lambda *s: torch.randn(*s, dtype=torch.bfloat16) * 0.05)(D, D)),
        }
    _A_in = _A_setup()

    def _B_setup():
        B, S, D, F = 1, 2048, 640, 1728
        return {
            "x": _w((lambda *s: torch.randn(*s, dtype=torch.bfloat16) * 0.05)(B, S, D)),
            "w1": _w((lambda *s: torch.randn(*s, dtype=torch.bfloat16) * 1.6)(D, F) * (D ** -0.5)),
            "b1": _w((lambda *s: torch.randn(*s, dtype=torch.bfloat16) * 1.6)(F) * 0.01),
            "w2": _w((lambda *s: torch.randn(*s, dtype=torch.bfloat16) * 0.2)(F, D) * (F ** -0.5)),
            "b2": _w((lambda *s: torch.randn(*s, dtype=torch.bfloat16) * 0.2)(D) * 0.01),
        }
    _B_in = _B_setup()

    return {
        "A_x": _A_in["x"],
        "A_wqkv": _A_in["wqkv"],
        "A_wo": _A_in["wo"],
        "B_x": _B_in["x"],
        "B_w1": _B_in["w1"],
        "B_b1": _B_in["b1"],
        "B_w2": _B_in["w2"],
        "B_b2": _B_in["b2"],
    }

def reference(A_x, A_wqkv, A_wo, B_x, B_w1, B_b1, B_w2, B_b2):
    def _A_ref(x, wqkv, wo):
        B, S, D = x.shape
        H = 32; Dh = D // H
        qkv = (lambda x, *d: x.permute(*d))((x @ wqkv).reshape(B, S, 3, H, Dh), 2, 0, 3, 1, 4)
        q, k, v = qkv[0], qkv[1], qkv[2]
        scale = Dh ** -0.5
        scores = torch.einsum("bhqd,bhkd->bhqk", q, k) * scale
        p = _softmax(scores, axis=-1)
        o = torch.einsum("bhqk,bhkd->bhqd", p, v)
        return (lambda x, *d: x.permute(*d))(o, 0, 2, 1, 3).reshape(B, S, D) @ wo

    def _B_ref(x, w1, b1, w2, b2):
        h = torch.einsum("bsd,df->bsf", x, w1) + b1
        h = _gelu(h)
        return torch.einsum("bsf,fd->bsd", h, w2) + b2

    x = A_x
    h = x + _A_ref(_layernorm(x, axis=-1), A_wqkv, A_wo)
    h2 = h + _B_ref(_layernorm(h, axis=-1), B_w1, B_b1, B_w2, B_b2)
    return h2
```

```cpp
#include <hip/hip_runtime.h>
#include <hip/hip_bf16.h>
#include <stdint.h>

typedef unsigned short u16;
constexpr int S = 2048, D = 640, H = 32, DH = 20, F = 1728;

__device__ __forceinline__ float bf2f(u16 b) { return __uint_as_float(((unsigned)b) << 16); }
__device__ __forceinline__ u16 f2bf(float f) { unsigned u = __float_as_uint(f); u += 0x7fffu + ((u >> 16) & 1u); return (u16)(u >> 16); }

__global__ void ln_kernel(const u16* x, u16* y) {
    int row = blockIdx.x, t = threadIdx.x;
    float v[10], s = 0.f;
    for (int i = 0; i < 10; ++i) { v[i] = bf2f(x[row * D + t + 64 * i]); s += v[i]; }
    for (int o = 32; o; o >>= 1) s += __shfl_xor(s, o);
    float m = s / D, q = 0.f;
    for (int i = 0; i < 10; ++i) { float d = v[i] - m; q += d * d; }
    for (int o = 32; o; o >>= 1) q += __shfl_xor(q, o);
    float r = rsqrtf(q / D + 1e-5f);
    for (int i = 0; i < 10; ++i) y[row * D + t + 64 * i] = f2bf((v[i] - m) * r);
}

template <int MODE>
__global__ void gemm_kernel(const u16* A, const u16* B, u16* C, const u16* bias, const u16* res, int M, int N, int K) {
    __shared__ float As[16][65], Bs[16][65];
    int tx = threadIdx.x & 15, ty = threadIdx.x >> 4;
    int m0 = blockIdx.y * 64, n0 = blockIdx.x * 64;
    float acc[4][4] = {};
    for (int k0 = 0; k0 < K; k0 += 16) {
        for (int i = threadIdx.x; i < 1024; i += 256) {
            int r = i >> 4, c = i & 15;
            As[c][r] = bf2f(A[(m0 + r) * K + k0 + c]);
            int r2 = i >> 6, c2 = i & 63;
            Bs[r2][c2] = bf2f(B[(k0 + r2) * N + n0 + c2]);
        }
        __syncthreads();
        for (int k = 0; k < 16; ++k) {
            float a[4], b[4];
            for (int i = 0; i < 4; ++i) { a[i] = As[k][ty * 4 + i]; b[i] = Bs[k][tx * 4 + i]; }
            for (int i = 0; i < 4; ++i) for (int j = 0; j < 4; ++j) acc[i][j] += a[i] * b[j];
        }
        __syncthreads();
    }
    for (int i = 0; i < 4; ++i) for (int j = 0; j < 4; ++j) {
        int m = m0 + ty * 4 + i, n = n0 + tx * 4 + j;
        float v = acc[i][j];
        if (MODE >= 2) v += bf2f(bias[n]);
        if (MODE == 2) { float u = 0.7978845608f * (v + 0.044715f * v * v * v); v = 0.5f * v * (1.f + tanhf(u)); }
        if (MODE == 1 || MODE == 3) v += bf2f(res[m * N + n]);
        C[m * N + n] = f2bf(v);
    }
}

__global__ void attn_kernel(const u16* qkv, u16* o) {
    __shared__ float Ks[64][DH], Vs[64][DH];
    int h = blockIdx.y, row = blockIdx.x * 64 + threadIdx.x;
    float q[DH], acc[DH] = {}, m = -1e30f, l = 0.f;
    for (int d = 0; d < DH; ++d) q[d] = bf2f(qkv[row * 3 * D + h * DH + d]) * 0.22360679775f;
    for (int k0 = 0; k0 < S; k0 += 64) {
        __syncthreads();
        for (int d = 0; d < DH; ++d) {
            Ks[threadIdx.x][d] = bf2f(qkv[(k0 + threadIdx.x) * 3 * D + D + h * DH + d]);
            Vs[threadIdx.x][d] = bf2f(qkv[(k0 + threadIdx.x) * 3 * D + 2 * D + h * DH + d]);
        }
        __syncthreads();
        for (int j = 0; j < 64; ++j) {
            float s = 0.f;
            for (int d = 0; d < DH; ++d) s += q[d] * Ks[j][d];
            float mn = fmaxf(m, s), a = __expf(m - mn), p = __expf(s - mn);
            l = l * a + p;
            for (int d = 0; d < DH; ++d) acc[d] = acc[d] * a + p * Vs[j][d];
            m = mn;
        }
    }
    for (int d = 0; d < DH; ++d) o[row * D + h * DH + d] = f2bf(acc[d] / l);
}

extern "C" void kernel_launch(void* const* d_in, const int* in_sizes, int n_in, void* d_out, int out_size, void* d_ws, size_t ws_size, hipStream_t stream) {
    const u16* x = (const u16*)d_in[0]; const u16* wqkv = (const u16*)d_in[1]; const u16* wo = (const u16*)d_in[2];
    const u16* w1 = (const u16*)d_in[4]; const u16* b1 = (const u16*)d_in[5]; const u16* w2 = (const u16*)d_in[6]; const u16* b2 = (const u16*)d_in[7];
    u16* ws = (u16*)d_ws;
    u16* xn = ws; u16* qkv = xn + S * D; u16* o = qkv + S * 3 * D; u16* h = o + S * D; u16* hn = h + S * D; u16* g = hn + S * D;
    ln_kernel<<<S, 64, 0, stream>>>(x, xn);
    gemm_kernel<0><<<dim3(3 * D / 64, S / 64), 256, 0, stream>>>(xn, wqkv, qkv, nullptr, nullptr, S, 3 * D, D);
    attn_kernel<<<dim3(S / 64, H), 64, 0, stream>>>(qkv, o);
    gemm_kernel<1><<<dim3(D / 64, S / 64), 256, 0, stream>>>(o, wo, h, nullptr, x, S, D, D);
    ln_kernel<<<S, 64, 0, stream>>>(h, hn);
    gemm_kernel<2><<<dim3(F / 64, S / 64), 256, 0, stream>>>(hn, w1, g, b1, nullptr, S, F, D);
    gemm_kernel<3><<<dim3(D / 64, S / 64), 256, 0, stream>>>(g, w2, (u16*)d_out, b2, h, S, D, F);
}
```

```cpp
#include <hip/hip_runtime.h>
#include <hip/hip_bf16.h>
#include <stdint.h>

typedef unsigned short u16;
typedef short bf16x8 __attribute__((ext_vector_type(8)));
typedef short s16x4 __attribute__((ext_vector_type(4)));
typedef float f32x16 __attribute__((ext_vector_type(16)));
typedef float f32x8 __attribute__((ext_vector_type(8)));
typedef float f32x4 __attribute__((ext_vector_type(4)));
typedef float f32x2 __attribute__((ext_vector_type(2)));
typedef unsigned u32x8 __attribute__((ext_vector_type(8)));
typedef unsigned u32x4 __attribute__((ext_vector_type(4)));
typedef unsigned u32x2 __attribute__((ext_vector_type(2)));
typedef __bf16 bf2 __attribute__((ext_vector_type(2)));
#define LAS __attribute__((address_space(3)))
#define DI __device__ __forceinline__
typedef LAS char* lptr;

constexpr int SEQ = 2048, DM = 640, NH = 32, FF = 1728;
constexpr int QREC = 1280, KVREC = 2624;

constexpr size_t WS_Q = 0;
constexpr size_t WS_KV = WS_Q + (size_t)NH * 64 * QREC;
constexpr size_t WS_O = WS_KV + (size_t)NH * 64 * KVREC + 16384;
constexpr size_t WS_H = WS_O + (size_t)128 * 20 * 1024;
constexpr size_t WS_G = WS_H + (size_t)128 * 20 * 1024;
constexpr size_t WS_WO = WS_G + (size_t)128 * 54 * 1024;
constexpr size_t WS_W1 = WS_WO + (size_t)8 * 20 * 5 * 1024;
constexpr size_t WS_W2 = WS_W1 + (size_t)108 * 20 * 1024;
constexpr size_t WS_ST = WS_W2 + (size_t)8 * 54 * 5 * 1024;
constexpr size_t WS_CS = WS_ST + (size_t)SEQ * 8 * 8;
constexpr size_t WS_END = WS_CS + (size_t)4 * FF * 4;

template <int I, int N, class F> DI void sfor(F&& f) {
    if constexpr (I < N) { f(std::integral_constant<int, I>{}); sfor<I + 1, N>(f); }
}

DI unsigned pk2(float lo, float hi) { f32x2 v = {lo, hi}; bf2 b = __builtin_convertvector(v, bf2); return __builtin_bit_cast(unsigned, b); }
DI float bflo(unsigned u) { return __uint_as_float(u << 16); }
DI float bfhi(unsigned u) { return __uint_as_float(u & 0xffff0000u); }
DI float rbf(float x) { return bflo(pk2(x, x)); }
DI float dot2(unsigned a, unsigned b, float c) { return __builtin_amdgcn_fdot2_f32_bf16(__builtin_bit_cast(bf2, a), __builtin_bit_cast(bf2, b), c, false); }
DI float swap32_add(float v) { auto r = __builtin_amdgcn_permlane32_swap(__float_as_uint(v), __float_as_uint(v), false, false); return __uint_as_float(r[0]) + __uint_as_float(r[1]); }
DI float swap32_max(float v) { auto r = __builtin_amdgcn_permlane32_swap(__float_as_uint(v), __float_as_uint(v), false, false); return fmaxf(__uint_as_float(r[0]), __uint_as_float(r[1])); }
DI float swap16_add(float v) { auto r = __builtin_amdgcn_permlane16_swap(__float_as_uint(v), __float_as_uint(v), false, false); return __uint_as_float(r[0]) + __uint_as_float(r[1]); }
DI f32x16 mfma32(u32x4 a, u32x4 b, f32x16 c) { return __builtin_amdgcn_mfma_f32_32x32x16_bf16(__builtin_bit_cast(bf16x8, a), __builtin_bit_cast(bf16x8, b), c, 0, 0, 0); }
DI f32x16 mfma32k8(u32x2 a, u32x2 b, f32x16 c) { return __builtin_amdgcn_mfma_f32_32x32x8bf16_1k(__builtin_bit_cast(s16x4, a), __builtin_bit_cast(s16x4, b), c, 0, 0, 0); }
DI f32x4 mfma16(u32x4 a, u32x4 b, f32x4 c) { return __builtin_amdgcn_mfma_f32_16x16x32_bf16(__builtin_bit_cast(bf16x8, a), __builtin_bit_cast(bf16x8, b), c, 0, 0, 0); }
DI u32x4 lds128(lptr p) { return *(LAS u32x4*)p; }
DI u32x2 ldstr(lptr p) { return __builtin_bit_cast(u32x2, __builtin_amdgcn_ds_read_tr16_b64_v4i16((LAS s16x4*)p)); }

template <int OFF> DI void glds(unsigned voff, const void* sbase, unsigned ldsb) {
    asm volatile("s_add_u32 m0, %2, %3\n\ts_nop 0\n\tglobal_load_lds_dwordx4 %0, %1" ::"v"(voff), "s"(sbase), "s"(ldsb), "i"(OFF) : "memory", "scc");
}
template <int IMM> DI void gld128(u32x4& d, unsigned voff, const void* sbase) {
    asm volatile("global_load_dwordx4 %0, %1, %2 offset:%3" : "=v"(d) : "v"(voff), "s"(sbase), "i"(IMM) : "memory");
}
template <int IMM> DI void gld64(u32x2& d, unsigned voff, const void* sbase) {
    asm volatile("global_load_dwordx2 %0, %1, %2 offset:%3" : "=v"(d) : "v"(voff), "s"(sbase), "i"(IMM) : "memory");
}
template <int IMM> DI void gld32(unsigned& d, unsigned voff, const void* sbase) {
    asm volatile("global_load_dword %0, %1, %2 offset:%3" : "=v"(d) : "v"(voff), "s"(sbase), "i"(IMM) : "memory");
}
template <int IMM> DI void gldu16(unsigned& d, unsigned voff, const void* sbase) {
    asm volatile("global_load_ushort %0, %1, %2 offset:%3" : "=v"(d) : "v"(voff), "s"(sbase), "i"(IMM) : "memory");
}
template <int N> DI void wait_vm() { asm volatile("s_waitcnt vmcnt(%0)" ::"i"(N) : "memory"); }
template <int N> DI void wait_vm_bar() { asm volatile("s_waitcnt vmcnt(%0) lgkmcnt(0)\n\ts_barrier" ::"i"(N) : "memory"); }
DI void lds_bar() { asm volatile("s_waitcnt lgkmcnt(0)\n\ts_barrier" ::: "memory"); }
template <class T> DI void pin(T& v) { asm volatile("" : "+v"(v)); }

DI void ln_stats(float s1, float s2, float& m, float& rs) {
    const float mean = s1 * (1.0f / DM);
    const float var = fmaxf(s2 * (1.0f / DM) - mean * mean, 0.0f);
    m = rbf(mean);
    rs = 1.0f / rbf(sqrtf(rbf(rbf(var) + 1e-5f)));
}

constexpr int A_STAGE = 32768, A_LDS = 5 * A_STAGE;

template <bool ISV>
DI void qkv_body(const u16* x, const u16* wqkv, char* ws, lptr lds, int tb, int cg, int w, int lane) {
    const int th = w >> 1, fh = w & 1, hi = lane >> 5, l31 = lane & 31;
    const unsigned ldsb = (unsigned)(size_t)lds;
    unsigned vx[4];
#pragma unroll
    for (int j = 0; j < 4; ++j) {
        const int row = 32 * w + 8 * j + (lane >> 3);
        vx[j] = row * 1280 + (((lane & 7) ^ ((row >> 1) & 7)) << 4);
    }
    const unsigned vw = (lane >> 4) * 3840 + (((lane & 15) ^ ((lane >> 4) << 2)) << 4);
    const char* xs = (const char*)x + (size_t)tb * 128 * 1280;
    const char* wsrc = (const char*)wqkv + (size_t)(16 * w) * 3840 + cg * 256;
    const unsigned ldw = ldsb + w * 4096;
    auto issue = [&](auto S_) {
        constexpr int S = S_.value, SL = (S % 5) * A_STAGE;
        sfor<0, 4>([&](auto J) { glds<SL + J.value * 1024>(vx[J.value], xs + S * 128, ldw); });
        sfor<0, 4>([&](auto J) { glds<SL + 16384 + J.value * 1024>(vw, wsrc + (size_t)(S * 64 + J.value * 4) * 3840, ldw); });
    };
    sfor<0, 5>(issue);

    int xo[2][4], wo[2];
#pragma unroll
    for (int ks = 0; ks < 4; ++ks) {
        const int c = (((2 * ks + hi) ^ ((l31 >> 1) & 7)) << 4);
        xo[0][ks] = (64 * th + 32 * fh + l31) * 128 + c;
        xo[1][ks] = (64 * th + 32 * (1 - fh) + l31) * 128 + c;
    }
    {
        const int q = (lane >> 2) & 3, p = lane & 3, cb = (lane >> 4) & 1;
#pragma unroll
        for (int f = 0; f < 2; ++f) {
            const int ft = 2 * fh + (f == 0 ? th : 1 - th);
            wo[f] = 16384 + (8 * hi + q) * 256 + (((4 * (ft ^ q)) + 2 * cb + (p >> 1)) << 4) + 8 * (p & 1);
        }
    }
    f32x16 acc[2][2] = {};
    float s1 = 0.f, s2 = 0.f, cs = 0.f;
    u32x4 xf[2][2], wf[2][2];
    auto rd = [&](auto S_, auto K_) {
        constexpr int SL = (S_.value % 5) * A_STAGE, KS = K_.value, B = KS & 1;
        xf[B][0] = lds128(lds + SL + xo[0][KS]);
        xf[B][1] = lds128(lds + SL + xo[1][KS]);
#pragma unroll
        for (int f = 0; f < 2; ++f) {
            const u32x2 a = ldstr(lds + SL + KS * 4096 + wo[f]), b = ldstr(lds + SL + KS * 4096 + 1024 + wo[f]);
            wf[B][f] = u32x4{a[0], a[1], b[0], b[1]};
        }
    };
    auto mm = [&](auto K_) {
        constexpr int B = K_.value & 1;
#pragma unroll
        for (int t = 0; t < 2; ++t)
#pragma unroll
            for (int f = 0; f < 2; ++f) acc[t][f] = ISV ? mfma32(xf[B][t], wf[B][f], acc[t][f]) : mfma32(wf[B][f], xf[B][t], acc[t][f]);
#pragma unroll
        for (int e = 0; e < 4; ++e) {
            const unsigned xv = xf[B][0][e], wv = wf[B][0][e];
            s1 = dot2(xv, 0x3f803f80u, s1);
            s2 = dot2(xv, xv, s2);
            cs = dot2(wv, 0x3f803f80u, cs);
        }
    };
    using I0 = std::integral_constant<int, 0>; using I1 = std::integral_constant<int, 1>;
    using I2 = std::integral_constant<int, 2>; using I3 = std::integral_constant<int, 3>;
    wait_vm_bar<32>();
    rd(I0{}, I0{});
    sfor<0, 10>([&](auto S_) {
        constexpr int S = S_.value;
        rd(S_, I1{}); mm(I0{});
        rd(S_, I2{}); mm(I1{});
        rd(S_, I3{}); mm(I2{});
        if constexpr (S < 9) {
            wait_vm_bar<8 * ((S + 4 < 9 ? S + 4 : 9) - (S + 1))>();
            if constexpr (S + 5 < 10) issue(std::integral_constant<int, S + 5>{});
            rd(std::integral_constant<int, S + 1>{}, I0{});
        }
        mm(I3{});
    });
    lds_bar();
    LAS float* T1 = (LAS float*)lds;
    LAS float* T2 = T1 + 128;
    LAS float* TC = T1 + 256;
    LAS f32x2* TS = (LAS f32x2*)(T1 + 384) + w * 64;
    s1 = swap32_add(s1); s2 = swap32_add(s2); cs = swap32_add(cs);
    if (hi == 0) { T1[64 * th + 32 * fh + l31] = s1; T2[64 * th + 32 * fh + l31] = s2; TC[64 * fh + 32 * th + l31] = cs; }
    lds_bar();
    {
        float m, rs;
        ln_stats(T1[64 * th + lane], T2[64 * th + lane], m, rs);
        TS[lane] = f32x2{m, rs};
    }
    asm volatile("s_waitcnt lgkmcnt(0)" ::: "memory");
    if constexpr (!ISV) {
        const bool isq = cg < 5;
        const float sc = isq ? 0.22360679774997896f * 1.4426950408889634f : 1.0f;
        char* dst = ws + (isq ? WS_Q : WS_KV);
        const int rec = isq ? QREC : KVREC, cgm = isq ? cg : cg - 5;
#pragma unroll
        for (int t = 0; t < 2; ++t) {
            const int ta = t == 0 ? fh : 1 - fh;
            const f32x2 st = TS[32 * ta + l31];
            const float m = st[0], rs = st[1] * sc;
            const int tt = 4 * tb + 2 * th + ta;
#pragma unroll
            for (int f = 0; f < 2; ++f) {
                const int fa = f == 0 ? th : 1 - th;
#pragma unroll
                for (int g = 0; g < 4; ++g) {
                    const int n = 64 * fh + 32 * fa + 8 * g + 4 * hi;
                    const f32x4 c4 = *(LAS f32x4*)(TC + n);
                    const int gq = 32 * cgm + (n >> 2), head = (gq * 205) >> 10, dq = gq - 5 * head;
                    const int off = (head * 64 + tt) * rec + (dq < 4 ? ((dq >> 1) * 32 + l31) * 16 + (dq & 1) * 8 : 1024 + l31 * 8);
                    u32x2 o;
                    o[0] = pk2((acc[t][f][4 * g] - m * c4[0]) * rs, (acc[t][f][4 * g + 1] - m * c4[1]) * rs);
                    o[1] = pk2((acc[t][f][4 * g + 2] - m * c4[2]) * rs, (acc[t][f][4 * g + 3] - m * c4[3]) * rs);
                    *(u32x2*)(dst + off) = o;
                }
            }
        }
    } else {
        char* dst = ws + WS_KV;
#pragma unroll
        for (int f = 0; f < 2; ++f) {
            const int fa = f == 0 ? th : 1 - th;
            const int n = 64 * fh + 32 * fa + l31;
            const float c = TC[n];
            const int col = 128 * (cg - 10) + n, head = (col * 3277) >> 16, d = col - 20 * head;
#pragma unroll
            for (int t = 0; t < 2; ++t) {
                const int ta = t == 0 ? fh : 1 - fh;
                const int tt = 4 * tb + 2 * th + ta;
#pragma unroll
                for (int s = 0; s < 2; ++s) {
                    u32x4 o;
#pragma unroll
                    for (int g = 0; g < 2; ++g) {
                        const int tok = 32 * ta + 16 * s + 8 * g + 4 * hi, r = 8 * s + 4 * g;
                        const f32x4 a = *(LAS f32x4*)(TS + tok), b = *(LAS f32x4*)(TS + tok + 2);
                        o[2 * g] = pk2((acc[t][f][r] - a[0] * c) * a[1], (acc[t][f][r + 1] - a[2] * c) * a[3]);
                        o[2 * g + 1] = pk2((acc[t][f][r + 2] - b[0] * c) * b[1], (acc[t][f][r + 3] - b[2] * c) * b[3]);
                    }
                    *(u32x4*)(dst + (head * 64 + tt) * KVREC + 1280 + (2 * s + hi) * 336 + d * 16) = o;
                }
            }
        }
    }
}

__global__ __launch_bounds__(256, 1) void k_qkv(const u16* x, const u16* wqkv, char* ws) {
    extern __shared__ __attribute__((aligned(16))) char smem[];
    const int b = blockIdx.x, xcd = b & 7, i = b >> 3;
    const int tb = 4 * (xcd >> 1) + (i & 3), cg = (xcd & 1) * 8 + (i >> 2);
    const int w = __builtin_amdgcn_readfirstlane(threadIdx.x >> 6), lane = threadIdx.x & 63;
    if (cg == 15) {
#pragma unroll
        for (int j = 0; j < 2; ++j) {
            const int e = tb * 512 + j * 256 + threadIdx.x;
            *(u32x4*)(ws + WS_KV + (size_t)(e >> 2) * KVREC + 1280 + (e & 3) * 336 + 320) = u32x4{0x3f803f80u, 0x3f803f80u, 0x3f803f80u, 0x3f803f80u};
        }
        return;
    }
    if (cg >= 10) qkv_body<true>(x, wqkv, ws, (lptr)smem, tb, cg, w, lane);
    else qkv_body<false>(x, wqkv, ws, (lptr)smem, tb, cg, w, lane);
}

#define ATTN_BODY_ASM \
    "v_mfma_f32_32x32x16_bf16 v[80:95], v[188:191], v[180:183], v[144:159]\n\t" \
    "v_exp_f32 v64, v64\n\t" \
    "v_exp_f32 v65, v65\n\t" \
    "v_exp_f32 v66, v66\n\t" \
    "v_cvt_pk_bf16_f32 v160, v64, v65\n\t" \
    "v_exp_f32 v67, v67\n\t" \
    "v_mfma_f32_32x32x8_bf16 v[80:95], v[196:197], v[186:187], v[80:95]\n\t" \
    "global_load_dwordx4 v[188:191], %[vk16], %[sb] offset:0\n\t" \
    "global_load_dwordx2 v[196:197], %[vk8], %[sb] offset:0\n\t" \
    "v_cvt_pk_bf16_f32 v161, v66, v67\n\t" \
    "v_exp_f32 v68, v68\n\t" \
    "v_exp_f32 v69, v69\n\t" \
    "v_exp_f32 v70, v70\n\t" \
    "v_cvt_pk_bf16_f32 v162, v68, v69\n\t" \
    "v_exp_f32 v71, v71\n\t" \
    "v_mfma_f32_32x32x16_bf16 v[112:127], v[208:211], v[168:171], v[112:127]\n\t" \
    "v_cvt_pk_bf16_f32 v163, v70, v71\n\t" \
    "v_exp_f32 v72, v72\n\t" \
    "v_exp_f32 v73, v73\n\t" \
    "v_exp_f32 v74, v74\n\t" \
    "v_cvt_pk_bf16_f32 v164, v72, v73\n\t" \
    "v_exp_f32 v75, v75\n\t" \
    "v_mfma_f32_32x32x16_bf16 v[112:127], v[212:215], v[172:175], v[112:127]\n\t" \
    "global_load_dwordx4 v[208:211], %[vv], %[sb] offset:-2624\n\t" \
    "global_load_dwordx4 v[212:215], %[vv], %[sb] offset:-1952\n\t" \
    "v_cvt_pk_bf16_f32 v165, v74, v75\n\t" \
    "v_exp_f32 v76, v76\n\t" \
    "v_exp_f32 v77, v77\n\t" \
    "v_exp_f32 v78, v78\n\t" \
    "v_cvt_pk_bf16_f32 v166, v76, v77\n\t" \
    "v_exp_f32 v79, v79\n\t" \
    "s_waitcnt vmcnt(4)\n\t" \
    "v_cvt_pk_bf16_f32 v167, v78, v79\n\t" \
    "v_mfma_f32_32x32x16_bf16 v[64:79], v[192:195], v[176:179], v[128:143]\n\t" \
    "v_exp_f32 v80, v80\n\t" \
    "v_exp_f32 v81, v81\n\t" \
    "v_exp_f32 v82, v82\n\t" \
    "v_cvt_pk_bf16_f32 v168, v80, v81\n\t" \
    "v_exp_f32 v83, v83\n\t" \
    "v_mfma_f32_32x32x8_bf16 v[64:79], v[198:199], v[184:185], v[64:79]\n\t" \
    "v_cvt_pk_bf16_f32 v169, v82, v83\n\t" \
    "v_exp_f32 v84, v84\n\t" \
    "v_exp_f32 v85, v85\n\t" \
    "v_exp_f32 v86, v86\n\t" \
    "v_cvt_pk_bf16_f32 v170, v84, v85\n\t" \
    "v_exp_f32 v87, v87\n\t" \
    "v_mfma_f32_32x32x16_bf16 v[96:111], v[200:203], v[160:163], v[96:111]\n\t" \
    "v_cvt_pk_bf16_f32 v171, v86, v87\n\t" \
    "v_exp_f32 v88, v88\n\t" \
    "v_exp_f32 v89, v89\n\t" \
    "v_exp_f32 v90, v90\n\t" \
    "v_cvt_pk_bf16_f32 v172, v88, v89\n\t" \
    "v_exp_f32 v91, v91\n\t" \
    "v_mfma_f32_32x32x16_bf16 v[96:111], v[204:207], v[164:167], v[96:111]\n\t" \
    "v_cvt_pk_bf16_f32 v173, v90, v91\n\t" \
    "v_exp_f32 v92, v92\n\t" \
    "v_exp_f32 v93, v93\n\t" \
    "v_exp_f32 v94, v94\n\t" \
    "v_cvt_pk_bf16_f32 v174, v92, v93\n\t" \
    "v_exp_f32 v95, v95\n\t" \
    "s_nop 0\n\t" \
    "v_cvt_pk_bf16_f32 v175, v94, v95\n\t" \
    "v_mfma_f32_32x32x16_bf16 v[80:95], v[192:195], v[180:183], v[144:159]\n\t" \
    "v_exp_f32 v64, v64\n\t" \
    "v_exp_f32 v65, v65\n\t" \
    "v_exp_f32 v66, v66\n\t" \
    "v_cvt_pk_bf16_f32 v160, v64, v65\n\t" \
    "v_exp_f32 v67, v67\n\t" \
    "v_mfma_f32_32x32x8_bf16 v[80:95], v[198:199], v[186:187], v[80:95]\n\t" \
    "global_load_dwordx4 v[192:195], %[vk16], %[sb] offset:2624\n\t" \
    "global_load_dwordx2 v[198:199], %[vk8], %[sb] offset:2624\n\t" \
    "v_cvt_pk_bf16_f32 v161, v66, v67\n\t" \
    "v_exp_f32 v68, v68\n\t" \
    "v_exp_f32 v69, v69\n\t" \
    "v_exp_f32 v70, v70\n\t" \
    "v_cvt_pk_bf16_f32 v162, v68, v69\n\t" \
    "v_exp_f32 v71, v71\n\t" \
    "v_mfma_f32_32x32x16_bf16 v[112:127], v[200:203], v[168:171], v[112:127]\n\t" \
    "v_cvt_pk_bf16_f32 v163, v70, v71\n\t" \
    "v_exp_f32 v72, v72\n\t" \
    "v_exp_f32 v73, v73\n\t" \
    "v_exp_f32 v74, v74\n\t" \
    "v_cvt_pk_bf16_f32 v164, v72, v73\n\t" \
    "v_exp_f32 v75, v75\n\t" \
    "v_mfma_f32_32x32x16_bf16 v[112:127], v[204:207], v[172:175], v[112:127]\n\t" \
    "global_load_dwordx4 v[200:203], %[vv], %[sb] offset:0\n\t" \
    "global_load_dwordx4 v[204:207], %[vv], %[sb] offset:672\n\t" \
    "v_cvt_pk_bf16_f32 v165, v74, v75\n\t" \
    "v_exp_f32 v76, v76\n\t" \
    "v_exp_f32 v77, v77\n\t" \
    "v_exp_f32 v78, v78\n\t" \
    "v_cvt_pk_bf16_f32 v166, v76, v77\n\t" \
    "v_exp_f32 v79, v79\n\t" \
    "s_waitcnt vmcnt(4)\n\t" \
    "v_cvt_pk_bf16_f32 v167, v78, v79\n\t" \
    "v_mfma_f32_32x32x16_bf16 v[64:79], v[188:191], v[176:179], v[128:143]\n\t" \
    "v_exp_f32 v80, v80\n\t" \
    "v_exp_f32 v81, v81\n\t" \
    "v_exp_f32 v82, v82\n\t" \
    "v_cvt_pk_bf16_f32 v168, v80, v81\n\t" \
    "v_exp_f32 v83, v83\n\t" \
    "v_mfma_f32_32x32x8_bf16 v[64:79], v[196:197], v[184:185], v[64:79]\n\t" \
    "v_cvt_pk_bf16_f32 v169, v82, v83\n\t" \
    "v_exp_f32 v84, v84\n\t" \
    "v_exp_f32 v85, v85\n\t" \
    "v_exp_f32 v86, v86\n\t" \
    "v_cvt_pk_bf16_f32 v170, v84, v85\n\t" \
    "v_exp_f32 v87, v87\n\t" \
    "v_mfma_f32_32x32x16_bf16 v[96:111], v[208:211], v[160:163], v[96:111]\n\t" \
    "v_cvt_pk_bf16_f32 v171, v86, v87\n\t" \
    "v_exp_f32 v88, v88\n\t" \
    "v_exp_f32 v89, v89\n\t" \
    "v_exp_f32 v90, v90\n\t" \
    "v_cvt_pk_bf16_f32 v172, v88, v89\n\t" \
    "v_exp_f32 v91, v91\n\t" \
    "v_mfma_f32_32x32x16_bf16 v[96:111], v[212:215], v[164:167], v[96:111]\n\t" \
    "v_cvt_pk_bf16_f32 v173, v90, v91\n\t" \
    "v_exp_f32 v92, v92\n\t" \
    "v_exp_f32 v93, v93\n\t" \
    "v_exp_f32 v94, v94\n\t" \
    "v_cvt_pk_bf16_f32 v174, v92, v93\n\t" \
    "v_exp_f32 v95, v95\n\t" \
    "s_nop 0\n\t" \
    "v_cvt_pk_bf16_f32 v175, v94, v95\n\t"


constexpr int B_X = 0, B_STG = 32768, B_CS = B_STG + 5 * 4096, B_LDS = B_CS + 5 * 256;

DI int up_start(int c) { return 27 * (c >> 2) + 7 * (c & 3); }

DI void attn_slow(const char* kv, unsigned vk16, unsigned vk8, unsigned vv, u32x4 q16, u32x2 q8, f32x16& o, float& m) {
    o = f32x16{};
    m = -3.0e38f;
    for (int t = 0; t < 32; ++t) {
        const char* r = kv + t * KVREC;
        const u32x4 k16 = *(const u32x4*)(r + vk16), v0 = *(const u32x4*)(r + vv), v1 = *(const u32x4*)(r + vv + 672);
        const u32x2 k8 = *(const u32x2*)(r + vk8);
        f32x16 s = mfma32k8(k8, q8, mfma32(k16, q16, f32x16{}));
        float mx = s[0];
#pragma unroll
        for (int i = 1; i < 16; ++i) mx = fmaxf(mx, s[i]);
        const float mn = fmaxf(m, swap32_max(mx));
        const float al = __builtin_amdgcn_exp2f(m - mn);
        m = mn;
        u32x4 p0, p1;
#pragma unroll
        for (int i = 0; i < 4; ++i) {
            p0[i] = pk2(__builtin_amdgcn_exp2f(s[2 * i] - mn), __builtin_amdgcn_exp2f(s[2 * i + 1] - mn));
            p1[i] = pk2(__builtin_amdgcn_exp2f(s[8 + 2 * i] - mn), __builtin_amdgcn_exp2f(s[9 + 2 * i] - mn));
        }
#pragma unroll
        for (int i = 0; i < 16; ++i) o[i] *= al;
        o = mfma32(v1, p1, mfma32(v0, p0, o));
    }
}

__global__ __launch_bounds__(512, 2) void k_attn(const u16* wo, const u16* w1, const u16* w2, char* ws) {
    extern __shared__ __attribute__((aligned(16))) char smem[];
    const lptr lds = (lptr)smem;
    const unsigned ldsb = (unsigned)(size_t)lds;
    const int b = blockIdx.x, head = 4 * (b & 7) + ((b >> 3) & 3), qt = b >> 5;
    const int w = __builtin_amdgcn_readfirstlane(threadIdx.x >> 6), lane = threadIdx.x & 63;
    const int qg = w & 3, kh = w >> 2, hi = lane >> 5, l31 = lane & 31;

    const int tta = 8 * qt + 2 * qg + kh, ttb = 8 * qt + 2 * qg + 1 - kh;
    const char* qa = ws + WS_Q + (size_t)(head * 64 + tta) * QREC;
    const char* qb = ws + WS_Q + (size_t)(head * 64 + ttb) * QREC;
    const char* kv = ws + WS_KV + (size_t)(head * 64 + 32 * kh) * KVREC;
    const unsigned vk16 = lane * 16, vk8 = 1024 + l31 * 8, vv = 1280 + hi * 336 + (l31 < 20 ? l31 : 20) * 16;

    u32x4 QA16, QB16, K16a, K16b, V0a, V1a, V0b, V1b;
    u32x2 QA8, QB8, K8a, K8b;
    gld128<0>(QA16, vk16, qa); gld64<0>(QA8, vk8, qa);
    gld128<0>(QB16, vk16, qb); gld64<0>(QB8, vk8, qb);
    gld128<0>(K16a, vk16, kv); gld64<0>(K8a, vk8, kv);
    gld128<KVREC>(K16b, vk16, kv); gld64<KVREC>(K8b, vk8, kv);
    gld128<0>(V0a, vv, kv); gld128<672>(V1a, vv, kv);

    int kap, col64, kind;
    if (b < 108) { kind = 1; col64 = b % 27; kap = 5 * (b / 27) + w; }
    else if (b < 148) { kind = 0; col64 = (b - 108) % 10; kap = 5 * ((b - 108) / 10) + w; }
    else { kind = 2; const int T = 5 * (b - 148) + w; col64 = T % 10; kap = T / 10; }

    asm volatile("s_waitcnt vmcnt(4)" : "+v"(QA16), "+v"(QA8), "+v"(QB16), "+v"(QB8), "+v"(K16a), "+v"(K8a)::"memory");
    if (hi) { QA8 = u32x2{0u, 0u}; QB8 = u32x2{0u, 0u}; }
    f32x16 SA = mfma32k8(K8a, QA8, mfma32(K16a, QA16, f32x16{}));
    f32x16 SB = mfma32k8(K8a, QB8, mfma32(K16a, QB16, f32x16{}));
    float ma = SA[0], mb = SB[0];
#pragma unroll
    for (int i = 1; i < 16; ++i) { ma = fmaxf(ma, SA[i]); mb = fmaxf(mb, SB[i]); }
    ma = swap32_max(ma); mb = swap32_max(mb);
    f32x16 CA, CB, OA = {}, OB = {};
#pragma unroll
    for (int i = 0; i < 16; ++i) { SA[i] -= ma; CA[i] = -ma; CB[i] = -mb; }
    u32x8 PA = {}, PB = {};
    V0b = u32x4{0u, 0u, 0u, 0u}; V1b = V0b;
    const char* sb = kv + 2 * KVREC;
    asm volatile("s_nop 7" : "+v"(SA), "+v"(SB));
    for (int it = 0; it < 16; ++it) {
        asm volatile(ATTN_BODY_ASM
                     : "+{v[64:79]}"(SA), "+{v[80:95]}"(SB), "+{v[96:111]}"(OA), "+{v[112:127]}"(OB), "+{v[160:167]}"(PA), "+{v[168:175]}"(PB),
                       "+{v[188:191]}"(K16a), "+{v[192:195]}"(K16b), "+{v[196:197]}"(K8a), "+{v[198:199]}"(K8b),
                       "+{v[200:203]}"(V0a), "+{v[204:207]}"(V1a), "+{v[208:211]}"(V0b), "+{v[212:215]}"(V1b)
                     : "{v[128:143]}"(CA), "{v[144:159]}"(CB), "{v[176:179]}"(QA16), "{v[180:183]}"(QB16), "{v[184:185]}"(QA8), "{v[186:187]}"(QB8),
                       [vk16] "v"(vk16), [vk8] "v"(vk8), [vv] "v"(vv), [sb] "s"(sb)
                     : "memory");
        sb += 2 * KVREC;
        if (it == 2 && w < 5) {
            const char* src = kind == 0 ? (const char*)wo : kind == 1 ? (const char*)w1 : (const char*)w2;
            const int pitch = kind == 1 ? FF * 2 : DM * 2;
            sfor<0, 4>([&](auto J) {
                const int rho = 8 * J.value + (lane >> 3), t = J.value >> 1, g = (rho >> 2) & 3, q = rho & 3;
                int k;
                if (kind == 0) {
                    const int G = kap / 5, r = kap - 5 * G, u = 4 * r + g;
                    k = r < 4 ? 20 * (8 * G + (u >> 1)) + 8 * t + 4 * (u & 1) + q : 20 * (8 * G + 2 * g + t) + 16 + q;
                } else if (kind == 1) {
                    const int ft = kap < 16 ? 5 * (kap >> 1) + 2 * (kap & 1) + t : 5 * (2 * (kap - 16) + t) + 4;
                    k = 16 * ft + 4 * g + q;
                } else {
                    const int lam = 2 * (kap - 48) + t;
                    const int ft = kap < 48 ? up_start(kap / 3) + 2 * (kap % 3) + t : up_start(4 * (lam / 3) + lam % 3) + 6;
                    k = 16 * ft + 4 * g + q;
                }
                glds<B_STG + J.value * 1024>(k * pitch + (((lane & 7) ^ (2 * ((rho >> 1) & 3))) << 4), src + col64 * 128, ldsb + w * 4096);
            });
        }
        if (it == 9 && w < 5) {
            const int g = lane >> 4, q = (lane >> 2) & 3, p = lane & 3;
            const lptr st = lds + B_STG + w * 4096 + (4 * g + q) * 128 + 8 * (p & 1);
            const int sw = 2 * (((4 * g + q) >> 1) & 3);
#pragma unroll
            for (int j = 0; j < 4; ++j) {
                const int pos = ((2 * j + (p >> 1)) ^ sw) << 4;
                const u32x2 a = ldstr(st + pos), c = ldstr(st + 2048 + pos);
                const int ft = 4 * col64 + j;
                const size_t piece = kind == 0 ? WS_WO + (size_t)(((ft / 5) * 20 + kap) * 5 + ft % 5) * 1024
                                   : kind == 1 ? WS_W1 + (size_t)(ft * 20 + kap) * 1024
                                               : WS_W2 + (size_t)(((ft / 5) * 54 + kap) * 5 + ft % 5) * 1024;
                *(u32x4*)(ws + piece + lane * 16) = u32x4{a[0], a[1], c[0], c[1]};
                if (kind == 1) {
                    const unsigned a0 = a[0], a1 = a[1], c0 = c[0], c1 = c[1];
                    float s = dot2(a0, 0x3f803f80u, dot2(a1, 0x3f803f80u, dot2(c0, 0x3f803f80u, dot2(c1, 0x3f803f80u, 0.f))));
                    s = swap32_add(swap16_add(s));
                    if (lane < 16) *(LAS float*)(lds + B_CS + w * 256 + (16 * j + lane) * 4) = s;
                }
            }
        }
    }
    asm volatile("s_waitcnt vmcnt(0)\n\t"
                 "v_mfma_f32_32x32x16_bf16 v[112:127], v[208:211], v[168:171], v[112:127]\n\t"
                 "v_mfma_f32_32x32x16_bf16 v[112:127], v[212:215], v[172:175], v[112:127]\n\t"
                 "s_nop 15"
                 : "+{v[64:79]}"(SA), "+{v[96:111]}"(OA), "+{v[112:127]}"(OB), "+{v[168:175]}"(PB), "+{v[188:191]}"(K16a), "+{v[192:195]}"(K16b), "+{v[196:197]}"(K8a),
                   "+{v[198:199]}"(K8b), "+{v[200:203]}"(V0a), "+{v[204:207]}"(V1a), "+{v[208:211]}"(V0b), "+{v[212:215]}"(V1b)::"memory");

    {
        float big = fmaxf(fabsf(OA[12]), fabsf(OB[12]));
        bool ok = big <= 1.8e19f;
#pragma unroll
        for (int i = 0; i < 12; ++i) ok = ok && fabsf(OA[i]) <= 3.0e38f && fabsf(OB[i]) <= 3.0e38f;
        if (__builtin_expect(__any(!ok), 0)) {
            attn_slow(kv, vk16, vk8, vv, QA16, QA8, OA, ma);
            attn_slow(kv, vk16, vk8, vv, QB16, QB8, OB, mb);
        }
    }
    {
        const lptr xw = lds + B_X + w * 4096 + lane * 16;
        *(LAS f32x4*)(xw) = f32x4{OB[0], OB[1], OB[2], OB[3]};
        *(LAS f32x4*)(xw + 1024) = f32x4{OB[4], OB[5], OB[6], OB[7]};
        *(LAS f32x4*)(xw + 2048) = f32x4{OB[8], OB[9], OB[10], OB[11]};
        *(LAS f32x4*)(xw + 3072) = f32x4{OB[12], mb, 0.f, 0.f};
    }
    lds_bar();
    {
        const lptr xr = lds + B_X + (w ^ 4) * 4096 + lane * 16;
        const f32x4 e0 = *(LAS f32x4*)(xr), e1 = *(LAS f32x4*)(xr + 1024), e2 = *(LAS f32x4*)(xr + 2048), e3 = *(LAS f32x4*)(xr + 3072);
        const float M = fmaxf(ma, e3[1]);
        float fa = __builtin_amdgcn_exp2f(ma - M), fb = __builtin_amdgcn_exp2f(e3[1] - M);
        const float inv = 1.0f / (OA[12] * fa + e3[0] * fb);
        fa *= inv; fb *= inv;
        u32x4 o16; u32x2 o8;
        o16[0] = pk2(OA[0] * fa + e0[0] * fb, OA[1] * fa + e0[1] * fb); o16[1] = pk2(OA[2] * fa + e0[2] * fb, OA[3] * fa + e0[3] * fb);
        o16[2] = pk2(OA[4] * fa + e1[0] * fb, OA[5] * fa + e1[1] * fb); o16[3] = pk2(OA[6] * fa + e1[2] * fb, OA[7] * fa + e1[3] * fb);
        o8[0] = pk2(OA[8] * fa + e2[0] * fb, OA[9] * fa + e2[1] * fb); o8[1] = pk2(OA[10] * fa + e2[2] * fb, OA[11] * fa + e2[3] * fb);
        const int tt16 = 2 * tta + (l31 >> 4), tk = l31 & 15, G = head >> 3, j = head & 7, u = 2 * j + hi;
        char* ob = ws + WS_O + (size_t)(tt16 * 20 + 5 * G) * 1024;
        *(u32x4*)(ob + (u >> 2) * 1024 + ((u & 3) * 16 + tk) * 16) = o16;
        if (hi == 0) *(u32x2*)(ob + 4096 + ((j >> 1) * 16 + tk) * 16 + (j & 1) * 8) = o8;
    }
    if (b < 108 && w == 0) {
        LAS float* c = (LAS float*)(lds + B_CS) + lane;
        ((float*)(ws + WS_CS))[(b / 27) * FF + 64 * col64 + lane] = (c[0] + c[64]) + (c[128] + c[192]) + c[256];
    }
}

constexpr int C_LDS = 100 * 1024;

__global__ __launch_bounds__(256, 1) void k_wo(const u16* x, char* ws) {
    extern __shared__ __attribute__((aligned(16))) char smem[];
    const lptr lds = (lptr)smem;
    const unsigned ldsb = (unsigned)(size_t)lds;
    const int b = blockIdx.x, tb = 4 * (b & 7) + ((b >> 3) & 3), cg = b >> 5;
    const int w = __builtin_amdgcn_readfirstlane(threadIdx.x >> 6), lane = threadIdx.x & 63;
    const int tt16 = 4 * tb + w, tk = lane & 15, g = lane >> 4;
    const unsigned vl = lane * 16;
    const char* wsrc = ws + WS_WO + (size_t)cg * 100 * 1024 + w * 5120;
    const char* osrc = ws + WS_O + (size_t)tt16 * 20 * 1024;
    u32x4 of[20];
    sfor<0, 5>([&](auto S_) {
        constexpr int S = S_.value;
        sfor<0, 5>([&](auto J) { glds<S * 20480 + J.value * 1024>(vl, wsrc + S * 20480 + J.value * 1024, ldsb + w * 5120); });
        sfor<0, 4>([&](auto J) { gld128<J.value * 1024>(of[4 * S + J.value], vl, osrc + S * 4096); });
    });
    u32x2 xr[5];
    const unsigned vxr = (16 * tt16 + tk) * 1280 + 8 * g;
    sfor<0, 5>([&](auto I) { gld64<32 * I.value>(xr[I.value], vxr, (const char*)x + cg * 160); });
    f32x4 acc[5] = {};
    sfor<0, 5>([&](auto S_) {
        constexpr int S = S_.value;
        wait_vm_bar<5 + 9 * (4 - S)>();
        sfor<0, 4>([&](auto J) { pin(of[4 * S + J.value]); });
#pragma unroll
        for (int k = 0; k < 4; ++k)
#pragma unroll
            for (int i = 0; i < 5; ++i) acc[i] = mfma16(lds128(lds + ((4 * S + k) * 5 + i) * 1024 + vl), of[4 * S + k], acc[i]);
    });
    asm volatile("s_waitcnt vmcnt(0)" : "+v"(xr[0]), "+v"(xr[1]), "+v"(xr[2]), "+v"(xr[3]), "+v"(xr[4])::"memory");
    u32x2 h[5];
    float s1 = 0.f, s2 = 0.f;
#pragma unroll
    for (int i = 0; i < 5; ++i) {
        const unsigned a0 = pk2(acc[i][0], acc[i][1]), a1 = pk2(acc[i][2], acc[i][3]), x0 = xr[i][0], x1 = xr[i][1];
        const unsigned h0 = pk2(bflo(x0) + bflo(a0), bfhi(x0) + bfhi(a0)), h1 = pk2(bflo(x1) + bflo(a1), bfhi(x1) + bfhi(a1));
        h[i] = u32x2{h0, h1};
        s1 = dot2(h0, 0x3f803f80u, dot2(h1, 0x3f803f80u, s1));
        s2 = dot2(h0, h0, dot2(h1, h1, s2));
    }
    char* hb = ws + WS_H + (size_t)tt16 * 20 * 1024 + vl;
    *(u32x4*)(hb + (2 * cg) * 1024) = u32x4{h[0][0], h[0][1], h[1][0], h[1][1]};
    *(u32x4*)(hb + (2 * cg + 1) * 1024) = u32x4{h[2][0], h[2][1], h[3][0], h[3][1]};
    *(u32x2*)(hb + (16 + (cg >> 1)) * 1024 + (cg & 1) * 8) = h[4];
    s1 = swap32_add(swap16_add(s1)); s2 = swap32_add(swap16_add(s2));
    if (g == 0) *(f32x2*)(ws + WS_ST + (size_t)(16 * tt16 + tk) * 64 + cg * 8) = f32x2{s1, s2};
}

constexpr int D_TAB = 140 * 1024, D_LDS = D_TAB + 1024;

DI float gelu_tanh(float a) {
    const float z = a * fmaf(a * a, -0.044715f * 2.0f * 0.7978845608028654f * 1.4426950408889634f, -2.0f * 0.7978845608028654f * 1.4426950408889634f);
    return a * __builtin_amdgcn_rcpf(1.0f + __builtin_amdgcn_exp2f(z));
}

__global__ __launch_bounds__(256, 1) void k_up(const u16* b1, char* ws) {
    extern __shared__ __attribute__((aligned(16))) char smem[];
    const lptr lds = (lptr)smem;
    const unsigned ldsb = (unsigned)(size_t)lds;
    const int b = blockIdx.x, tb = 2 * (b & 7) + ((b >> 3) & 1), c = b >> 4;
    const int w = __builtin_amdgcn_readfirstlane(threadIdx.x >> 6), lane = threadIdx.x & 63;
    const int tt16 = 8 * tb + 2 * w, tk = lane & 15, g = lane >> 4;
    const int ft0 = up_start(c);
    const bool has7 = (c & 3) != 3;
    const unsigned vl = lane * 16;
    const char* wsrc = ws + WS_W1 + (size_t)(ft0 * 20 + w) * 1024;
    const char* w7 = wsrc + (has7 ? 6 : 5) * 20480;
    const char* hsrc = ws + WS_H + (size_t)tt16 * 20 * 1024;

    u32x4 st0, st1;
    unsigned c0, c1, c2, c3, bb;
    {
        const unsigned vs = tk * 64 + g * 16;
        const char* sp = ws + WS_ST + (size_t)tt16 * 16 * 64;
        gld128<0>(st0, vs, sp); gld128<1024>(st1, vs, sp);
        const int n = min(16 * ft0 + (int)(threadIdx.x & 127), FF - 1);
        const char* cp = ws + WS_CS;
        gld32<0>(c0, n * 4, cp); gld32<0>(c1, n * 4, cp + FF * 4); gld32<0>(c2, n * 4, cp + FF * 8); gld32<0>(c3, n * 4, cp + FF * 12);
        gldu16<0>(bb, n * 2, b1);
    }
    u32x4 hf[20][2];
    auto issue = [&](auto S_) {
        constexpr int S = S_.value;
        sfor<0, 6>([&](auto I) { glds<S * 28672 + I.value * 1024>(vl, wsrc + I.value * 20480 + S * 4096, ldsb + w * 7168); });
        glds<S * 28672 + 6144>(vl, w7 + S * 4096, ldsb + w * 7168);
        sfor<0, 4>([&](auto J) {
            gld128<J.value * 1024>(hf[4 * S + J.value][0], vl, hsrc + S * 4096);
            gld128<J.value * 1024>(hf[4 * S + J.value][1], vl, hsrc + 20480 + S * 4096);
        });
    };
    sfor<0, 3>(issue);
    f32x4 acc[2][7] = {};
    sfor<0, 5>([&](auto S_) {
        constexpr int S = S_.value;
        wait_vm_bar<15 * ((S + 2 < 4 ? S + 2 : 4) - S)>();
        sfor<0, 4>([&](auto J) { pin(hf[4 * S + J.value][0]); pin(hf[4 * S + J.value][1]); });
        if constexpr (S == 0) {
            pin(c0); pin(c1); pin(c2); pin(c3); pin(bb); pin(st0); pin(st1);
            if (threadIdx.x < 128) {
                ((LAS float*)(lds + D_TAB))[threadIdx.x] = (__uint_as_float(c0) + __uint_as_float(c1)) + (__uint_as_float(c2) + __uint_as_float(c3));
                ((LAS float*)(lds + D_TAB))[128 + threadIdx.x] = bflo(bb);
            }
        }
        if constexpr (S + 3 < 5) issue(std::integral_constant<int, S + 3>{});
#pragma unroll
        for (int k = 0; k < 4; ++k)
#pragma unroll
            for (int i = 0; i < 7; ++i) {
                const u32x4 wf = lds128(lds + ((4 * S + k) * 7 + i) * 1024 + vl);
                acc[0][i] = mfma16(wf, hf[4 * S + k][0], acc[0][i]);
                acc[1][i] = mfma16(wf, hf[4 * S + k][1], acc[1][i]);
            }
    });
#pragma unroll
    for (int t = 0; t < 2; ++t) {
        const u32x4 st = t == 0 ? st0 : st1;
        const unsigned u0 = st[0], u1 = st[1], u2 = st[2], u3 = st[3];
        const float s1 = swap32_add(swap16_add(__uint_as_float(u0) + __uint_as_float(u2)));
        const float s2 = swap32_add(swap16_add(__uint_as_float(u1) + __uint_as_float(u3)));
        float m, rs;
        ln_stats(s1, s2, m, rs);
        u32x2 o[7];
#pragma unroll
        for (int i = 0; i < 7; ++i) {
            const f32x4 cs = *(LAS f32x4*)(lds + D_TAB + (16 * i + 4 * g) * 4), bv = *(LAS f32x4*)(lds + D_TAB + 512 + (16 * i + 4 * g) * 4);
            float v[4];
#pragma unroll
            for (int e = 0; e < 4; ++e) v[e] = gelu_tanh(fmaf(acc[t][i][e] - m * cs[e], rs, bv[e]));
            o[i] = u32x2{pk2(v[0], v[1]), pk2(v[2], v[3])};
        }
        char* gb = ws + WS_G + (size_t)(tt16 + t) * 54 * 1024 + vl;
#pragma unroll
        for (int p = 0; p < 3; ++p) *(u32x4*)(gb + (3 * c + p) * 1024) = u32x4{o[2 * p][0], o[2 * p][1], o[2 * p + 1][0], o[2 * p + 1][1]};
        if (has7) { const int lam = 3 * (c >> 2) + (c & 3); *(u32x2*)(gb + (48 + (lam >> 1)) * 1024 + (lam & 1) * 8) = o[6]; }
    }
}

constexpr int E_SLOTS = 7, E_LDS = E_SLOTS * 20480;

__global__ __launch_bounds__(256, 1) void k_down(const u16* b2, char* ws, u16* out) {
    extern __shared__ __attribute__((aligned(16))) char smem[];
    const lptr lds = (lptr)smem;
    const unsigned ldsb = (unsigned)(size_t)lds;
    const int b = blockIdx.x, tb = 4 * (b & 7) + ((b >> 3) & 3), cg = b >> 5;
    const int w = __builtin_amdgcn_readfirstlane(threadIdx.x >> 6), lane = threadIdx.x & 63;
    const int tt16 = 4 * tb + w, tk = lane & 15, g = lane >> 4;
    const unsigned vl = lane * 16;
    const char* wsrc = ws + WS_W2 + (size_t)cg * 270 * 1024;
    const char* gsrc = ws + WS_G + (size_t)tt16 * 54 * 1024;

    u32x4 r01, r23;
    u32x2 r4, bv[5];
    {
        const char* hb = ws + WS_H + (size_t)tt16 * 20 * 1024;
        gld128<0>(r01, vl, hb + 2 * cg * 1024); gld128<1024>(r23, vl, hb + 2 * cg * 1024);
        gld64<0>(r4, vl, hb + (16 + (cg >> 1)) * 1024 + (cg & 1) * 8);
        sfor<0, 5>([&](auto I) { gld64<32 * I.value>(bv[I.value], 8 * g, (const char*)b2 + cg * 160); });
    }
    u32x4 gf[54];
    auto issue = [&](auto S_) {
        constexpr int S = S_.value, SL = (S % E_SLOTS) * 20480;
        if constexpr (S < 13) {
            sfor<0, 5>([&](auto J) { glds<SL + J.value * 1024>(vl, wsrc + S * 20480 + w * 5120 + J.value * 1024, ldsb + w * 5120); });
            sfor<0, 4>([&](auto J) { gld128<J.value * 1024>(gf[4 * S + J.value], vl, gsrc + S * 4096); });
        } else {
            const int p0 = w == 3 ? 7 : 3 * w;
            sfor<0, 3>([&](auto J) { glds<SL + J.value * 1024>(vl, wsrc + S * 20480 + p0 * 1024 + J.value * 1024, ldsb + p0 * 1024); });
            sfor<0, 2>([&](auto J) { gld128<J.value * 1024>(gf[4 * S + J.value], vl, gsrc + S * 4096); });
        }
    };
    sfor<0, 6>(issue);
    f32x4 acc[5] = {};
    sfor<0, 14>([&](auto S_) {
        constexpr int S = S_.value, SL = (S % E_SLOTS) * 20480, NK = S < 13 ? 4 : 2;
        constexpr int LAST = S + 5 < 13 ? S + 5 : 13;
        wait_vm_bar<(LAST == S ? 0 : 9 * (LAST - S) - (LAST == 13 ? 4 : 0))>();
        sfor<0, NK>([&](auto J) { pin(gf[4 * S + J.value]); });
        if constexpr (S + 6 < 14) issue(std::integral_constant<int, S + 6>{});
#pragma unroll
        for (int k = 0; k < NK; ++k)
#pragma unroll
            for (int i = 0; i < 5; ++i) acc[i] = mfma16(lds128(lds + SL + (k * 5 + i) * 1024 + vl), gf[4 * S + k], acc[i]);
    });
    pin(r01); pin(r23); pin(r4);
    char* ob = (char*)out + (size_t)(16 * tt16 + tk) * 1280 + cg * 160 + 8 * g;
#pragma unroll
    for (int i = 0; i < 5; ++i) {
        pin(bv[i]);
        const unsigned h0 = i == 0 ? r01[0] : i == 1 ? r01[2] : i == 2 ? r23[0] : i == 3 ? r23[2] : r4[0];
        const unsigned h1 = i == 0 ? r01[1] : i == 1 ? r01[3] : i == 2 ? r23[1] : i == 3 ? r23[3] : r4[1];
        const unsigned y0 = pk2(acc[i][0], acc[i][1]), y1 = pk2(acc[i][2], acc[i][3]), b0 = bv[i][0], b1 = bv[i][1];
        const unsigned z0 = pk2(bflo(y0) + bflo(b0), bfhi(y0) + bfhi(b0)), z1 = pk2(bflo(y1) + bflo(b1), bfhi(y1) + bfhi(b1));
        *(u32x2*)(ob + 32 * i) = u32x2{pk2(bflo(h0) + bflo(z0), bfhi(h0) + bfhi(z0)), pk2(bflo(h1) + bflo(z1), bfhi(h1) + bfhi(z1))};
    }
}

extern "C" void kernel_launch(void* const* d_in, const int* in_sizes, int n_in, void* d_out, int out_size, void* d_ws, size_t ws_size, hipStream_t stream) {
    const u16* x = (const u16*)d_in[0];
    const u16* wqkv = (const u16*)d_in[1];
    const u16* wo = (const u16*)d_in[2];
    const u16* w1 = (const u16*)d_in[4];
    const u16* b1 = (const u16*)d_in[5];
    const u16* w2 = (const u16*)d_in[6];
    const u16* b2 = (const u16*)d_in[7];
    char* ws = (char*)d_ws;
    (void)hipFuncSetAttribute((const void*)k_qkv, hipFuncAttributeMaxDynamicSharedMemorySize, A_LDS);
    (void)hipFuncSetAttribute((const void*)k_attn, hipFuncAttributeMaxDynamicSharedMemorySize, B_LDS);
    (void)hipFuncSetAttribute((const void*)k_wo, hipFuncAttributeMaxDynamicSharedMemorySize, C_LDS);
    (void)hipFuncSetAttribute((const void*)k_up, hipFuncAttributeMaxDynamicSharedMemorySize, D_LDS);
    (void)hipFuncSetAttribute((const void*)k_down, hipFuncAttributeMaxDynamicSharedMemorySize, E_LDS);
    k_qkv<<<256, 256, A_LDS, stream>>>(x, wqkv, ws);
    k_attn<<<256, 512, B_LDS, stream>>>(wo, w1, w2, ws);
    k_wo<<<256, 256, C_LDS, stream>>>(x, ws);
    k_up<<<256, 256, D_LDS, stream>>>(b1, ws);
    k_down<<<256, 256, E_LDS, stream>>>(b2, ws, (u16*)d_out);
}
```

```cpp
#include <hip/hip_runtime.h>
#include <hip/hip_bf16.h>
#include <stdint.h>

typedef unsigned short u16;
typedef short bf16x8 __attribute__((ext_vector_type(8)));
typedef short s16x4 __attribute__((ext_vector_type(4)));
typedef float f32x16 __attribute__((ext_vector_type(16)));
typedef float f32x8 __attribute__((ext_vector_type(8)));
typedef float f32x4 __attribute__((ext_vector_type(4)));
typedef float f32x2 __attribute__((ext_vector_type(2)));
typedef unsigned u32x8 __attribute__((ext_vector_type(8)));
typedef unsigned u32x4 __attribute__((ext_vector_type(4)));
typedef unsigned u32x2 __attribute__((ext_vector_type(2)));
typedef __bf16 bf2 __attribute__((ext_vector_type(2)));
#define LAS __attribute__((address_space(3)))
#define DI __device__ __forceinline__
typedef LAS char* lptr;

constexpr int SEQ = 2048, DM = 640, NH = 32, FF = 1728;
constexpr int QREC = 1280, KVREC = 2624;

constexpr size_t WS_Q = 0;
constexpr size_t WS_KV = WS_Q + (size_t)NH * 64 * QREC;
constexpr size_t WS_O = WS_KV + (size_t)NH * 64 * KVREC + 16384;
constexpr size_t WS_H = WS_O + (size_t)128 * 20 * 1024;
constexpr size_t WS_G = WS_H + (size_t)128 * 20 * 1024;
constexpr size_t WS_WO = WS_G + (size_t)128 * 54 * 1024;
constexpr size_t WS_W1 = WS_WO + (size_t)8 * 20 * 5 * 1024;
constexpr size_t WS_W2 = WS_W1 + (size_t)108 * 20 * 1024;
constexpr size_t WS_ST = WS_W2 + (size_t)8 * 54 * 5 * 1024;
constexpr size_t WS_CS = WS_ST + (size_t)SEQ * 8 * 8;
constexpr size_t WS_END = WS_CS + (size_t)4 * FF * 4;

template <int I, int N, class F> DI void sfor(F&& f) {
    if constexpr (I < N) { f(std::integral_constant<int, I>{}); sfor<I + 1, N>(f); }
}

DI unsigned pk2(float lo, float hi) { f32x2 v = {lo, hi}; bf2 b = __builtin_convertvector(v, bf2); return __builtin_bit_cast(unsigned, b); }
DI float bflo(unsigned u) { return __uint_as_float(u << 16); }
DI float bfhi(unsigned u) { return __uint_as_float(u & 0xffff0000u); }
DI float rbf(float x) { return bflo(pk2(x, x)); }
DI float dot2(unsigned a, unsigned b, float c) { return __builtin_amdgcn_fdot2_f32_bf16(__builtin_bit_cast(bf2, a), __builtin_bit_cast(bf2, b), c, false); }
DI float swap32_add(float v) { auto r = __builtin_amdgcn_permlane32_swap(__float_as_uint(v), __float_as_uint(v), false, false); return __uint_as_float(r[0]) + __uint_as_float(r[1]); }
DI float swap32_max(float v) { auto r = __builtin_amdgcn_permlane32_swap(__float_as_uint(v), __float_as_uint(v), false, false); return fmaxf(__uint_as_float(r[0]), __uint_as_float(r[1])); }
DI float swap16_add(float v) { auto r = __builtin_amdgcn_permlane16_swap(__float_as_uint(v), __float_as_uint(v), false, false); return __uint_as_float(r[0]) + __uint_as_float(r[1]); }
DI f32x16 mfma32(u32x4 a, u32x4 b, f32x16 c) { return __builtin_amdgcn_mfma_f32_32x32x16_bf16(__builtin_bit_cast(bf16x8, a), __builtin_bit_cast(bf16x8, b), c, 0, 0, 0); }
DI f32x16 mfma32k8(u32x2 a, u32x2 b, f32x16 c) { return __builtin_amdgcn_mfma_f32_32x32x8bf16_1k(__builtin_bit_cast(s16x4, a), __builtin_bit_cast(s16x4, b), c, 0, 0, 0); }
DI f32x4 mfma16(u32x4 a, u32x4 b, f32x4 c) { return __builtin_amdgcn_mfma_f32_16x16x32_bf16(__builtin_bit_cast(bf16x8, a), __builtin_bit_cast(bf16x8, b), c, 0, 0, 0); }
DI u32x4 lds128(lptr p) { return *(LAS u32x4*)p; }
DI u32x2 ldstr(lptr p) { return __builtin_bit_cast(u32x2, __builtin_amdgcn_ds_read_tr16_b64_v4i16((LAS s16x4*)p)); }

template <int OFF> DI void glds(unsigned voff, const void* sbase, unsigned ldsb) {
    asm volatile("s_add_u32 m0, %2, %3\n\ts_nop 0\n\tglobal_load_lds_dwordx4 %0, %1" ::"v"(voff), "s"(sbase), "s"(ldsb), "i"(OFF) : "memory", "scc");
}
template <int IMM> DI void gld128(u32x4& d, unsigned voff, const void* sbase) {
    asm volatile("global_load_dwordx4 %0, %1, %2 offset:%3" : "=v"(d) : "v"(voff), "s"(sbase), "i"(IMM) : "memory");
}
template <int IMM> DI void gld64(u32x2& d, unsigned voff, const void* sbase) {
    asm volatile("global_load_dwordx2 %0, %1, %2 offset:%3" : "=v"(d) : "v"(voff), "s"(sbase), "i"(IMM) : "memory");
}
template <int IMM> DI void gld32(unsigned& d, unsigned voff, const void* sbase) {
    asm volatile("global_load_dword %0, %1, %2 offset:%3" : "=v"(d) : "v"(voff), "s"(sbase), "i"(IMM) : "memory");
}
template <int IMM> DI void gldu16(unsigned& d, unsigned voff, const void* sbase) {
    asm volatile("global_load_ushort %0, %1, %2 offset:%3" : "=v"(d) : "v"(voff), "s"(sbase), "i"(IMM) : "memory");
}
template <int N> DI void wait_vm() { asm volatile("s_waitcnt vmcnt(%0)" ::"i"(N) : "memory"); }
template <int N> DI void wait_vm_bar() { asm volatile("s_waitcnt vmcnt(%0) lgkmcnt(0)\n\ts_barrier" ::"i"(N) : "memory"); }
DI void lds_bar() { asm volatile("s_waitcnt lgkmcnt(0)\n\ts_barrier" ::: "memory"); }
template <class T> DI void pin(T& v) { asm volatile("" : "+v"(v)); }

#define STWAIT
#define STDECL
#define ST(i)
#define STFLUSH(k)

DI void ln_stats(float s1, float s2, float& m, float& rs) {
    const float mean = s1 * (1.0f / DM);
    const float var = fmaxf(s2 * (1.0f / DM) - mean * mean, 0.0f);
    m = rbf(mean);
    rs = 1.0f / rbf(sqrtf(rbf(rbf(var) + 1e-5f)));
}

constexpr int A_STAGE = 32768, A_LDS = 5 * A_STAGE;

template <bool ISV>
DI void qkv_body(const u16* x, const u16* wqkv, char* ws, lptr lds, int tb, int cg, int w, int lane) {
    STDECL; ST(0);
    const int th = w >> 1, fh = w & 1, hi = lane >> 5, l31 = lane & 31;
    const unsigned ldsb = (unsigned)(size_t)lds;
    unsigned vx[4];
#pragma unroll
    for (int j = 0; j < 4; ++j) {
        const int row = 32 * w + 8 * j + (lane >> 3);
        vx[j] = row * 1280 + (((lane & 7) ^ ((row >> 1) & 7)) << 4);
    }
    const unsigned vw = (lane >> 4) * 3840 + (((lane & 15) ^ ((lane >> 4) << 2)) << 4);
    const char* xs = (const char*)x + (size_t)tb * 128 * 1280;
    const char* wsrc = (const char*)wqkv + (size_t)(16 * w) * 3840 + cg * 256;
    const unsigned ldw = ldsb + w * 4096;
    auto issue = [&](auto S_) {
        constexpr int S = S_.value, SL = (S % 5) * A_STAGE;
        sfor<0, 4>([&](auto J) { glds<SL + J.value * 1024>(vx[J.value], xs + S * 128, ldw); });
        sfor<0, 4>([&](auto J) { glds<SL + 16384 + J.value * 1024>(vw, wsrc + (size_t)(S * 64 + J.value * 4) * 3840, ldw); });
    };
    sfor<0, 5>(issue);

    int xo[2][4], wo[2];
#pragma unroll
    for (int ks = 0; ks < 4; ++ks) {
        const int c = (((2 * ks + hi) ^ ((l31 >> 1) & 7)) << 4);
        xo[0][ks] = (64 * th + 32 * fh + l31) * 128 + c;
        xo[1][ks] = (64 * th + 32 * (1 - fh) + l31) * 128 + c;
    }
    {
        const int q = (lane >> 2) & 3, p = lane & 3, cb = (lane >> 4) & 1;
#pragma unroll
        for (int f = 0; f < 2; ++f) {
            const int ft = 2 * fh + (f == 0 ? th : 1 - th);
            wo[f] = 16384 + (8 * hi + q) * 256 + (((4 * (ft ^ q)) + 2 * cb + (p >> 1)) << 4) + 8 * (p & 1);
        }
    }
    f32x16 acc[2][2] = {};
    float s1 = 0.f, s2 = 0.f, cs = 0.f;
    u32x4 xf[2][2], wf[2][2];
    auto rd = [&](auto S_, auto K_) {
        constexpr int SL = (S_.value % 5) * A_STAGE, KS = K_.value, B = KS & 1;
        xf[B][0] = lds128(lds + SL + xo[0][KS]);
        xf[B][1] = lds128(lds + SL + xo[1][KS]);
#pragma unroll
        for (int f = 0; f < 2; ++f) {
            const u32x2 a = ldstr(lds + SL + KS * 4096 + wo[f]), b = ldstr(lds + SL + KS * 4096 + 1024 + wo[f]);
            wf[B][f] = u32x4{a[0], a[1], b[0], b[1]};
        }
    };
    auto mm = [&](auto K_) {
        constexpr int B = K_.value & 1;
#pragma unroll
        for (int t = 0; t < 2; ++t)
#pragma unroll
            for (int f = 0; f < 2; ++f) acc[t][f] = ISV ? mfma32(xf[B][t], wf[B][f], acc[t][f]) : mfma32(wf[B][f], xf[B][t], acc[t][f]);
#pragma unroll
        for (int e = 0; e < 4; ++e) {
            const unsigned xv = xf[B][0][e], wv = wf[B][0][e];
            s1 = dot2(xv, 0x3f803f80u, s1);
            s2 = dot2(xv, xv, s2);
            cs = dot2(wv, 0x3f803f80u, cs);
        }
    };
    using I0 = std::integral_constant<int, 0>; using I1 = std::integral_constant<int, 1>;
    using I2 = std::integral_constant<int, 2>; using I3 = std::integral_constant<int, 3>;
    ST(1);
    wait_vm_bar<32>();
    ST(2);
    rd(I0{}, I0{});
    sfor<0, 10>([&](auto S_) {
        constexpr int S = S_.value;
        rd(S_, I1{}); mm(I0{});
        rd(S_, I2{}); mm(I1{});
        rd(S_, I3{}); mm(I2{});
        if constexpr (S < 9) {
            wait_vm_bar<8 * ((S + 4 < 9 ? S + 4 : 9) - (S + 1))>();
            if constexpr (S + 5 < 10) issue(std::integral_constant<int, S + 5>{});
            rd(std::integral_constant<int, S + 1>{}, I0{});
        }
        mm(I3{});
    });
    lds_bar();
    ST(3);
    LAS float* T1 = (LAS float*)lds;
    LAS float* T2 = T1 + 128;
    LAS float* TC = T1 + 256;
    LAS f32x2* TS = (LAS f32x2*)(T1 + 384) + w * 64;
    s1 = swap32_add(s1); s2 = swap32_add(s2); cs = swap32_add(cs);
    if (hi == 0) { T1[64 * th + 32 * fh + l31] = s1; T2[64 * th + 32 * fh + l31] = s2; TC[64 * fh + 32 * th + l31] = cs; }
    lds_bar();
    {
        float m, rs;
        ln_stats(T1[64 * th + lane], T2[64 * th + lane], m, rs);
        TS[lane] = f32x2{m, rs};
    }
    asm volatile("s_waitcnt lgkmcnt(0)" ::: "memory");
    if constexpr (!ISV) {
        const bool isq = cg < 5;
        const float sc = isq ? 0.22360679774997896f * 1.4426950408889634f : 1.0f;
        char* dst = ws + (isq ? WS_Q : WS_KV);
        const int rec = isq ? QREC : KVREC, cgm = isq ? cg : cg - 5;
#pragma unroll
        for (int t = 0; t < 2; ++t) {
            const int ta = t == 0 ? fh : 1 - fh;
            const f32x2 st = TS[32 * ta + l31];
            const float m = st[0], rs = st[1] * sc;
            const int tt = 4 * tb + 2 * th + ta;
#pragma unroll
            for (int f = 0; f < 2; ++f) {
                const int fa = f == 0 ? th : 1 - th;
#pragma unroll
                for (int g = 0; g < 4; ++g) {
                    const int n = 64 * fh + 32 * fa + 8 * g + 4 * hi;
                    const f32x4 c4 = *(LAS f32x4*)(TC + n);
                    const int gq = 32 * cgm + (n >> 2), head = (gq * 205) >> 10, dq = gq - 5 * head;
                    const int off = (head * 64 + tt) * rec + (dq < 4 ? ((dq >> 1) * 32 + l31) * 16 + (dq & 1) * 8 : 1024 + l31 * 8);
                    u32x2 o;
                    o[0] = pk2((acc[t][f][4 * g] - m * c4[0]) * rs, (acc[t][f][4 * g + 1] - m * c4[1]) * rs);
                    o[1] = pk2((acc[t][f][4 * g + 2] - m * c4[2]) * rs, (acc[t][f][4 * g + 3] - m * c4[3]) * rs);
                    *(u32x2*)(dst + off) = o;
                }
            }
        }
    } else {
        char* dst = ws + WS_KV;
#pragma unroll
        for (int f = 0; f < 2; ++f) {
            const int fa = f == 0 ? th : 1 - th;
            const int n = 64 * fh + 32 * fa + l31;
            const float c = TC[n];
            const int col = 128 * (cg - 10) + n, head = (col * 3277) >> 16, d = col - 20 * head;
#pragma unroll
            for (int t = 0; t < 2; ++t) {
                const int ta = t == 0 ? fh : 1 - fh;
                const int tt = 4 * tb + 2 * th + ta;
#pragma unroll
                for (int s = 0; s < 2; ++s) {
                    u32x4 o;
#pragma unroll
                    for (int g = 0; g < 2; ++g) {
                        const int tok = 32 * ta + 16 * s + 8 * g + 4 * hi, r = 8 * s + 4 * g;
                        const f32x4 a = *(LAS f32x4*)(TS + tok), b = *(LAS f32x4*)(TS + tok + 2);
                        o[2 * g] = pk2((acc[t][f][r] - a[0] * c) * a[1], (acc[t][f][r + 1] - a[2] * c) * a[3]);
                        o[2 * g + 1] = pk2((acc[t][f][r + 2] - b[0] * c) * b[1], (acc[t][f][r + 3] - b[2] * c) * b[3]);
                    }
                    *(u32x4*)(dst + (head * 64 + tt) * KVREC + 1280 + (2 * s + hi) * 336 + d * 16) = o;
                }
            }
        }
    }
    ST(4); STWAIT; ST(5); STFLUSH(0);
}

__global__ __launch_bounds__(256, 1) void k_qkv(const u16* x, const u16* wqkv, char* ws) {
    extern __shared__ __attribute__((aligned(16))) char smem[];
    const int b = blockIdx.x, xcd = b & 7, i = b >> 3;
    const int tb = 4 * (xcd >> 1) + (i & 3), cg = (xcd & 1) * 8 + (i >> 2);
    const int w = __builtin_amdgcn_readfirstlane(threadIdx.x >> 6), lane = threadIdx.x & 63;
    if (cg == 15) {
#pragma unroll
        for (int j = 0; j < 2; ++j) {
            const int e = tb * 512 + j * 256 + threadIdx.x;
            *(u32x4*)(ws + WS_KV + (size_t)(e >> 2) * KVREC + 1280 + (e & 3) * 336 + 320) = u32x4{0x3f803f80u, 0x3f803f80u, 0x3f803f80u, 0x3f803f80u};
        }
        return;
    }
    if (cg >= 10) qkv_body<true>(x, wqkv, ws, (lptr)smem, tb, cg, w, lane);
    else qkv_body<false>(x, wqkv, ws, (lptr)smem, tb, cg, w, lane);
}

#define ATTN_BODY_ASM \
    "v_mfma_f32_32x32x16_bf16 v[80:95], v[188:191], v[180:183], v[144:159]\n\t" \
    "v_exp_f32 v64, v64\n\t" \
    "v_exp_f32 v65, v65\n\t" \
    "v_exp_f32 v66, v66\n\t" \
    "v_cvt_pk_bf16_f32 v160, v64, v65\n\t" \
    "v_exp_f32 v67, v67\n\t" \
    "v_mfma_f32_32x32x8_bf16 v[80:95], v[196:197], v[186:187], v[80:95]\n\t" \
    "global_load_dwordx4 v[188:191], %[vk16], %[sb] offset:0\n\t" \
    "global_load_dwordx2 v[196:197], %[vk8], %[sb] offset:0\n\t" \
    "v_cvt_pk_bf16_f32 v161, v66, v67\n\t" \
    "v_exp_f32 v68, v68\n\t" \
    "v_exp_f32 v69, v69\n\t" \
    "v_exp_f32 v70, v70\n\t" \
    "v_cvt_pk_bf16_f32 v162, v68, v69\n\t" \
    "v_exp_f32 v71, v71\n\t" \
    "v_mfma_f32_32x32x16_bf16 v[112:127], v[208:211], v[168:171], v[112:127]\n\t" \
    "v_cvt_pk_bf16_f32 v163, v70, v71\n\t" \
    "v_exp_f32 v72, v72\n\t" \
    "v_exp_f32 v73, v73\n\t" \
    "v_exp_f32 v74, v74\n\t" \
    "v_cvt_pk_bf16_f32 v164, v72, v73\n\t" \
    "v_exp_f32 v75, v75\n\t" \
    "v_mfma_f32_32x32x16_bf16 v[112:127], v[212:215], v[172:175], v[112:127]\n\t" \
    "global_load_dwordx4 v[208:211], %[vv], %[sb] offset:-2624\n\t" \
    "global_load_dwordx4 v[212:215], %[vv], %[sb] offset:-1952\n\t" \
    "v_cvt_pk_bf16_f32 v165, v74, v75\n\t" \
    "v_exp_f32 v76, v76\n\t" \
    "v_exp_f32 v77, v77\n\t" \
    "v_exp_f32 v78, v78\n\t" \
    "v_cvt_pk_bf16_f32 v166, v76, v77\n\t" \
    "v_exp_f32 v79, v79\n\t" \
    "s_waitcnt vmcnt(4)\n\t" \
    "v_cvt_pk_bf16_f32 v167, v78, v79\n\t" \
    "v_mfma_f32_32x32x16_bf16 v[64:79], v[192:195], v[176:179], v[128:143]\n\t" \
    "v_exp_f32 v80, v80\n\t" \
    "v_exp_f32 v81, v81\n\t" \
    "v_exp_f32 v82, v82\n\t" \
    "v_cvt_pk_bf16_f32 v168, v80, v81\n\t" \
    "v_exp_f32 v83, v83\n\t" \
    "v_mfma_f32_32x32x8_bf16 v[64:79], v[198:199], v[184:185], v[64:79]\n\t" \
    "v_cvt_pk_bf16_f32 v169, v82, v83\n\t" \
    "v_exp_f32 v84, v84\n\t" \
    "v_exp_f32 v85, v85\n\t" \
    "v_exp_f32 v86, v86\n\t" \
    "v_cvt_pk_bf16_f32 v170, v84, v85\n\t" \
    "v_exp_f32 v87, v87\n\t" \
    "v_mfma_f32_32x32x16_bf16 v[96:111], v[200:203], v[160:163], v[96:111]\n\t" \
    "v_cvt_pk_bf16_f32 v171, v86, v87\n\t" \
    "v_exp_f32 v88, v88\n\t" \
    "v_exp_f32 v89, v89\n\t" \
    "v_exp_f32 v90, v90\n\t" \
    "v_cvt_pk_bf16_f32 v172, v88, v89\n\t" \
    "v_exp_f32 v91, v91\n\t" \
    "v_mfma_f32_32x32x16_bf16 v[96:111], v[204:207], v[164:167], v[96:111]\n\t" \
    "v_cvt_pk_bf16_f32 v173, v90, v91\n\t" \
    "v_exp_f32 v92, v92\n\t" \
    "v_exp_f32 v93, v93\n\t" \
    "v_exp_f32 v94, v94\n\t" \
    "v_cvt_pk_bf16_f32 v174, v92, v93\n\t" \
    "v_exp_f32 v95, v95\n\t" \
    "s_nop 0\n\t" \
    "v_cvt_pk_bf16_f32 v175, v94, v95\n\t" \
    "v_mfma_f32_32x32x16_bf16 v[80:95], v[192:195], v[180:183], v[144:159]\n\t" \
    "v_exp_f32 v64, v64\n\t" \
    "v_exp_f32 v65, v65\n\t" \
    "v_exp_f32 v66, v66\n\t" \
    "v_cvt_pk_bf16_f32 v160, v64, v65\n\t" \
    "v_exp_f32 v67, v67\n\t" \
    "v_mfma_f32_32x32x8_bf16 v[80:95], v[198:199], v[186:187], v[80:95]\n\t" \
    "global_load_dwordx4 v[192:195], %[vk16], %[sb] offset:2624\n\t" \
    "global_load_dwordx2 v[198:199], %[vk8], %[sb] offset:2624\n\t" \
    "v_cvt_pk_bf16_f32 v161, v66, v67\n\t" \
    "v_exp_f32 v68, v68\n\t" \
    "v_exp_f32 v69, v69\n\t" \
    "v_exp_f32 v70, v70\n\t" \
    "v_cvt_pk_bf16_f32 v162, v68, v69\n\t" \
    "v_exp_f32 v71, v71\n\t" \
    "v_mfma_f32_32x32x16_bf16 v[112:127], v[200:203], v[168:171], v[112:127]\n\t" \
    "v_cvt_pk_bf16_f32 v163, v70, v71\n\t" \
    "v_exp_f32 v72, v72\n\t" \
    "v_exp_f32 v73, v73\n\t" \
    "v_exp_f32 v74, v74\n\t" \
    "v_cvt_pk_bf16_f32 v164, v72, v73\n\t" \
    "v_exp_f32 v75, v75\n\t" \
    "v_mfma_f32_32x32x16_bf16 v[112:127], v[204:207], v[172:175], v[112:127]\n\t" \
    "global_load_dwordx4 v[200:203], %[vv], %[sb] offset:0\n\t" \
    "global_load_dwordx4 v[204:207], %[vv], %[sb] offset:672\n\t" \
    "v_cvt_pk_bf16_f32 v165, v74, v75\n\t" \
    "v_exp_f32 v76, v76\n\t" \
    "v_exp_f32 v77, v77\n\t" \
    "v_exp_f32 v78, v78\n\t" \
    "v_cvt_pk_bf16_f32 v166, v76, v77\n\t" \
    "v_exp_f32 v79, v79\n\t" \
    "s_waitcnt vmcnt(4)\n\t" \
    "v_cvt_pk_bf16_f32 v167, v78, v79\n\t" \
    "v_mfma_f32_32x32x16_bf16 v[64:79], v[188:191], v[176:179], v[128:143]\n\t" \
    "v_exp_f32 v80, v80\n\t" \
    "v_exp_f32 v81, v81\n\t" \
    "v_exp_f32 v82, v82\n\t" \
    "v_cvt_pk_bf16_f32 v168, v80, v81\n\t" \
    "v_exp_f32 v83, v83\n\t" \
    "v_mfma_f32_32x32x8_bf16 v[64:79], v[196:197], v[184:185], v[64:79]\n\t" \
    "v_cvt_pk_bf16_f32 v169, v82, v83\n\t" \
    "v_exp_f32 v84, v84\n\t" \
    "v_exp_f32 v85, v85\n\t" \
    "v_exp_f32 v86, v86\n\t" \
    "v_cvt_pk_bf16_f32 v170, v84, v85\n\t" \
    "v_exp_f32 v87, v87\n\t" \
    "v_mfma_f32_32x32x16_bf16 v[96:111], v[208:211], v[160:163], v[96:111]\n\t" \
    "v_cvt_pk_bf16_f32 v171, v86, v87\n\t" \
    "v_exp_f32 v88, v88\n\t" \
    "v_exp_f32 v89, v89\n\t" \
    "v_exp_f32 v90, v90\n\t" \
    "v_cvt_pk_bf16_f32 v172, v88, v89\n\t" \
    "v_exp_f32 v91, v91\n\t" \
    "v_mfma_f32_32x32x16_bf16 v[96:111], v[212:215], v[164:167], v[96:111]\n\t" \
    "v_cvt_pk_bf16_f32 v173, v90, v91\n\t" \
    "v_exp_f32 v92, v92\n\t" \
    "v_exp_f32 v93, v93\n\t" \
    "v_exp_f32 v94, v94\n\t" \
    "v_cvt_pk_bf16_f32 v174, v92, v93\n\t" \
    "v_exp_f32 v95, v95\n\t" \
    "s_nop 0\n\t" \
    "v_cvt_pk_bf16_f32 v175, v94, v95\n\t"

constexpr int B_X = 0, B_STG = 32768, B_CS = B_STG + 5 * 4096, B_LDS = B_CS + 5 * 256;

DI int up_start(int c) { return 27 * (c >> 2) + 7 * (c & 3); }

DI void attn_slow(const char* kv, unsigned vk16, unsigned vk8, unsigned vv, u32x4 q16, u32x2 q8, f32x16& o, float& m) {
    o = f32x16{};
    m = -3.0e38f;
    for (int t = 0; t < 32; ++t) {
        const char* r = kv + t * KVREC;
        const u32x4 k16 = *(const u32x4*)(r + vk16), v0 = *(const u32x4*)(r + vv), v1 = *(const u32x4*)(r + vv + 672);
        const u32x2 k8 = *(const u32x2*)(r + vk8);
        f32x16 s = mfma32k8(k8, q8, mfma32(k16, q16, f32x16{}));
        float mx = s[0];
#pragma unroll
        for (int i = 1; i < 16; ++i) mx = fmaxf(mx, s[i]);
        const float mn = fmaxf(m, swap32_max(mx));
        const float al = __builtin_amdgcn_exp2f(m - mn);
        m = mn;
        u32x4 p0, p1;
#pragma unroll
        for (int i = 0; i < 4; ++i) {
            p0[i] = pk2(__builtin_amdgcn_exp2f(s[2 * i] - mn), __builtin_amdgcn_exp2f(s[2 * i + 1] - mn));
            p1[i] = pk2(__builtin_amdgcn_exp2f(s[8 + 2 * i] - mn), __builtin_amdgcn_exp2f(s[9 + 2 * i] - mn));
        }
#pragma unroll
        for (int i = 0; i < 16; ++i) o[i] *= al;
        o = mfma32(v1, p1, mfma32(v0, p0, o));
    }
}

__global__ __launch_bounds__(512, 2) void k_attn(const u16* wo, const u16* w1, const u16* w2, char* ws) {
    extern __shared__ __attribute__((aligned(16))) char smem[];
    const lptr lds = (lptr)smem;
    const unsigned ldsb = (unsigned)(size_t)lds;
    const int b = blockIdx.x, head = 4 * (b & 7) + ((b >> 3) & 3), qt = b >> 5;
    const int w = __builtin_amdgcn_readfirstlane(threadIdx.x >> 6), lane = threadIdx.x & 63;
    const int qg = w & 3, kh = w >> 2, hi = lane >> 5, l31 = lane & 31;
    STDECL; ST(0);

    const int tta = 8 * qt + 2 * qg + kh, ttb = 8 * qt + 2 * qg + 1 - kh;
    const char* qa = ws + WS_Q + (size_t)(head * 64 + tta) * QREC;
    const char* qb = ws + WS_Q + (size_t)(head * 64 + ttb) * QREC;
    const char* kv = ws + WS_KV + (size_t)(head * 64 + 32 * kh) * KVREC;
    const unsigned vk16 = lane * 16, vk8 = 1024 + l31 * 8, vv = 1280 + hi * 336 + (l31 < 20 ? l31 : 20) * 16;

    u32x4 QA16, QB16, K16a, K16b, V0a, V1a, V0b, V1b;
    u32x2 QA8, QB8, K8a, K8b;
    gld128<0>(QA16, vk16, qa); gld64<0>(QA8, vk8, qa);
    gld128<0>(QB16, vk16, qb); gld64<0>(QB8, vk8, qb);
    gld128<0>(K16a, vk16, kv); gld64<0>(K8a, vk8, kv);
    gld128<KVREC>(K16b, vk16, kv); gld64<KVREC>(K8b, vk8, kv);
    gld128<0>(V0a, vv, kv); gld128<672>(V1a, vv, kv);

    int kap, col64, kind;
    if (b < 108) { kind = 1; col64 = b % 27; kap = 5 * (b / 27) + w; }
    else if (b < 148) { kind = 0; col64 = (b - 108) % 10; kap = 5 * ((b - 108) / 10) + w; }
    else { kind = 2; const int T = 5 * (b - 148) + w; col64 = T % 10; kap = T / 10; }

    asm volatile("s_waitcnt vmcnt(4)" : "+v"(QA16), "+v"(QA8), "+v"(QB16), "+v"(QB8), "+v"(K16a), "+v"(K8a)::"memory");
    ST(1);
    if (hi) { QA8 = u32x2{0u, 0u}; QB8 = u32x2{0u, 0u}; }
    f32x16 SA = mfma32k8(K8a, QA8, mfma32(K16a, QA16, f32x16{}));
    f32x16 SB = mfma32k8(K8a, QB8, mfma32(K16a, QB16, f32x16{}));
    float ma = SA[0], mb = SB[0];
#pragma unroll
    for (int i = 1; i < 16; ++i) { ma = fmaxf(ma, SA[i]); mb = fmaxf(mb, SB[i]); }
    ma = swap32_max(ma); mb = swap32_max(mb);
    f32x16 CA, CB, OA = {}, OB = {};
#pragma unroll
    for (int i = 0; i < 16; ++i) { SA[i] -= ma; CA[i] = -ma; CB[i] = -mb; }
    u32x8 PA = {}, PB = {};
    V0b = u32x4{0u, 0u, 0u, 0u}; V1b = V0b;
    const char* sb = kv + 2 * KVREC;
    asm volatile("s_nop 7" : "+v"(SA), "+v"(SB));
    ST(2);
    for (int it = 0; it < 16; ++it) {
        asm volatile(ATTN_BODY_ASM
                     : "+{v[64:79]}"(SA), "+{v[80:95]}"(SB), "+{v[96:111]}"(OA), "+{v[112:127]}"(OB), "+{v[160:167]}"(PA), "+{v[168:175]}"(PB),
                       "+{v[188:191]}"(K16a), "+{v[192:195]}"(K16b), "+{v[196:197]}"(K8a), "+{v[198:199]}"(K8b),
                       "+{v[200:203]}"(V0a), "+{v[204:207]}"(V1a), "+{v[208:211]}"(V0b), "+{v[212:215]}"(V1b)
                     : "{v[128:143]}"(CA), "{v[144:159]}"(CB), "{v[176:179]}"(QA16), "{v[180:183]}"(QB16), "{v[184:185]}"(QA8), "{v[186:187]}"(QB8),
                       [vk16] "v"(vk16), [vk8] "v"(vk8), [vv] "v"(vv), [sb] "s"(sb)
                     : "memory");
        sb += 2 * KVREC;
        if (it == 2 && w < 5) {
            const char* src = kind == 0 ? (const char*)wo : kind == 1 ? (const char*)w1 : (const char*)w2;
            const int pitch = kind == 1 ? FF * 2 : DM * 2;
            sfor<0, 4>([&](auto J) {
                const int rho = 8 * J.value + (lane >> 3), t = J.value >> 1, g = (rho >> 2) & 3, q = rho & 3;
                int k;
                if (kind == 0) {
                    const int G = kap / 5, r = kap - 5 * G, u = 4 * r + g;
                    k = r < 4 ? 20 * (8 * G + (u >> 1)) + 8 * t + 4 * (u & 1) + q : 20 * (8 * G + 2 * g + t) + 16 + q;
                } else if (kind == 1) {
                    const int ft = kap < 16 ? 5 * (kap >> 1) + 2 * (kap & 1) + t : 5 * (2 * (kap - 16) + t) + 4;
                    k = 16 * ft + 4 * g + q;
                } else {
                    const int lam = 2 * (kap - 48) + t;
                    const int ft = kap < 48 ? up_start(kap / 3) + 2 * (kap % 3) + t : up_start(4 * (lam / 3) + lam % 3) + 6;
                    k = 16 * ft + 4 * g + q;
                }
                glds<B_STG + J.value * 1024>(k * pitch + (((lane & 7) ^ (2 * ((rho >> 1) & 3))) << 4), src + col64 * 128, ldsb + w * 4096);
            });
        }
        if (it == 9 && w < 5) {
            const int g = lane >> 4, q = (lane >> 2) & 3, p = lane & 3;
            const lptr st = lds + B_STG + w * 4096 + (4 * g + q) * 128 + 8 * (p & 1);
            const int sw = 2 * (((4 * g + q) >> 1) & 3);
#pragma unroll
            for (int j = 0; j < 4; ++j) {
                const int pos = ((2 * j + (p >> 1)) ^ sw) << 4;
                const u32x2 a = ldstr(st + pos), c = ldstr(st + 2048 + pos);
                const int ft = 4 * col64 + j;
                const size_t piece = kind == 0 ? WS_WO + (size_t)(((ft / 5) * 20 + kap) * 5 + ft % 5) * 1024
                                   : kind == 1 ? WS_W1 + (size_t)(ft * 20 + kap) * 1024
                                               : WS_W2 + (size_t)(((ft / 5) * 54 + kap) * 5 + ft % 5) * 1024;
                *(u32x4*)(ws + piece + lane * 16) = u32x4{a[0], a[1], c[0], c[1]};
                if (kind == 1) {
                    const unsigned a0 = a[0], a1 = a[1], c0 = c[0], c1 = c[1];
                    float s = dot2(a0, 0x3f803f80u, dot2(a1, 0x3f803f80u, dot2(c0, 0x3f803f80u, dot2(c1, 0x3f803f80u, 0.f))));
                    s = swap32_add(swap16_add(s));
                    if (lane < 16) *(LAS float*)(lds + B_CS + w * 256 + (16 * j + lane) * 4) = s;
                }
            }
        }
    }
    asm volatile("s_waitcnt vmcnt(0)\n\t"
                 "v_mfma_f32_32x32x16_bf16 v[112:127], v[208:211], v[168:171], v[112:127]\n\t"
                 "v_mfma_f32_32x32x16_bf16 v[112:127], v[212:215], v[172:175], v[112:127]\n\t"
                 "s_nop 15"
                 : "+{v[64:79]}"(SA), "+{v[96:111]}"(OA), "+{v[112:127]}"(OB), "+{v[168:175]}"(PB), "+{v[188:191]}"(K16a), "+{v[192:195]}"(K16b), "+{v[196:197]}"(K8a),
                   "+{v[198:199]}"(K8b), "+{v[200:203]}"(V0a), "+{v[204:207]}"(V1a), "+{v[208:211]}"(V0b), "+{v[212:215]}"(V1b)::"memory");

    ST(3);
    {
        float big = fmaxf(fabsf(OA[12]), fabsf(OB[12]));
        bool ok = big <= 1.8e19f;
#pragma unroll
        for (int i = 0; i < 12; ++i) ok = ok && fabsf(OA[i]) <= 3.0e38f && fabsf(OB[i]) <= 3.0e38f;
        if (__builtin_expect(__any(!ok), 0)) {
            attn_slow(kv, vk16, vk8, vv, QA16, QA8, OA, ma);
            attn_slow(kv, vk16, vk8, vv, QB16, QB8, OB, mb);
        }
    }
    {
        const lptr xw = lds + B_X + w * 4096 + lane * 16;
        *(LAS f32x4*)(xw) = f32x4{OB[0], OB[1], OB[2], OB[3]};
        *(LAS f32x4*)(xw + 1024) = f32x4{OB[4], OB[5], OB[6], OB[7]};
        *(LAS f32x4*)(xw + 2048) = f32x4{OB[8], OB[9], OB[10], OB[11]};
        *(LAS f32x4*)(xw + 3072) = f32x4{OB[12], mb, 0.f, 0.f};
    }
    lds_bar();
    ST(4);
    {
        const lptr xr = lds + B_X + (w ^ 4) * 4096 + lane * 16;
        const f32x4 e0 = *(LAS f32x4*)(xr), e1 = *(LAS f32x4*)(xr + 1024), e2 = *(LAS f32x4*)(xr + 2048), e3 = *(LAS f32x4*)(xr + 3072);
        const float M = fmaxf(ma, e3[1]);
        float fa = __builtin_amdgcn_exp2f(ma - M), fb = __builtin_amdgcn_exp2f(e3[1] - M);
        const float inv = 1.0f / (OA[12] * fa + e3[0] * fb);
        fa *= inv; fb *= inv;
        u32x4 o16; u32x2 o8;
        o16[0] = pk2(OA[0] * fa + e0[0] * fb, OA[1] * fa + e0[1] * fb); o16[1] = pk2(OA[2] * fa + e0[2] * fb, OA[3] * fa + e0[3] * fb);
        o16[2] = pk2(OA[4] * fa + e1[0] * fb, OA[5] * fa + e1[1] * fb); o16[3] = pk2(OA[6] * fa + e1[2] * fb, OA[7] * fa + e1[3] * fb);
        o8[0] = pk2(OA[8] * fa + e2[0] * fb, OA[9] * fa + e2[1] * fb); o8[1] = pk2(OA[10] * fa + e2[2] * fb, OA[11] * fa + e2[3] * fb);
        const int tt16 = 2 * tta + (l31 >> 4), tk = l31 & 15, G = head >> 3, j = head & 7, u = 2 * j + hi;
        char* ob = ws + WS_O + (size_t)(tt16 * 20 + 5 * G) * 1024;
        *(u32x4*)(ob + (u >> 2) * 1024 + ((u & 3) * 16 + tk) * 16) = o16;
        if (hi == 0) *(u32x2*)(ob + 4096 + ((j >> 1) * 16 + tk) * 16 + (j & 1) * 8) = o8;
    }
    if (b < 108 && w == 0) {
        LAS float* c = (LAS float*)(lds + B_CS) + lane;
        ((float*)(ws + WS_CS))[(b / 27) * FF + 64 * col64 + lane] = (c[0] + c[64]) + (c[128] + c[192]) + c[256];
    }
    ST(5); STWAIT; ST(6); STFLUSH(1);
}

constexpr int C_LDS = 100 * 1024;

__global__ __launch_bounds__(256, 1) void k_wo(const u16* x, char* ws) {
    extern __shared__ __attribute__((aligned(16))) char smem[];
    const lptr lds = (lptr)smem;
    const unsigned ldsb = (unsigned)(size_t)lds;
    const int b = blockIdx.x, tb = 4 * (b & 7) + ((b >> 3) & 3), cg = b >> 5;
    const int w = __builtin_amdgcn_readfirstlane(threadIdx.x >> 6), lane = threadIdx.x & 63;
    const int tt16 = 4 * tb + w, tk = lane & 15, g = lane >> 4;
    const unsigned vl = lane * 16;
    STDECL; ST(0);
    const char* wsrc = ws + WS_WO + (size_t)cg * 100 * 1024 + w * 5120;
    const char* osrc = ws + WS_O + (size_t)tt16 * 20 * 1024;
    u32x4 of[20];
    sfor<0, 5>([&](auto S_) {
        constexpr int S = S_.value;
        sfor<0, 5>([&](auto J) { glds<S * 20480 + J.value * 1024>(vl, wsrc + S * 20480 + J.value * 1024, ldsb + w * 5120); });
        sfor<0, 4>([&](auto J) { gld128<J.value * 1024>(of[4 * S + J.value], vl, osrc + S * 4096); });
    });
    u32x2 xr[5];
    const unsigned vxr = (16 * tt16 + tk) * 1280 + 8 * g;
    sfor<0, 5>([&](auto I) { gld64<32 * I.value>(xr[I.value], vxr, (const char*)x + cg * 160); });
    f32x4 acc[5] = {};
    sfor<0, 5>([&](auto S_) {
        constexpr int S = S_.value;
        if constexpr (S == 0) ST(1);
        wait_vm_bar<5 + 9 * (4 - S)>();
        if constexpr (S == 0) ST(2);
        sfor<0, 4>([&](auto J) { pin(of[4 * S + J.value]); });
#pragma unroll
        for (int k = 0; k < 4; ++k)
#pragma unroll
            for (int i = 0; i < 5; ++i) acc[i] = mfma16(lds128(lds + ((4 * S + k) * 5 + i) * 1024 + vl), of[4 * S + k], acc[i]);
    });
    asm volatile("s_waitcnt vmcnt(0)" : "+v"(xr[0]), "+v"(xr[1]), "+v"(xr[2]), "+v"(xr[3]), "+v"(xr[4])::"memory");
    ST(3);
    u32x2 h[5];
    float s1 = 0.f, s2 = 0.f;
#pragma unroll
    for (int i = 0; i < 5; ++i) {
        const unsigned a0 = pk2(acc[i][0], acc[i][1]), a1 = pk2(acc[i][2], acc[i][3]), x0 = xr[i][0], x1 = xr[i][1];
        const unsigned h0 = pk2(bflo(x0) + bflo(a0), bfhi(x0) + bfhi(a0)), h1 = pk2(bflo(x1) + bflo(a1), bfhi(x1) + bfhi(a1));
        h[i] = u32x2{h0, h1};
        s1 = dot2(h0, 0x3f803f80u, dot2(h1, 0x3f803f80u, s1));
        s2 = dot2(h0, h0, dot2(h1, h1, s2));
    }
    char* hb = ws + WS_H + (size_t)tt16 * 20 * 1024 + vl;
    *(u32x4*)(hb + (2 * cg) * 1024) = u32x4{h[0][0], h[0][1], h[1][0], h[1][1]};
    *(u32x4*)(hb + (2 * cg + 1) * 1024) = u32x4{h[2][0], h[2][1], h[3][0], h[3][1]};
    *(u32x2*)(hb + (16 + (cg >> 1)) * 1024 + (cg & 1) * 8) = h[4];
    s1 = swap32_add(swap16_add(s1)); s2 = swap32_add(swap16_add(s2));
    if (g == 0) *(f32x2*)(ws + WS_ST + (size_t)(16 * tt16 + tk) * 64 + cg * 8) = f32x2{s1, s2};
    ST(4); STWAIT; ST(5); STFLUSH(2);
}

constexpr int D_TAB = 140 * 1024, D_LDS = D_TAB + 1024;

DI float gelu_tanh(float a) {
    const float z = a * fmaf(a * a, -0.044715f * 2.0f * 0.7978845608028654f * 1.4426950408889634f, -2.0f * 0.7978845608028654f * 1.4426950408889634f);
    return a * __builtin_amdgcn_rcpf(1.0f + __builtin_amdgcn_exp2f(z));
}

__global__ __launch_bounds__(256, 1) void k_up(const u16* b1, char* ws) {
    extern __shared__ __attribute__((aligned(16))) char smem[];
    const lptr lds = (lptr)smem;
    const unsigned ldsb = (unsigned)(size_t)lds;
    const int b = blockIdx.x, tb = 2 * (b & 7) + ((b >> 3) & 1), c = b >> 4;
    const int w = __builtin_amdgcn_readfirstlane(threadIdx.x >> 6), lane = threadIdx.x & 63;
    const int tt16 = 8 * tb + 2 * w, tk = lane & 15, g = lane >> 4;
    const int ft0 = up_start(c);
    const bool has7 = (c & 3) != 3;
    const unsigned vl = lane * 16;
    STDECL; ST(0);
    const char* wsrc = ws + WS_W1 + (size_t)(ft0 * 20 + w) * 1024;
    const char* w7 = wsrc + (has7 ? 6 : 5) * 20480;
    const char* hsrc = ws + WS_H + (size_t)tt16 * 20 * 1024;

    u32x4 st0, st1;
    unsigned c0, c1, c2, c3, bb;
    {
        const unsigned vs = tk * 64 + g * 16;
        const char* sp = ws + WS_ST + (size_t)tt16 * 16 * 64;
        gld128<0>(st0, vs, sp); gld128<1024>(st1, vs, sp);
        const int n = min(16 * ft0 + (int)(threadIdx.x & 127), FF - 1);
        const char* cp = ws + WS_CS;
        gld32<0>(c0, n * 4, cp); gld32<0>(c1, n * 4, cp + FF * 4); gld32<0>(c2, n * 4, cp + FF * 8); gld32<0>(c3, n * 4, cp + FF * 12);
        gldu16<0>(bb, n * 2, b1);
    }
    u32x4 hf[20][2];
    auto issue = [&](auto S_) {
        constexpr int S = S_.value;
        sfor<0, 6>([&](auto I) { glds<S * 28672 + I.value * 1024>(vl, wsrc + I.value * 20480 + S * 4096, ldsb + w * 7168); });
        glds<S * 28672 + 6144>(vl, w7 + S * 4096, ldsb + w * 7168);
        sfor<0, 4>([&](auto J) {
            gld128<J.value * 1024>(hf[4 * S + J.value][0], vl, hsrc + S * 4096);
            gld128<J.value * 1024>(hf[4 * S + J.value][1], vl, hsrc + 20480 + S * 4096);
        });
    };
    sfor<0, 3>(issue);
    f32x4 acc[2][7] = {};
    sfor<0, 5>([&](auto S_) {
        constexpr int S = S_.value;
        if constexpr (S == 0) ST(1);
        wait_vm_bar<15 * ((S + 2 < 4 ? S + 2 : 4) - S)>();
        if constexpr (S == 0) ST(2);
        sfor<0, 4>([&](auto J) { pin(hf[4 * S + J.value][0]); pin(hf[4 * S + J.value][1]); });
        if constexpr (S == 0) {
            pin(c0); pin(c1); pin(c2); pin(c3); pin(bb); pin(st0); pin(st1);
            if (threadIdx.x < 128) {
                ((LAS float*)(lds + D_TAB))[threadIdx.x] = (__uint_as_float(c0) + __uint_as_float(c1)) + (__uint_as_float(c2) + __uint_as_float(c3));
                ((LAS float*)(lds + D_TAB))[128 + threadIdx.x] = bflo(bb);
            }
        }
        if constexpr (S + 3 < 5) issue(std::integral_constant<int, S + 3>{});
#pragma unroll
        for (int k = 0; k < 4; ++k)
#pragma unroll
            for (int i = 0; i < 7; ++i) {
                const u32x4 wf = lds128(lds + ((4 * S + k) * 7 + i) * 1024 + vl);
                acc[0][i] = mfma16(wf, hf[4 * S + k][0], acc[0][i]);
                acc[1][i] = mfma16(wf, hf[4 * S + k][1], acc[1][i]);
            }
    });
    ST(3);
#pragma unroll
    for (int t = 0; t < 2; ++t) {
        const u32x4 st = t == 0 ? st0 : st1;
        const unsigned u0 = st[0], u1 = st[1], u2 = st[2], u3 = st[3];
        const float s1 = swap32_add(swap16_add(__uint_as_float(u0) + __uint_as_float(u2)));
        const float s2 = swap32_add(swap16_add(__uint_as_float(u1) + __uint_as_float(u3)));
        float m, rs;
        ln_stats(s1, s2, m, rs);
        u32x2 o[7];
#pragma unroll
        for (int i = 0; i < 7; ++i) {
            const f32x4 cs = *(LAS f32x4*)(lds + D_TAB + (16 * i + 4 * g) * 4), bv = *(LAS f32x4*)(lds + D_TAB + 512 + (16 * i + 4 * g) * 4);
            float v[4];
#pragma unroll
            for (int e = 0; e < 4; ++e) v[e] = gelu_tanh(fmaf(acc[t][i][e] - m * cs[e], rs, bv[e]));
            o[i] = u32x2{pk2(v[0], v[1]), pk2(v[2], v[3])};
        }
        char* gb = ws + WS_G + (size_t)(tt16 + t) * 54 * 1024 + vl;
#pragma unroll
        for (int p = 0; p < 3; ++p) *(u32x4*)(gb + (3 * c + p) * 1024) = u32x4{o[2 * p][0], o[2 * p][1], o[2 * p + 1][0], o[2 * p + 1][1]};
        if (has7) { const int lam = 3 * (c >> 2) + (c & 3); *(u32x2*)(gb + (48 + (lam >> 1)) * 1024 + (lam & 1) * 8) = o[6]; }
    }
    ST(4); STWAIT; ST(5); STFLUSH(3);
}

constexpr int E_SLOTS = 7, E_LDS = E_SLOTS * 20480;

__global__ __launch_bounds__(256, 1) void k_down(const u16* b2, char* ws, u16* out) {
    extern __shared__ __attribute__((aligned(16))) char smem[];
    const lptr lds = (lptr)smem;
    const unsigned ldsb = (unsigned)(size_t)lds;
    const int b = blockIdx.x, tb = 4 * (b & 7) + ((b >> 3) & 3), cg = b >> 5;
    const int w = __builtin_amdgcn_readfirstlane(threadIdx.x >> 6), lane = threadIdx.x & 63;
    const int tt16 = 4 * tb + w, tk = lane & 15, g = lane >> 4;
    const unsigned vl = lane * 16;
    STDECL; ST(0);
    const char* wsrc = ws + WS_W2 + (size_t)cg * 270 * 1024;
    const char* gsrc = ws + WS_G + (size_t)tt16 * 54 * 1024;

    u32x4 r01, r23;
    u32x2 r4, bv[5];
    {
        const char* hb = ws + WS_H + (size_t)tt16 * 20 * 1024;
        gld128<0>(r01, vl, hb + 2 * cg * 1024); gld128<1024>(r23, vl, hb + 2 * cg * 1024);
        gld64<0>(r4, vl, hb + (16 + (cg >> 1)) * 1024 + (cg & 1) * 8);
        sfor<0, 5>([&](auto I) { gld64<32 * I.value>(bv[I.value], 8 * g, (const char*)b2 + cg * 160); });
    }
    u32x4 gf[54];
    auto issue = [&](auto S_) {
        constexpr int S = S_.value, SL = (S % E_SLOTS) * 20480;
        if constexpr (S < 13) {
            sfor<0, 5>([&](auto J) { glds<SL + J.value * 1024>(vl, wsrc + S * 20480 + w * 5120 + J.value * 1024, ldsb + w * 5120); });
            sfor<0, 4>([&](auto J) { gld128<J.value * 1024>(gf[4 * S + J.value], vl, gsrc + S * 4096); });
        } else {
            const int p0 = w == 3 ? 7 : 3 * w;
            sfor<0, 3>([&](auto J) { glds<SL + J.value * 1024>(vl, wsrc + S * 20480 + p0 * 1024 + J.value * 1024, ldsb + p0 * 1024); });
            sfor<0, 2>([&](auto J) { gld128<J.value * 1024>(gf[4 * S + J.value], vl, gsrc + S * 4096); });
        }
    };
    sfor<0, 6>(issue);
    f32x4 acc[5] = {};
    sfor<0, 14>([&](auto S_) {
        constexpr int S = S_.value, SL = (S % E_SLOTS) * 20480, NK = S < 13 ? 4 : 2;
        constexpr int LAST = S + 5 < 13 ? S + 5 : 13;
        if constexpr (S == 0) ST(1);
        wait_vm_bar<(LAST == S ? 0 : 9 * (LAST - S) - (LAST == 13 ? 4 : 0))>();
        if constexpr (S == 0) ST(2);
        sfor<0, NK>([&](auto J) { pin(gf[4 * S + J.value]); });
        if constexpr (S + 6 < 14) issue(std::integral_constant<int, S + 6>{});
#pragma unroll
        for (int k = 0; k < NK; ++k)
#pragma unroll
            for (int i = 0; i < 5; ++i) acc[i] = mfma16(lds128(lds + SL + (k * 5 + i) * 1024 + vl), gf[4 * S + k], acc[i]);
    });
    ST(3);
    pin(r01); pin(r23); pin(r4);
    char* ob = (char*)out + (size_t)(16 * tt16 + tk) * 1280 + cg * 160 + 8 * g;
#pragma unroll
    for (int i = 0; i < 5; ++i) {
        pin(bv[i]);
        const unsigned h0 = i == 0 ? r01[0] : i == 1 ? r01[2] : i == 2 ? r23[0] : i == 3 ? r23[2] : r4[0];
        const unsigned h1 = i == 0 ? r01[1] : i == 1 ? r01[3] : i == 2 ? r23[1] : i == 3 ? r23[3] : r4[1];
        const unsigned y0 = pk2(acc[i][0], acc[i][1]), y1 = pk2(acc[i][2], acc[i][3]), b0 = bv[i][0], b1 = bv[i][1];
        const unsigned z0 = pk2(bflo(y0) + bflo(b0), bfhi(y0) + bfhi(b0)), z1 = pk2(bflo(y1) + bflo(b1), bfhi(y1) + bfhi(b1));
        *(u32x2*)(ob + 32 * i) = u32x2{pk2(bflo(h0) + bflo(z0), bfhi(h0) + bfhi(z0)), pk2(bflo(h1) + bflo(z1), bfhi(h1) + bfhi(z1))};
    }
    ST(4); STWAIT; ST(5); STFLUSH(4);
}

extern "C" void kernel_launch(void* const* d_in, const int* in_sizes, int n_in, void* d_out, int out_size, void* d_ws, size_t ws_size, hipStream_t stream) {
    const u16* x = (const u16*)d_in[0];
    const u16* wqkv = (const u16*)d_in[1];
    const u16* wo = (const u16*)d_in[2];
    const u16* w1 = (const u16*)d_in[4];
    const u16* b1 = (const u16*)d_in[5];
    const u16* w2 = (const u16*)d_in[6];
    const u16* b2 = (const u16*)d_in[7];
    char* ws = (char*)d_ws;
    (void)hipFuncSetAttribute((const void*)k_qkv, hipFuncAttributeMaxDynamicSharedMemorySize, A_LDS);
    (void)hipFuncSetAttribute((const void*)k_attn, hipFuncAttributeMaxDynamicSharedMemorySize, B_LDS);
    (void)hipFuncSetAttribute((const void*)k_wo, hipFuncAttributeMaxDynamicSharedMemorySize, C_LDS);
    (void)hipFuncSetAttribute((const void*)k_up, hipFuncAttributeMaxDynamicSharedMemorySize, D_LDS);
    (void)hipFuncSetAttribute((const void*)k_down, hipFuncAttributeMaxDynamicSharedMemorySize, E_LDS);
    k_qkv<<<256, 256, A_LDS, stream>>>(x, wqkv, ws);
    k_attn<<<256, 512, B_LDS, stream>>>(wo, w1, w2, ws);
    k_wo<<<256, 256, C_LDS, stream>>>(x, ws);
    k_up<<<256, 256, D_LDS, stream>>>(b1, ws);
    k_down<<<256, 256, E_LDS, stream>>>(b2, ws, (u16*)d_out);
}
```

```cpp
#include <hip/hip_runtime.h>
#include <hip/hip_bf16.h>
#include <stdint.h>

typedef unsigned short u16;
typedef short bf16x8 __attribute__((ext_vector_type(8)));
typedef short s16x4 __attribute__((ext_vector_type(4)));
typedef float f32x16 __attribute__((ext_vector_type(16)));
typedef float f32x8 __attribute__((ext_vector_type(8)));
typedef float f32x4 __attribute__((ext_vector_type(4)));
typedef float f32x2 __attribute__((ext_vector_type(2)));
typedef unsigned u32x8 __attribute__((ext_vector_type(8)));
typedef unsigned u32x4 __attribute__((ext_vector_type(4)));
typedef unsigned u32x2 __attribute__((ext_vector_type(2)));
typedef __bf16 bf2 __attribute__((ext_vector_type(2)));
#define LAS __attribute__((address_space(3)))
#define DI __device__ __forceinline__
typedef LAS char* lptr;

constexpr int SEQ = 2048, DM = 640, NH = 32, FF = 1728;
constexpr int QREC = 1280, KVREC = 2560;

constexpr int FL_E = 0, FL_R = 2048, FL_A = 4096, FL_B = FL_A + 256 * 128, FL_C = FL_B + 256 * 128, FL_D = FL_C + 256 * 128;
constexpr size_t WS_Q = 262144;
constexpr size_t WS_KV = WS_Q + (size_t)NH * 64 * QREC;
constexpr size_t WS_O = WS_KV + (size_t)NH * 64 * KVREC + 4096;
constexpr size_t WS_H = WS_O + (size_t)128 * 20 * 1024;
constexpr size_t WS_G = WS_H + (size_t)128 * 20 * 1024;
constexpr size_t WS_WO = WS_G + (size_t)128 * 54 * 1024;
constexpr size_t WS_W1 = WS_WO + (size_t)8 * 20 * 5 * 1024;
constexpr size_t WS_W2 = WS_W1 + (size_t)108 * 20 * 1024;
constexpr size_t WS_ST = WS_W2 + (size_t)8 * 54 * 5 * 1024;
constexpr size_t WS_CS = WS_ST + (size_t)SEQ * 16 * 8;
constexpr size_t WS_END = WS_CS + (size_t)8 * FF * 4;

template <int V> using IC = std::integral_constant<int, V>;
#define SBAR() __builtin_amdgcn_sched_barrier(0)
template <int I, int N, class F> DI void sfor(F&& f) {
    if constexpr (I < N) { f(std::integral_constant<int, I>{}); sfor<I + 1, N>(f); }
}

DI unsigned pk2(float lo, float hi) { f32x2 v = {lo, hi}; bf2 b = __builtin_convertvector(v, bf2); return __builtin_bit_cast(unsigned, b); }
DI float bflo(unsigned u) { return __uint_as_float(u << 16); }
DI float bfhi(unsigned u) { return __uint_as_float(u & 0xffff0000u); }
DI float rbf(float x) { return bflo(pk2(x, x)); }
DI float dot2(unsigned a, unsigned b, float c) { return __builtin_amdgcn_fdot2_f32_bf16(__builtin_bit_cast(bf2, a), __builtin_bit_cast(bf2, b), c, false); }
DI float swap32_add(float v) { auto r = __builtin_amdgcn_permlane32_swap(__float_as_uint(v), __float_as_uint(v), false, false); return __uint_as_float(r[0]) + __uint_as_float(r[1]); }
DI float swap32_max(float v) { auto r = __builtin_amdgcn_permlane32_swap(__float_as_uint(v), __float_as_uint(v), false, false); return fmaxf(__uint_as_float(r[0]), __uint_as_float(r[1])); }
DI float swap16_add(float v) { auto r = __builtin_amdgcn_permlane16_swap(__float_as_uint(v), __float_as_uint(v), false, false); return __uint_as_float(r[0]) + __uint_as_float(r[1]); }
DI f32x16 mfma32(u32x4 a, u32x4 b, f32x16 c) { return __builtin_amdgcn_mfma_f32_32x32x16_bf16(__builtin_bit_cast(bf16x8, a), __builtin_bit_cast(bf16x8, b), c, 0, 0, 0); }
DI f32x16 mfma32k8(u32x2 a, u32x2 b, f32x16 c) { return __builtin_amdgcn_mfma_f32_32x32x8bf16_1k(__builtin_bit_cast(s16x4, a), __builtin_bit_cast(s16x4, b), c, 0, 0, 0); }
DI f32x4 mfma16(u32x4 a, u32x4 b, f32x4 c) { return __builtin_amdgcn_mfma_f32_16x16x32_bf16(__builtin_bit_cast(bf16x8, a), __builtin_bit_cast(bf16x8, b), c, 0, 0, 0); }
DI u32x4 lds128(lptr p) { return *(LAS u32x4*)p; }
DI u32x2 ldstr(lptr p) { return __builtin_bit_cast(u32x2, __builtin_amdgcn_ds_read_tr16_b64_v4i16((LAS s16x4*)p)); }

template <int OFF> DI void glds(unsigned voff, const void* sbase, unsigned ldsb) {
    asm volatile("s_add_u32 m0, %2, %3\n\ts_nop 0\n\tglobal_load_lds_dwordx4 %0, %1" ::"v"(voff), "s"(sbase), "s"(ldsb), "i"(OFF) : "memory", "scc");
}
template <int IMM> DI void gld128(u32x4& d, unsigned voff, const void* sbase) {
    asm volatile("global_load_dwordx4 %0, %1, %2 offset:%3" : "=v"(d) : "v"(voff), "s"(sbase), "i"(IMM) : "memory");
}
template <int IMM> DI void gld64(u32x2& d, unsigned voff, const void* sbase) {
    asm volatile("global_load_dwordx2 %0, %1, %2 offset:%3" : "=v"(d) : "v"(voff), "s"(sbase), "i"(IMM) : "memory");
}
template <int IMM> DI void gld32(unsigned& d, unsigned voff, const void* sbase) {
    asm volatile("global_load_dword %0, %1, %2 offset:%3" : "=v"(d) : "v"(voff), "s"(sbase), "i"(IMM) : "memory");
}
template <int IMM> DI void gldu16(unsigned& d, unsigned voff, const void* sbase) {
    asm volatile("global_load_ushort %0, %1, %2 offset:%3" : "=v"(d) : "v"(voff), "s"(sbase), "i"(IMM) : "memory");
}
DI void glds_s(unsigned voff, const void* sbase, unsigned ldsa) {
    asm volatile("s_mov_b32 m0, %2\n\ts_nop 0\n\tglobal_load_lds_dwordx4 %0, %1 sc1" ::"v"(voff), "s"(sbase), "s"(ldsa) : "memory");
}
template <int OFF, unsigned LO, unsigned HI> DI void glds_m(unsigned voff, const void* sbase, unsigned ldsb) {
    asm volatile("s_add_u32 m0, %2, %3\n\ts_mov_b32 exec_lo, %4\n\ts_mov_b32 exec_hi, %5\n\tglobal_load_lds_dwordx4 %0, %1 sc1\n\ts_mov_b64 exec, -1"
                 ::"v"(voff), "s"(sbase), "s"(ldsb), "i"(OFF), "i"(LO), "i"(HI) : "memory", "scc");
}
template <int IMM> DI void gld128s(u32x4& d, unsigned voff, const void* sbase) {
    asm volatile("global_load_dwordx4 %0, %1, %2 offset:%3 sc1" : "=v"(d) : "v"(voff), "s"(sbase), "i"(IMM) : "memory");
}
template <int IMM> DI void gld64s(u32x2& d, unsigned voff, const void* sbase) {
    asm volatile("global_load_dwordx2 %0, %1, %2 offset:%3 sc1" : "=v"(d) : "v"(voff), "s"(sbase), "i"(IMM) : "memory");
}
DI void gst128(unsigned voff, const void* sbase, u32x4 v) {
    asm volatile("global_store_dwordx4 %0, %1, %2 sc1\n\ts_nop 1" ::"v"(voff), "v"(v), "s"(sbase) : "memory");
}
DI void gst64(unsigned voff, const void* sbase, u32x2 v) {
    asm volatile("global_store_dwordx2 %0, %1, %2 sc1" ::"v"(voff), "v"(v), "s"(sbase) : "memory");
}
DI void warm64(unsigned voff, const void* sbase, unsigned ldsa) {
    asm volatile("s_mov_b32 m0, %2\n\ts_nop 0\n\tglobal_load_lds_dword %0, %1 sc1" ::"v"(voff), "s"(sbase), "s"(ldsa) : "memory");
}
template <int N> DI void wait_vm() { asm volatile("s_waitcnt vmcnt(%0)" ::"i"(N) : "memory"); }
template <int N> DI void wait_vm_bar() { asm volatile("s_waitcnt vmcnt(%0) lgkmcnt(0)\n\ts_barrier" ::"i"(N) : "memory"); }
DI void lds_bar() { asm volatile("s_waitcnt lgkmcnt(0)\n\ts_barrier" ::: "memory"); }
template <class T> DI void pin(T& v) { asm volatile("" : "+v"(v)); }

#define STARG
#define STPASS
#define STWAIT
#define STDECL
#define ST(i)
#define STR(i)
#define STFLUSH(k)

DI void ln_stats(float s1, float s2, float& m, float& rs) {
    const float mean = s1 * (1.0f / DM);
    const float var = fmaxf(s2 * (1.0f / DM) - mean * mean, 0.0f);
    m = rbf(mean);
    rs = __builtin_amdgcn_rcpf(rbf(__builtin_amdgcn_sqrtf(rbf(rbf(var) + 1e-5f))));
}

#define QKV_LOOP_QK \
    "s_nop 1\n\t" \
    "1:\n\t" \
    "s_waitcnt lgkmcnt(0)\n\t" \
    "v_mfma_f32_32x32x16_bf16 v[64:79], v[40:43], v[32:35], v[64:79]\n\t" \
    "ds_read_b128 v[48:51], %[x1] offset:0\n\t" \
    "ds_read_b64_tr_b16 v[56:57], %[w0] offset:4096\n\t" \
    "ds_read_b64_tr_b16 v[58:59], %[w0] offset:5120\n\t" \
    "v_lshlrev_b32 v14, 16, v32\n\t" \
    "v_and_b32 v15, 0xffff0000, v32\n\t" \
    "v_mfma_f32_32x32x16_bf16 v[80:95], v[44:47], v[32:35], v[80:95]\n\t" \
    "ds_read_b64_tr_b16 v[60:61], %[w1] offset:4096\n\t" \
    "ds_read_b64_tr_b16 v[62:63], %[w1] offset:5120\n\t" \
    "v_fmac_f32 v18, v14, v14\n\t" \
    "v_fmac_f32 v19, v15, v15\n\t" \
    "v_lshlrev_b32 v16, 16, v33\n\t" \
    "v_and_b32 v17, 0xffff0000, v33\n\t" \
    "v_mfma_f32_16x16x32_bf16 v[24:27], v[20:23], v[32:35], v[24:27]\n\t" \
    "ds_read_b128 v[52:55], %[y1] offset:0\n\t" \
    "v_fmac_f32 v18, v16, v16\n\t" \
    "v_mfma_f32_32x32x16_bf16 v[96:111], v[40:43], v[36:39], v[96:111]\n\t" \
    "v_fmac_f32 v19, v17, v17\n\t" \
    "v_lshlrev_b32 v14, 16, v34\n\t" \
    "v_and_b32 v15, 0xffff0000, v34\n\t" \
    "v_fmac_f32 v18, v14, v14\n\t" \
    "v_fmac_f32 v19, v15, v15\n\t" \
    "v_mfma_f32_32x32x16_bf16 v[112:127], v[44:47], v[36:39], v[112:127]\n\t" \
    "v_lshlrev_b32 v16, 16, v35\n\t" \
    "v_and_b32 v17, 0xffff0000, v35\n\t" \
    "v_fmac_f32 v18, v16, v16\n\t" \
    "v_fmac_f32 v19, v17, v17\n\t" \
    "v_mfma_f32_16x16x32_bf16 v[28:31], v[20:23], v[40:43], v[28:31]\n\t" \
    "s_waitcnt lgkmcnt(0)\n\t" \
    "v_mfma_f32_32x32x16_bf16 v[64:79], v[56:59], v[48:51], v[64:79]\n\t" \
    "ds_read_b128 v[32:35], %[x2] offset:0\n\t" \
    "ds_read_b64_tr_b16 v[40:41], %[w0] offset:8192\n\t" \
    "ds_read_b64_tr_b16 v[42:43], %[w0] offset:9216\n\t" \
    "v_lshlrev_b32 v14, 16, v48\n\t" \
    "v_and_b32 v15, 0xffff0000, v48\n\t" \
    "v_mfma_f32_32x32x16_bf16 v[80:95], v[60:63], v[48:51], v[80:95]\n\t" \
    "ds_read_b64_tr_b16 v[44:45], %[w1] offset:8192\n\t" \
    "ds_read_b64_tr_b16 v[46:47], %[w1] offset:9216\n\t" \
    "v_fmac_f32 v18, v14, v14\n\t" \
    "v_fmac_f32 v19, v15, v15\n\t" \
    "v_lshlrev_b32 v16, 16, v49\n\t" \
    "v_and_b32 v17, 0xffff0000, v49\n\t" \
    "v_mfma_f32_16x16x32_bf16 v[24:27], v[20:23], v[48:51], v[24:27]\n\t" \
    "ds_read_b128 v[36:39], %[y2] offset:0\n\t" \
    "v_fmac_f32 v18, v16, v16\n\t" \
    "v_mfma_f32_32x32x16_bf16 v[96:111], v[56:59], v[52:55], v[96:111]\n\t" \
    "v_fmac_f32 v19, v17, v17\n\t" \
    "v_lshlrev_b32 v14, 16, v50\n\t" \
    "v_and_b32 v15, 0xffff0000, v50\n\t" \
    "v_fmac_f32 v18, v14, v14\n\t" \
    "v_fmac_f32 v19, v15, v15\n\t" \
    "v_mfma_f32_32x32x16_bf16 v[112:127], v[60:63], v[52:55], v[112:127]\n\t" \
    "v_lshlrev_b32 v16, 16, v51\n\t" \
    "v_and_b32 v17, 0xffff0000, v51\n\t" \
    "v_fmac_f32 v18, v16, v16\n\t" \
    "v_fmac_f32 v19, v17, v17\n\t" \
    "v_mfma_f32_16x16x32_bf16 v[28:31], v[20:23], v[56:59], v[28:31]\n\t" \
    "s_waitcnt lgkmcnt(0)\n\t" \
    "v_mfma_f32_32x32x16_bf16 v[64:79], v[40:43], v[32:35], v[64:79]\n\t" \
    "ds_read_b128 v[48:51], %[x3] offset:0\n\t" \
    "ds_read_b64_tr_b16 v[56:57], %[w0] offset:12288\n\t" \
    "ds_read_b64_tr_b16 v[58:59], %[w0] offset:13312\n\t" \
    "v_lshlrev_b32 v14, 16, v32\n\t" \
    "v_and_b32 v15, 0xffff0000, v32\n\t" \
    "v_mfma_f32_32x32x16_bf16 v[80:95], v[44:47], v[32:35], v[80:95]\n\t" \
    "ds_read_b64_tr_b16 v[60:61], %[w1] offset:12288\n\t" \
    "ds_read_b64_tr_b16 v[62:63], %[w1] offset:13312\n\t" \
    "v_fmac_f32 v18, v14, v14\n\t" \
    "v_fmac_f32 v19, v15, v15\n\t" \
    "v_lshlrev_b32 v16, 16, v33\n\t" \
    "v_and_b32 v17, 0xffff0000, v33\n\t" \
    "v_mfma_f32_16x16x32_bf16 v[24:27], v[20:23], v[32:35], v[24:27]\n\t" \
    "ds_read_b128 v[52:55], %[y3] offset:0\n\t" \
    "v_fmac_f32 v18, v16, v16\n\t" \
    "v_mfma_f32_32x32x16_bf16 v[96:111], v[40:43], v[36:39], v[96:111]\n\t" \
    "v_fmac_f32 v19, v17, v17\n\t" \
    "v_lshlrev_b32 v14, 16, v34\n\t" \
    "v_and_b32 v15, 0xffff0000, v34\n\t" \
    "v_fmac_f32 v18, v14, v14\n\t" \
    "v_fmac_f32 v19, v15, v15\n\t" \
    "v_mfma_f32_32x32x16_bf16 v[112:127], v[44:47], v[36:39], v[112:127]\n\t" \
    "v_lshlrev_b32 v16, 16, v35\n\t" \
    "v_and_b32 v17, 0xffff0000, v35\n\t" \
    "v_fmac_f32 v18, v16, v16\n\t" \
    "v_fmac_f32 v19, v17, v17\n\t" \
    "v_mfma_f32_16x16x32_bf16 v[28:31], v[20:23], v[40:43], v[28:31]\n\t" \
    "s_waitcnt lgkmcnt(0)\n\t" \
    "s_barrier\n\t" \
    "v_mfma_f32_32x32x16_bf16 v[64:79], v[56:59], v[48:51], v[64:79]\n\t" \
    "ds_read_b128 v[32:35], %[x0] offset:32768\n\t" \
    "ds_read_b64_tr_b16 v[40:41], %[w0] offset:32768\n\t" \
    "ds_read_b64_tr_b16 v[42:43], %[w0] offset:33792\n\t" \
    "v_lshlrev_b32 v14, 16, v48\n\t" \
    "v_and_b32 v15, 0xffff0000, v48\n\t" \
    "v_mfma_f32_32x32x16_bf16 v[80:95], v[60:63], v[48:51], v[80:95]\n\t" \
    "ds_read_b64_tr_b16 v[44:45], %[w1] offset:32768\n\t" \
    "ds_read_b64_tr_b16 v[46:47], %[w1] offset:33792\n\t" \
    "v_fmac_f32 v18, v14, v14\n\t" \
    "v_fmac_f32 v19, v15, v15\n\t" \
    "v_lshlrev_b32 v16, 16, v49\n\t" \
    "v_and_b32 v17, 0xffff0000, v49\n\t" \
    "v_mfma_f32_16x16x32_bf16 v[24:27], v[20:23], v[48:51], v[24:27]\n\t" \
    "ds_read_b128 v[36:39], %[y0] offset:32768\n\t" \
    "v_fmac_f32 v18, v16, v16\n\t" \
    "v_mfma_f32_32x32x16_bf16 v[96:111], v[56:59], v[52:55], v[96:111]\n\t" \
    "v_fmac_f32 v19, v17, v17\n\t" \
    "v_lshlrev_b32 v14, 16, v50\n\t" \
    "v_and_b32 v15, 0xffff0000, v50\n\t" \
    "v_fmac_f32 v18, v14, v14\n\t" \
    "v_fmac_f32 v19, v15, v15\n\t" \
    "v_mfma_f32_32x32x16_bf16 v[112:127], v[60:63], v[52:55], v[112:127]\n\t" \
    "v_lshlrev_b32 v16, 16, v51\n\t" \
    "v_and_b32 v17, 0xffff0000, v51\n\t" \
    "v_fmac_f32 v18, v16, v16\n\t" \
    "v_fmac_f32 v19, v17, v17\n\t" \
    "v_mfma_f32_16x16x32_bf16 v[28:31], v[20:23], v[56:59], v[28:31]\n\t" \
    "s_waitcnt lgkmcnt(0)\n\t" \
    "v_mfma_f32_32x32x16_bf16 v[64:79], v[40:43], v[32:35], v[64:79]\n\t" \
    "ds_read_b128 v[48:51], %[x1] offset:32768\n\t" \
    "ds_read_b64_tr_b16 v[56:57], %[w0] offset:36864\n\t" \
    "ds_read_b64_tr_b16 v[58:59], %[w0] offset:37888\n\t" \
    "v_lshlrev_b32 v14, 16, v32\n\t" \
    "v_and_b32 v15, 0xffff0000, v32\n\t" \
    "v_mfma_f32_32x32x16_bf16 v[80:95], v[44:47], v[32:35], v[80:95]\n\t" \
    "ds_read_b64_tr_b16 v[60:61], %[w1] offset:36864\n\t" \
    "ds_read_b64_tr_b16 v[62:63], %[w1] offset:37888\n\t" \
    "v_fmac_f32 v18, v14, v14\n\t" \
    "v_fmac_f32 v19, v15, v15\n\t" \
    "v_lshlrev_b32 v16, 16, v33\n\t" \
    "v_and_b32 v17, 0xffff0000, v33\n\t" \
    "v_mfma_f32_16x16x32_bf16 v[24:27], v[20:23], v[32:35], v[24:27]\n\t" \
    "ds_read_b128 v[52:55], %[y1] offset:32768\n\t" \
    "v_fmac_f32 v18, v16, v16\n\t" \
    "v_mfma_f32_32x32x16_bf16 v[96:111], v[40:43], v[36:39], v[96:111]\n\t" \
    "v_fmac_f32 v19, v17, v17\n\t" \
    "v_lshlrev_b32 v14, 16, v34\n\t" \
    "v_and_b32 v15, 0xffff0000, v34\n\t" \
    "v_fmac_f32 v18, v14, v14\n\t" \
    "v_fmac_f32 v19, v15, v15\n\t" \
    "v_mfma_f32_32x32x16_bf16 v[112:127], v[44:47], v[36:39], v[112:127]\n\t" \
    "v_lshlrev_b32 v16, 16, v35\n\t" \
    "v_and_b32 v17, 0xffff0000, v35\n\t" \
    "v_fmac_f32 v18, v16, v16\n\t" \
    "v_fmac_f32 v19, v17, v17\n\t" \
    "v_mfma_f32_16x16x32_bf16 v[28:31], v[20:23], v[40:43], v[28:31]\n\t" \
    "v_xor_b32 %[x0], 0x10000, %[x0]\n\t" \
    "v_xor_b32 %[y0], 0x10000, %[y0]\n\t" \
    "s_waitcnt lgkmcnt(0)\n\t" \
    "v_mfma_f32_32x32x16_bf16 v[64:79], v[56:59], v[48:51], v[64:79]\n\t" \
    "ds_read_b128 v[32:35], %[x2] offset:32768\n\t" \
    "ds_read_b64_tr_b16 v[40:41], %[w0] offset:40960\n\t" \
    "ds_read_b64_tr_b16 v[42:43], %[w0] offset:41984\n\t" \
    "v_lshlrev_b32 v14, 16, v48\n\t" \
    "v_and_b32 v15, 0xffff0000, v48\n\t" \
    "v_mfma_f32_32x32x16_bf16 v[80:95], v[60:63], v[48:51], v[80:95]\n\t" \
    "ds_read_b64_tr_b16 v[44:45], %[w1] offset:40960\n\t" \
    "ds_read_b64_tr_b16 v[46:47], %[w1] offset:41984\n\t" \
    "v_fmac_f32 v18, v14, v14\n\t" \
    "v_fmac_f32 v19, v15, v15\n\t" \
    "v_lshlrev_b32 v16, 16, v49\n\t" \
    "v_and_b32 v17, 0xffff0000, v49\n\t" \
    "v_mfma_f32_16x16x32_bf16 v[24:27], v[20:23], v[48:51], v[24:27]\n\t" \
    "ds_read_b128 v[36:39], %[y2] offset:32768\n\t" \
    "v_fmac_f32 v18, v16, v16\n\t" \
    "v_mfma_f32_32x32x16_bf16 v[96:111], v[56:59], v[52:55], v[96:111]\n\t" \
    "v_fmac_f32 v19, v17, v17\n\t" \
    "v_lshlrev_b32 v14, 16, v50\n\t" \
    "v_and_b32 v15, 0xffff0000, v50\n\t" \
    "v_fmac_f32 v18, v14, v14\n\t" \
    "v_fmac_f32 v19, v15, v15\n\t" \
    "v_mfma_f32_32x32x16_bf16 v[112:127], v[60:63], v[52:55], v[112:127]\n\t" \
    "v_lshlrev_b32 v16, 16, v51\n\t" \
    "v_and_b32 v17, 0xffff0000, v51\n\t" \
    "v_fmac_f32 v18, v16, v16\n\t" \
    "v_fmac_f32 v19, v17, v17\n\t" \
    "v_mfma_f32_16x16x32_bf16 v[28:31], v[20:23], v[56:59], v[28:31]\n\t" \
    "v_xor_b32 %[x1], 0x10000, %[x1]\n\t" \
    "v_xor_b32 %[y1], 0x10000, %[y1]\n\t" \
    "s_waitcnt lgkmcnt(0)\n\t" \
    "v_mfma_f32_32x32x16_bf16 v[64:79], v[40:43], v[32:35], v[64:79]\n\t" \
    "ds_read_b128 v[48:51], %[x3] offset:32768\n\t" \
    "ds_read_b64_tr_b16 v[56:57], %[w0] offset:45056\n\t" \
    "ds_read_b64_tr_b16 v[58:59], %[w0] offset:46080\n\t" \
    "v_lshlrev_b32 v14, 16, v32\n\t" \
    "v_and_b32 v15, 0xffff0000, v32\n\t" \
    "v_mfma_f32_32x32x16_bf16 v[80:95], v[44:47], v[32:35], v[80:95]\n\t" \
    "ds_read_b64_tr_b16 v[60:61], %[w1] offset:45056\n\t" \
    "ds_read_b64_tr_b16 v[62:63], %[w1] offset:46080\n\t" \
    "v_fmac_f32 v18, v14, v14\n\t" \
    "v_fmac_f32 v19, v15, v15\n\t" \
    "v_lshlrev_b32 v16, 16, v33\n\t" \
    "v_and_b32 v17, 0xffff0000, v33\n\t" \
    "v_mfma_f32_16x16x32_bf16 v[24:27], v[20:23], v[32:35], v[24:27]\n\t" \
    "ds_read_b128 v[52:55], %[y3] offset:32768\n\t" \
    "v_fmac_f32 v18, v16, v16\n\t" \
    "v_mfma_f32_32x32x16_bf16 v[96:111], v[40:43], v[36:39], v[96:111]\n\t" \
    "v_fmac_f32 v19, v17, v17\n\t" \
    "v_lshlrev_b32 v14, 16, v34\n\t" \
    "v_and_b32 v15, 0xffff0000, v34\n\t" \
    "v_fmac_f32 v18, v14, v14\n\t" \
    "v_fmac_f32 v19, v15, v15\n\t" \
    "v_mfma_f32_32x32x16_bf16 v[112:127], v[44:47], v[36:39], v[112:127]\n\t" \
    "v_lshlrev_b32 v16, 16, v35\n\t" \
    "v_and_b32 v17, 0xffff0000, v35\n\t" \
    "v_fmac_f32 v18, v16, v16\n\t" \
    "v_fmac_f32 v19, v17, v17\n\t" \
    "v_mfma_f32_16x16x32_bf16 v[28:31], v[20:23], v[40:43], v[28:31]\n\t" \
    "v_xor_b32 %[w0], 0x10000, %[w0]\n\t" \
    "v_xor_b32 %[w1], 0x10000, %[w1]\n\t" \
    "s_waitcnt lgkmcnt(0)\n\t" \
    "s_barrier\n\t" \
    "v_mfma_f32_32x32x16_bf16 v[64:79], v[56:59], v[48:51], v[64:79]\n\t" \
    "ds_read_b128 v[32:35], %[x0] offset:0\n\t" \
    "ds_read_b64_tr_b16 v[40:41], %[w0] offset:0\n\t" \
    "ds_read_b64_tr_b16 v[42:43], %[w0] offset:1024\n\t" \
    "v_lshlrev_b32 v14, 16, v48\n\t" \
    "v_and_b32 v15, 0xffff0000, v48\n\t" \
    "v_mfma_f32_32x32x16_bf16 v[80:95], v[60:63], v[48:51], v[80:95]\n\t" \
    "ds_read_b64_tr_b16 v[44:45], %[w1] offset:0\n\t" \
    "ds_read_b64_tr_b16 v[46:47], %[w1] offset:1024\n\t" \
    "v_fmac_f32 v18, v14, v14\n\t" \
    "v_fmac_f32 v19, v15, v15\n\t" \
    "v_lshlrev_b32 v16, 16, v49\n\t" \
    "v_and_b32 v17, 0xffff0000, v49\n\t" \
    "v_mfma_f32_16x16x32_bf16 v[24:27], v[20:23], v[48:51], v[24:27]\n\t" \
    "ds_read_b128 v[36:39], %[y0] offset:0\n\t" \
    "v_fmac_f32 v18, v16, v16\n\t" \
    "v_mfma_f32_32x32x16_bf16 v[96:111], v[56:59], v[52:55], v[96:111]\n\t" \
    "v_fmac_f32 v19, v17, v17\n\t" \
    "v_lshlrev_b32 v14, 16, v50\n\t" \
    "v_and_b32 v15, 0xffff0000, v50\n\t" \
    "v_fmac_f32 v18, v14, v14\n\t" \
    "v_fmac_f32 v19, v15, v15\n\t" \
    "v_mfma_f32_32x32x16_bf16 v[112:127], v[60:63], v[52:55], v[112:127]\n\t" \
    "v_lshlrev_b32 v16, 16, v51\n\t" \
    "v_and_b32 v17, 0xffff0000, v51\n\t" \
    "v_fmac_f32 v18, v16, v16\n\t" \
    "v_fmac_f32 v19, v17, v17\n\t" \
    "v_mfma_f32_16x16x32_bf16 v[28:31], v[20:23], v[56:59], v[28:31]\n\t" \
    "v_xor_b32 %[x2], 0x10000, %[x2]\n\t" \
    "v_xor_b32 %[y2], 0x10000, %[y2]\n\t" \
    "v_xor_b32 %[x3], 0x10000, %[x3]\n\t" \
    "v_xor_b32 %[y3], 0x10000, %[y3]\n\t" \
    "s_sub_u32 %[cnt], %[cnt], 1\n\t" \
    "s_cbranch_scc0 1b\n\t" \
    "s_waitcnt lgkmcnt(0)\n\t" \
    "s_nop 15\n\t"
#define QKV_LOOP_V \
    "s_nop 1\n\t" \
    "1:\n\t" \
    "s_waitcnt lgkmcnt(0)\n\t" \
    "v_mfma_f32_32x32x16_bf16 v[64:79], v[32:35], v[40:43], v[64:79]\n\t" \
    "ds_read_b128 v[48:51], %[x1] offset:0\n\t" \
    "ds_read_b64_tr_b16 v[56:57], %[w0] offset:4096\n\t" \
    "ds_read_b64_tr_b16 v[58:59], %[w0] offset:5120\n\t" \
    "v_lshlrev_b32 v14, 16, v32\n\t" \
    "v_and_b32 v15, 0xffff0000, v32\n\t" \
    "v_mfma_f32_32x32x16_bf16 v[80:95], v[32:35], v[44:47], v[80:95]\n\t" \
    "ds_read_b64_tr_b16 v[60:61], %[w1] offset:4096\n\t" \
    "ds_read_b64_tr_b16 v[62:63], %[w1] offset:5120\n\t" \
    "v_fmac_f32 v18, v14, v14\n\t" \
    "v_fmac_f32 v19, v15, v15\n\t" \
    "v_lshlrev_b32 v16, 16, v33\n\t" \
    "v_and_b32 v17, 0xffff0000, v33\n\t" \
    "v_mfma_f32_16x16x32_bf16 v[24:27], v[20:23], v[32:35], v[24:27]\n\t" \
    "ds_read_b128 v[52:55], %[y1] offset:0\n\t" \
    "v_fmac_f32 v18, v16, v16\n\t" \
    "v_mfma_f32_32x32x16_bf16 v[96:111], v[36:39], v[40:43], v[96:111]\n\t" \
    "v_fmac_f32 v19, v17, v17\n\t" \
    "v_lshlrev_b32 v14, 16, v34\n\t" \
    "v_and_b32 v15, 0xffff0000, v34\n\t" \
    "v_fmac_f32 v18, v14, v14\n\t" \
    "v_fmac_f32 v19, v15, v15\n\t" \
    "v_mfma_f32_32x32x16_bf16 v[112:127], v[36:39], v[44:47], v[112:127]\n\t" \
    "v_lshlrev_b32 v16, 16, v35\n\t" \
    "v_and_b32 v17, 0xffff0000, v35\n\t" \
    "v_fmac_f32 v18, v16, v16\n\t" \
    "v_fmac_f32 v19, v17, v17\n\t" \
    "v_mfma_f32_16x16x32_bf16 v[28:31], v[20:23], v[40:43], v[28:31]\n\t" \
    "s_waitcnt lgkmcnt(0)\n\t" \
    "v_mfma_f32_32x32x16_bf16 v[64:79], v[48:51], v[56:59], v[64:79]\n\t" \
    "ds_read_b128 v[32:35], %[x2] offset:0\n\t" \
    "ds_read_b64_tr_b16 v[40:41], %[w0] offset:8192\n\t" \
    "ds_read_b64_tr_b16 v[42:43], %[w0] offset:9216\n\t" \
    "v_lshlrev_b32 v14, 16, v48\n\t" \
    "v_and_b32 v15, 0xffff0000, v48\n\t" \
    "v_mfma_f32_32x32x16_bf16 v[80:95], v[48:51], v[60:63], v[80:95]\n\t" \
    "ds_read_b64_tr_b16 v[44:45], %[w1] offset:8192\n\t" \
    "ds_read_b64_tr_b16 v[46:47], %[w1] offset:9216\n\t" \
    "v_fmac_f32 v18, v14, v14\n\t" \
    "v_fmac_f32 v19, v15, v15\n\t" \
    "v_lshlrev_b32 v16, 16, v49\n\t" \
    "v_and_b32 v17, 0xffff0000, v49\n\t" \
    "v_mfma_f32_16x16x32_bf16 v[24:27], v[20:23], v[48:51], v[24:27]\n\t" \
    "ds_read_b128 v[36:39], %[y2] offset:0\n\t" \
    "v_fmac_f32 v18, v16, v16\n\t" \
    "v_mfma_f32_32x32x16_bf16 v[96:111], v[52:55], v[56:59], v[96:111]\n\t" \
    "v_fmac_f32 v19, v17, v17\n\t" \
    "v_lshlrev_b32 v14, 16, v50\n\t" \
    "v_and_b32 v15, 0xffff0000, v50\n\t" \
    "v_fmac_f32 v18, v14, v14\n\t" \
    "v_fmac_f32 v19, v15, v15\n\t" \
    "v_mfma_f32_32x32x16_bf16 v[112:127], v[52:55], v[60:63], v[112:127]\n\t" \
    "v_lshlrev_b32 v16, 16, v51\n\t" \
    "v_and_b32 v17, 0xffff0000, v51\n\t" \
    "v_fmac_f32 v18, v16, v16\n\t" \
    "v_fmac_f32 v19, v17, v17\n\t" \
    "v_mfma_f32_16x16x32_bf16 v[28:31], v[20:23], v[56:59], v[28:31]\n\t" \
    "s_waitcnt lgkmcnt(0)\n\t" \
    "v_mfma_f32_32x32x16_bf16 v[64:79], v[32:35], v[40:43], v[64:79]\n\t" \
    "ds_read_b128 v[48:51], %[x3] offset:0\n\t" \
    "ds_read_b64_tr_b16 v[56:57], %[w0] offset:12288\n\t" \
    "ds_read_b64_tr_b16 v[58:59], %[w0] offset:13312\n\t" \
    "v_lshlrev_b32 v14, 16, v32\n\t" \
    "v_and_b32 v15, 0xffff0000, v32\n\t" \
    "v_mfma_f32_32x32x16_bf16 v[80:95], v[32:35], v[44:47], v[80:95]\n\t" \
    "ds_read_b64_tr_b16 v[60:61], %[w1] offset:12288\n\t" \
    "ds_read_b64_tr_b16 v[62:63], %[w1] offset:13312\n\t" \
    "v_fmac_f32 v18, v14, v14\n\t" \
    "v_fmac_f32 v19, v15, v15\n\t" \
    "v_lshlrev_b32 v16, 16, v33\n\t" \
    "v_and_b32 v17, 0xffff0000, v33\n\t" \
    "v_mfma_f32_16x16x32_bf16 v[24:27], v[20:23], v[32:35], v[24:27]\n\t" \
    "ds_read_b128 v[52:55], %[y3] offset:0\n\t" \
    "v_fmac_f32 v18, v16, v16\n\t" \
    "v_mfma_f32_32x32x16_bf16 v[96:111], v[36:39], v[40:43], v[96:111]\n\t" \
    "v_fmac_f32 v19, v17, v17\n\t" \
    "v_lshlrev_b32 v14, 16, v34\n\t" \
    "v_and_b32 v15, 0xffff0000, v34\n\t" \
    "v_fmac_f32 v18, v14, v14\n\t" \
    "v_fmac_f32 v19, v15, v15\n\t" \
    "v_mfma_f32_32x32x16_bf16 v[112:127], v[36:39], v[44:47], v[112:127]\n\t" \
    "v_lshlrev_b32 v16, 16, v35\n\t" \
    "v_and_b32 v17, 0xffff0000, v35\n\t" \
    "v_fmac_f32 v18, v16, v16\n\t" \
    "v_fmac_f32 v19, v17, v17\n\t" \
    "v_mfma_f32_16x16x32_bf16 v[28:31], v[20:23], v[40:43], v[28:31]\n\t" \
    "s_waitcnt lgkmcnt(0)\n\t" \
    "s_barrier\n\t" \
    "v_mfma_f32_32x32x16_bf16 v[64:79], v[48:51], v[56:59], v[64:79]\n\t" \
    "ds_read_b128 v[32:35], %[x0] offset:32768\n\t" \
    "ds_read_b64_tr_b16 v[40:41], %[w0] offset:32768\n\t" \
    "ds_read_b64_tr_b16 v[42:43], %[w0] offset:33792\n\t" \
    "v_lshlrev_b32 v14, 16, v48\n\t" \
    "v_and_b32 v15, 0xffff0000, v48\n\t" \
    "v_mfma_f32_32x32x16_bf16 v[80:95], v[48:51], v[60:63], v[80:95]\n\t" \
    "ds_read_b64_tr_b16 v[44:45], %[w1] offset:32768\n\t" \
    "ds_read_b64_tr_b16 v[46:47], %[w1] offset:33792\n\t" \
    "v_fmac_f32 v18, v14, v14\n\t" \
    "v_fmac_f32 v19, v15, v15\n\t" \
    "v_lshlrev_b32 v16, 16, v49\n\t" \
    "v_and_b32 v17, 0xffff0000, v49\n\t" \
    "v_mfma_f32_16x16x32_bf16 v[24:27], v[20:23], v[48:51], v[24:27]\n\t" \
    "ds_read_b128 v[36:39], %[y0] offset:32768\n\t" \
    "v_fmac_f32 v18, v16, v16\n\t" \
    "v_mfma_f32_32x32x16_bf16 v[96:111], v[52:55], v[56:59], v[96:111]\n\t" \
    "v_fmac_f32 v19, v17, v17\n\t" \
    "v_lshlrev_b32 v14, 16, v50\n\t" \
    "v_and_b32 v15, 0xffff0000, v50\n\t" \
    "v_fmac_f32 v18, v14, v14\n\t" \
    "v_fmac_f32 v19, v15, v15\n\t" \
    "v_mfma_f32_32x32x16_bf16 v[112:127], v[52:55], v[60:63], v[112:127]\n\t" \
    "v_lshlrev_b32 v16, 16, v51\n\t" \
    "v_and_b32 v17, 0xffff0000, v51\n\t" \
    "v_fmac_f32 v18, v16, v16\n\t" \
    "v_fmac_f32 v19, v17, v17\n\t" \
    "v_mfma_f32_16x16x32_bf16 v[28:31], v[20:23], v[56:59], v[28:31]\n\t" \
    "s_waitcnt lgkmcnt(0)\n\t" \
    "v_mfma_f32_32x32x16_bf16 v[64:79], v[32:35], v[40:43], v[64:79]\n\t" \
    "ds_read_b128 v[48:51], %[x1] offset:32768\n\t" \
    "ds_read_b64_tr_b16 v[56:57], %[w0] offset:36864\n\t" \
    "ds_read_b64_tr_b16 v[58:59], %[w0] offset:37888\n\t" \
    "v_lshlrev_b32 v14, 16, v32\n\t" \
    "v_and_b32 v15, 0xffff0000, v32\n\t" \
    "v_mfma_f32_32x32x16_bf16 v[80:95], v[32:35], v[44:47], v[80:95]\n\t" \
    "ds_read_b64_tr_b16 v[60:61], %[w1] offset:36864\n\t" \
    "ds_read_b64_tr_b16 v[62:63], %[w1] offset:37888\n\t" \
    "v_fmac_f32 v18, v14, v14\n\t" \
    "v_fmac_f32 v19, v15, v15\n\t" \
    "v_lshlrev_b32 v16, 16, v33\n\t" \
    "v_and_b32 v17, 0xffff0000, v33\n\t" \
    "v_mfma_f32_16x16x32_bf16 v[24:27], v[20:23], v[32:35], v[24:27]\n\t" \
    "ds_read_b128 v[52:55], %[y1] offset:32768\n\t" \
    "v_fmac_f32 v18, v16, v16\n\t" \
    "v_mfma_f32_32x32x16_bf16 v[96:111], v[36:39], v[40:43], v[96:111]\n\t" \
    "v_fmac_f32 v19, v17, v17\n\t" \
    "v_lshlrev_b32 v14, 16, v34\n\t" \
    "v_and_b32 v15, 0xffff0000, v34\n\t" \
    "v_fmac_f32 v18, v14, v14\n\t" \
    "v_fmac_f32 v19, v15, v15\n\t" \
    "v_mfma_f32_32x32x16_bf16 v[112:127], v[36:39], v[44:47], v[112:127]\n\t" \
    "v_lshlrev_b32 v16, 16, v35\n\t" \
    "v_and_b32 v17, 0xffff0000, v35\n\t" \
    "v_fmac_f32 v18, v16, v16\n\t" \
    "v_fmac_f32 v19, v17, v17\n\t" \
    "v_mfma_f32_16x16x32_bf16 v[28:31], v[20:23], v[40:43], v[28:31]\n\t" \
    "v_xor_b32 %[x0], 0x10000, %[x0]\n\t" \
    "v_xor_b32 %[y0], 0x10000, %[y0]\n\t" \
    "s_waitcnt lgkmcnt(0)\n\t" \
    "v_mfma_f32_32x32x16_bf16 v[64:79], v[48:51], v[56:59], v[64:79]\n\t" \
    "ds_read_b128 v[32:35], %[x2] offset:32768\n\t" \
    "ds_read_b64_tr_b16 v[40:41], %[w0] offset:40960\n\t" \
    "ds_read_b64_tr_b16 v[42:43], %[w0] offset:41984\n\t" \
    "v_lshlrev_b32 v14, 16, v48\n\t" \
    "v_and_b32 v15, 0xffff0000, v48\n\t" \
    "v_mfma_f32_32x32x16_bf16 v[80:95], v[48:51], v[60:63], v[80:95]\n\t" \
    "ds_read_b64_tr_b16 v[44:45], %[w1] offset:40960\n\t" \
    "ds_read_b64_tr_b16 v[46:47], %[w1] offset:41984\n\t" \
    "v_fmac_f32 v18, v14, v14\n\t" \
    "v_fmac_f32 v19, v15, v15\n\t" \
    "v_lshlrev_b32 v16, 16, v49\n\t" \
    "v_and_b32 v17, 0xffff0000, v49\n\t" \
    "v_mfma_f32_16x16x32_bf16 v[24:27], v[20:23], v[48:51], v[24:27]\n\t" \
    "ds_read_b128 v[36:39], %[y2] offset:32768\n\t" \
    "v_fmac_f32 v18, v16, v16\n\t" \
    "v_mfma_f32_32x32x16_bf16 v[96:111], v[52:55], v[56:59], v[96:111]\n\t" \
    "v_fmac_f32 v19, v17, v17\n\t" \
    "v_lshlrev_b32 v14, 16, v50\n\t" \
    "v_and_b32 v15, 0xffff0000, v50\n\t" \
    "v_fmac_f32 v18, v14, v14\n\t" \
    "v_fmac_f32 v19, v15, v15\n\t" \
    "v_mfma_f32_32x32x16_bf16 v[112:127], v[52:55], v[60:63], v[112:127]\n\t" \
    "v_lshlrev_b32 v16, 16, v51\n\t" \
    "v_and_b32 v17, 0xffff0000, v51\n\t" \
    "v_fmac_f32 v18, v16, v16\n\t" \
    "v_fmac_f32 v19, v17, v17\n\t" \
    "v_mfma_f32_16x16x32_bf16 v[28:31], v[20:23], v[56:59], v[28:31]\n\t" \
    "v_xor_b32 %[x1], 0x10000, %[x1]\n\t" \
    "v_xor_b32 %[y1], 0x10000, %[y1]\n\t" \
    "s_waitcnt lgkmcnt(0)\n\t" \
    "v_mfma_f32_32x32x16_bf16 v[64:79], v[32:35], v[40:43], v[64:79]\n\t" \
    "ds_read_b128 v[48:51], %[x3] offset:32768\n\t" \
    "ds_read_b64_tr_b16 v[56:57], %[w0] offset:45056\n\t" \
    "ds_read_b64_tr_b16 v[58:59], %[w0] offset:46080\n\t" \
    "v_lshlrev_b32 v14, 16, v32\n\t" \
    "v_and_b32 v15, 0xffff0000, v32\n\t" \
    "v_mfma_f32_32x32x16_bf16 v[80:95], v[32:35], v[44:47], v[80:95]\n\t" \
    "ds_read_b64_tr_b16 v[60:61], %[w1] offset:45056\n\t" \
    "ds_read_b64_tr_b16 v[62:63], %[w1] offset:46080\n\t" \
    "v_fmac_f32 v18, v14, v14\n\t" \
    "v_fmac_f32 v19, v15, v15\n\t" \
    "v_lshlrev_b32 v16, 16, v33\n\t" \
    "v_and_b32 v17, 0xffff0000, v33\n\t" \
    "v_mfma_f32_16x16x32_bf16 v[24:27], v[20:23], v[32:35], v[24:27]\n\t" \
    "ds_read_b128 v[52:55], %[y3] offset:32768\n\t" \
    "v_fmac_f32 v18, v16, v16\n\t" \
    "v_mfma_f32_32x32x16_bf16 v[96:111], v[36:39], v[40:43], v[96:111]\n\t" \
    "v_fmac_f32 v19, v17, v17\n\t" \
    "v_lshlrev_b32 v14, 16, v34\n\t" \
    "v_and_b32 v15, 0xffff0000, v34\n\t" \
    "v_fmac_f32 v18, v14, v14\n\t" \
    "v_fmac_f32 v19, v15, v15\n\t" \
    "v_mfma_f32_32x32x16_bf16 v[112:127], v[36:39], v[44:47], v[112:127]\n\t" \
    "v_lshlrev_b32 v16, 16, v35\n\t" \
    "v_and_b32 v17, 0xffff0000, v35\n\t" \
    "v_fmac_f32 v18, v16, v16\n\t" \
    "v_fmac_f32 v19, v17, v17\n\t" \
    "v_mfma_f32_16x16x32_bf16 v[28:31], v[20:23], v[40:43], v[28:31]\n\t" \
    "v_xor_b32 %[w0], 0x10000, %[w0]\n\t" \
    "v_xor_b32 %[w1], 0x10000, %[w1]\n\t" \
    "s_waitcnt lgkmcnt(0)\n\t" \
    "s_barrier\n\t" \
    "v_mfma_f32_32x32x16_bf16 v[64:79], v[48:51], v[56:59], v[64:79]\n\t" \
    "ds_read_b128 v[32:35], %[x0] offset:0\n\t" \
    "ds_read_b64_tr_b16 v[40:41], %[w0] offset:0\n\t" \
    "ds_read_b64_tr_b16 v[42:43], %[w0] offset:1024\n\t" \
    "v_lshlrev_b32 v14, 16, v48\n\t" \
    "v_and_b32 v15, 0xffff0000, v48\n\t" \
    "v_mfma_f32_32x32x16_bf16 v[80:95], v[48:51], v[60:63], v[80:95]\n\t" \
    "ds_read_b64_tr_b16 v[44:45], %[w1] offset:0\n\t" \
    "ds_read_b64_tr_b16 v[46:47], %[w1] offset:1024\n\t" \
    "v_fmac_f32 v18, v14, v14\n\t" \
    "v_fmac_f32 v19, v15, v15\n\t" \
    "v_lshlrev_b32 v16, 16, v49\n\t" \
    "v_and_b32 v17, 0xffff0000, v49\n\t" \
    "v_mfma_f32_16x16x32_bf16 v[24:27], v[20:23], v[48:51], v[24:27]\n\t" \
    "ds_read_b128 v[36:39], %[y0] offset:0\n\t" \
    "v_fmac_f32 v18, v16, v16\n\t" \
    "v_mfma_f32_32x32x16_bf16 v[96:111], v[52:55], v[56:59], v[96:111]\n\t" \
    "v_fmac_f32 v19, v17, v17\n\t" \
    "v_lshlrev_b32 v14, 16, v50\n\t" \
    "v_and_b32 v15, 0xffff0000, v50\n\t" \
    "v_fmac_f32 v18, v14, v14\n\t" \
    "v_fmac_f32 v19, v15, v15\n\t" \
    "v_mfma_f32_32x32x16_bf16 v[112:127], v[52:55], v[60:63], v[112:127]\n\t" \
    "v_lshlrev_b32 v16, 16, v51\n\t" \
    "v_and_b32 v17, 0xffff0000, v51\n\t" \
    "v_fmac_f32 v18, v16, v16\n\t" \
    "v_fmac_f32 v19, v17, v17\n\t" \
    "v_mfma_f32_16x16x32_bf16 v[28:31], v[20:23], v[56:59], v[28:31]\n\t" \
    "v_xor_b32 %[x2], 0x10000, %[x2]\n\t" \
    "v_xor_b32 %[y2], 0x10000, %[y2]\n\t" \
    "v_xor_b32 %[x3], 0x10000, %[x3]\n\t" \
    "v_xor_b32 %[y3], 0x10000, %[y3]\n\t" \
    "s_sub_u32 %[cnt], %[cnt], 1\n\t" \
    "s_cbranch_scc0 1b\n\t" \
    "s_waitcnt lgkmcnt(0)\n\t" \
    "s_nop 15\n\t"

constexpr int A_STAGE = 32768, A_IMG = 2 * A_STAGE, A_TAB = 4 * A_STAGE, A_END = A_TAB + 4096;

constexpr int quad_role(int head, int dq) { return head == 19 ? (dq == 0 ? 4 : dq - 1) : head == 25 ? (dq == 2 ? 4 : dq > 2 ? dq - 1 : dq) : dq; }
constexpr unsigned role_mask(int cgm, bool single) {
    unsigned m = 0;
    for (int n = 0; n < 32; ++n) { const int q = 32 * cgm + n, r = quad_role(q / 5, q % 5); m |= (single ? r == 4 : (r & 1) != 0) ? 1u << n : 0u; }
    return m;
}
template <bool S> DI unsigned role_bits(int cgm) {
    return cgm == 0 ? role_mask(0, S) : cgm == 1 ? role_mask(1, S) : cgm == 2 ? role_mask(2, S) : cgm == 3 ? role_mask(3, S) : role_mask(4, S);
}

template <bool ISV>
DI void qkv_consumer(char* ws, lptr lds, int tb, int cg, int w, int lane STARG) {
    const int th = w >> 1, fh = w & 1, hi = lane >> 5, l31 = lane & 31;
    unsigned xp[2][4], wp[2];
    const unsigned ldsb = (unsigned)(size_t)lds;
#pragma unroll
    for (int ks = 0; ks < 4; ++ks) {
        const int c = (((2 * ks + hi) ^ ((l31 >> 1) & 7)) << 4);
        xp[0][ks] = ldsb + (64 * th + 32 * fh + l31) * 128 + c;
        xp[1][ks] = ldsb + (64 * th + 32 * (1 - fh) + l31) * 128 + c;
    }
    {
        const int q = (lane >> 2) & 3, p = lane & 3, cb = (lane >> 4) & 1;
#pragma unroll
        for (int f = 0; f < 2; ++f) {
            const int ft = 2 * fh + (f == 0 ? th : 1 - th);
            wp[f] = ldsb + 16384 + (8 * hi + q) * 256 + (((4 * (ft ^ q)) + 2 * cb + (p >> 1)) << 4) + 8 * (p & 1);
        }
    }
    int oa[2][4];
    if constexpr (!ISV) {
        const int cgm = cg < 5 ? cg : cg - 5;
        const unsigned ms = role_bits<true>(cgm) >> (16 * fh + hi), mo = role_bits<false>(cgm) >> (16 * fh + hi);
        const int o0 = A_IMG + (2 * th + fh) * 8192 + (16 * fh + hi) * 256 + l31 * 16;
#pragma unroll
        for (int f = 0; f < 2; ++f)
#pragma unroll
            for (int g = 0; g < 4; ++g) {
                const int n = 8 * (f == 0 ? th : 1 - th) + 2 * g;
                oa[f][g] = o0 + n * 256 - ((ms >> n) & 1) * (l31 * 8) - ((mo >> n) & 1) * 248;
                pin(oa[f][g]);
            }
    } else {
#pragma unroll
        for (int f = 0; f < 2; ++f) {
            const int col = 128 * (cg - 10) + 64 * fh + 32 * (f == 0 ? th : 1 - th) + l31, head = (col * 3277) >> 16;
            oa[f][0] = (head * 64 + 4 * tb + 2 * th) * KVREC + 1280 + hi * 320 + (col - 20 * head) * 16;
            pin(oa[f][0]);
        }
    }
    const unsigned selv = (lane & 15) == ((lane >> 4) & 1) ? 0x3f803f80u : 0u;
    u32x4 sel = {selv, selv, selv, selv};
    f32x16 acc[2][2] = {};
    f32x4 sx = {}, sw = {};
    float s2a = 0.f, s2b = 0.f;
    u32x4 x00, x01, w00, w01, x10, x11, w10, w11;
    unsigned cnt = 4;
    asm volatile("" : "+{v[64:79]}"(acc[0][0]), "+{v[80:95]}"(acc[0][1]), "+{v[96:111]}"(acc[1][0]), "+{v[112:127]}"(acc[1][1]), "+{v[24:27]}"(sx), "+{v[28:31]}"(sw), "+{v[20:23]}"(sel));
    ST(1);
    lds_bar();
    ST(2);
    {
        x00 = lds128((lptr)(size_t)xp[0][0]); x01 = lds128((lptr)(size_t)xp[1][0]);
        const u32x2 a = ldstr((lptr)(size_t)wp[0]), b = ldstr((lptr)(size_t)(wp[0] + 1024)), c = ldstr((lptr)(size_t)wp[1]), d = ldstr((lptr)(size_t)(wp[1] + 1024));
        w00 = u32x4{a[0], a[1], b[0], b[1]}; w01 = u32x4{c[0], c[1], d[0], d[1]};
    }
#define QKV_OPERANDS \
        : "+{v[64:79]}"(acc[0][0]), "+{v[80:95]}"(acc[0][1]), "+{v[96:111]}"(acc[1][0]), "+{v[112:127]}"(acc[1][1]), "+{v[24:27]}"(sx), "+{v[28:31]}"(sw), \
          "+{v18}"(s2a), "+{v19}"(s2b), "+{v[32:35]}"(x00), "+{v[36:39]}"(x01), "+{v[40:43]}"(w00), "+{v[44:47]}"(w01), \
          [x0] "+v"(xp[0][0]), [x1] "+v"(xp[0][1]), [x2] "+v"(xp[0][2]), [x3] "+v"(xp[0][3]), \
          [y0] "+v"(xp[1][0]), [y1] "+v"(xp[1][1]), [y2] "+v"(xp[1][2]), [y3] "+v"(xp[1][3]), [w0] "+v"(wp[0]), [w1] "+v"(wp[1]), [cnt] "+s"(cnt) \
        : "{v[20:23]}"(sel) \
        : "memory", "scc", "v14", "v15", "v16", "v17", "v48", "v49", "v50", "v51", "v52", "v53", "v54", "v55", "v56", "v57", "v58", "v59", "v60", "v61", "v62", "v63"
    if constexpr (ISV) asm volatile(QKV_LOOP_V QKV_OPERANDS);
    else asm volatile(QKV_LOOP_QK QKV_OPERANDS);
    ST(3);
    LAS float* T1 = (LAS float*)(lds + A_TAB);
    LAS float* T2 = T1 + 128;
    LAS float* TC = T1 + 256;
    LAS f32x2* TS = (LAS f32x2*)(T1 + 384) + w * 64;
    const float s2 = swap32_add(s2a + s2b);
    if (hi == 0) T2[64 * th + 32 * fh + l31] = s2;
    if (lane < 16) {
        T1[64 * th + 32 * fh + lane] = sx[0]; T1[64 * th + 32 * fh + 16 + lane] = sx[1];
        TC[64 * fh + 32 * th + lane] = sw[0]; TC[64 * fh + 32 * th + 16 + lane] = sw[1];
    }
    lds_bar();
    if constexpr (!ISV) {
        f32x4 c4[2][4];
        float t1[2], t2[2];
#pragma unroll
        for (int t = 0; t < 2; ++t) { t1[t] = T1[64 * th + 32 * (t == 0 ? fh : 1 - fh) + l31]; t2[t] = T2[64 * th + 32 * (t == 0 ? fh : 1 - fh) + l31]; }
#pragma unroll
        for (int f = 0; f < 2; ++f)
#pragma unroll
            for (int g = 0; g < 4; ++g) c4[f][g] = *(LAS f32x4*)(TC + 64 * fh + 32 * (f == 0 ? th : 1 - th) + 8 * g + 4 * hi);
        const float sc = cg < 5 ? 0.22360679774997896f * 1.4426950408889634f : 1.0f;
        const int flip = fh ? -8192 : 8192;
#pragma unroll
        for (int t = 0; t < 2; ++t) {
            float m, rs;
            ln_stats(t1[t], t2[t], m, rs);
            rs *= sc;
            const float mr = -m * rs;
#pragma unroll
            for (int f = 0; f < 2; ++f)
#pragma unroll
                for (int g = 0; g < 4; ++g) {
                    const f32x4 v = f32x4{acc[t][f][4 * g], acc[t][f][4 * g + 1], acc[t][f][4 * g + 2], acc[t][f][4 * g + 3]} * rs + c4[f][g] * mr;
                    *(LAS u32x2*)(lds + oa[f][g] + t * flip) = u32x2{pk2(v[0], v[1]), pk2(v[2], v[3])};
                }
        }
    } else {
        {
            float m, rs;
            ln_stats(T1[64 * th + lane], T2[64 * th + lane], m, rs);
            TS[lane] = f32x2{-m * rs, rs};
        }
        const float c0 = TC[64 * fh + 32 * th + l31], c1 = TC[64 * fh + 32 * (1 - th) + l31];
        asm volatile("s_waitcnt lgkmcnt(0)" ::: "memory");
#pragma unroll
        for (int t = 0; t < 2; ++t) {
            const int ta = t == 0 ? fh : 1 - fh;
            f32x4 sa[4], sb[4];
#pragma unroll
            for (int g = 0; g < 4; ++g) { sa[g] = *(LAS f32x4*)(TS + 32 * ta + 8 * g + 4 * hi); sb[g] = *(LAS f32x4*)(TS + 32 * ta + 8 * g + 4 * hi + 2); }
#pragma unroll
            for (int f = 0; f < 2; ++f) {
                const float c = f == 0 ? c0 : c1;
#pragma unroll
                for (int s = 0; s < 2; ++s) {
                    u32x4 o;
#pragma unroll
                    for (int g2 = 0; g2 < 2; ++g2) {
                        const int g = 2 * s + g2;
                        o[2 * g2] = pk2(fmaf(acc[t][f][4 * g], sa[g][1], sa[g][0] * c), fmaf(acc[t][f][4 * g + 1], sa[g][3], sa[g][2] * c));
                        o[2 * g2 + 1] = pk2(fmaf(acc[t][f][4 * g + 2], sb[g][1], sb[g][0] * c), fmaf(acc[t][f][4 * g + 3], sb[g][3], sb[g][2] * c));
                    }
                    gst128(oa[f][0], ws + WS_KV + ta * KVREC + s * 640, o);
                }
            }
        }
    }
}

DI void qkv_loader(const u16* x, const u16* wqkv, lptr lds, int tb, int cg, int j, int lane) {
    unsigned vx[4];
#pragma unroll
    for (int i = 0; i < 4; ++i) {
        const int row = 32 * j + 8 * i + (lane >> 3);
        vx[i] = row * 1280 + (((lane & 7) ^ ((row >> 1) & 7)) << 4);
    }
    unsigned vw[4];
#pragma unroll
    for (int i = 0; i < 4; ++i) vw[i] = (16 * j + 4 * i + (lane >> 4)) * 3840 + (((lane & 15) ^ ((lane >> 4) << 2)) << 4);
    const char* xs = (const char*)x + (size_t)tb * 128 * 1280;
    const char* wsrc = (const char*)wqkv + cg * 256;
    const unsigned ldw = (unsigned)(size_t)lds + j * 4096;
#define A_ROT 0
#define A_DEPTH 2
    const int rot = A_ROT ? (cg + 3 * tb) % 10 : 0;
    auto issue = [&](int s) {
        const unsigned dst = ldw + (s & 3) * A_STAGE;
        const int k = s + rot >= 10 ? s + rot - 10 : s + rot;
        sfor<0, 4>([&](auto I) { glds<I.value * 1024>(vx[I.value], xs + k * 128, dst); glds<16384 + I.value * 1024>(vw[I.value], wsrc + (size_t)k * 64 * 3840, dst); });
    };
    issue(0); issue(1);
    wait_vm_bar<8>();
#pragma unroll 1
    for (int s = 2; s < A_DEPTH; ++s) issue(s);
#pragma unroll 1
    for (int s = A_DEPTH; s < 10; ++s) { issue(s); wait_vm_bar<8 * (A_DEPTH - 1)>(); }
    sfor<0, A_DEPTH - 1>([&](auto I) { wait_vm_bar<8 * (A_DEPTH - 2 - I.value)>(); });
    lds_bar();
    lds_bar();
}

DI void qkv_phase(const u16* x, const u16* wqkv, char* ws, lptr lds, int tb, int cg, int w, int lane STARG) {
    if (w >= 4) qkv_loader(x, wqkv, lds, tb, cg, w - 4, lane);
    else if (cg >= 10) qkv_consumer<true>(ws, lds, tb, cg, w, lane STPASS);
    else qkv_consumer<false>(ws, lds, tb, cg, w, lane STPASS);
    if (cg < 10) {
        const int cgm = cg < 5 ? cg : cg - 5, rec = cg < 5 ? QREC : KVREC;
        const char* dst = ws + (cg < 5 ? WS_Q : WS_KV) + (size_t)4 * tb * rec;
        const int nq = 4 * w + (lane >> 4), gq = 32 * cgm + nq, head = (gq * 205) >> 10, role = quad_role(head, gq - 5 * head);
        const unsigned off = head * 64 * rec + role * 256 + (lane & 15) * 16;
        lds_bar();
        u32x4 v[4];
#pragma unroll
        for (int t = 0; t < 4; ++t) v[t] = lds128(lds + A_IMG + t * 8192 + w * 1024 + lane * 16);
#pragma unroll
        for (int t = 0; t < 4; ++t) gst128(off, dst + t * rec, v[t]);
    }
}

#define ATTN_LOOP_ASM \
    "s_nop 4\n\t" \
    "1:\n\t" \
    "v_mfma_f32_32x32x16_bf16 v[64:79], v[156:159], v[144:147], 0\n\t" \
    "ds_read_b128 v[160:163], %[va] offset:0\n\t" \
    "v_exp_f32 v80, v80\n\t" \
    "v_exp_f32 v81, v81\n\t" \
    "v_exp_f32 v82, v82\n\t" \
    "v_cvt_pk_bf16_f32 v136, v80, v81\n\t" \
    "v_exp_f32 v83, v83\n\t" \
    "v_mfma_f32_32x32x8_bf16 v[64:79], v[164:165], v[152:153], v[64:79]\n\t" \
    "v_cvt_pk_bf16_f32 v137, v82, v83\n\t" \
    "v_exp_f32 v84, v84\n\t" \
    "v_exp_f32 v85, v85\n\t" \
    "v_exp_f32 v86, v86\n\t" \
    "v_cvt_pk_bf16_f32 v138, v84, v85\n\t" \
    "v_exp_f32 v87, v87\n\t" \
    "v_mfma_f32_32x32x16_bf16 v[96:111], v[176:179], v[128:131], v[96:111]\n\t" \
    "ds_read_b64 v[166:167], %[vb] offset:1024\n\t" \
    "v_cvt_pk_bf16_f32 v139, v86, v87\n\t" \
    "v_exp_f32 v88, v88\n\t" \
    "v_exp_f32 v89, v89\n\t" \
    "v_exp_f32 v90, v90\n\t" \
    "v_cvt_pk_bf16_f32 v140, v88, v89\n\t" \
    "v_exp_f32 v91, v91\n\t" \
    "v_mfma_f32_32x32x16_bf16 v[96:111], v[180:183], v[132:135], v[96:111]\n\t" \
    "ds_read_b128 v[168:171], %[va] offset:1280\n\t" \
    "v_cvt_pk_bf16_f32 v141, v90, v91\n\t" \
    "v_exp_f32 v92, v92\n\t" \
    "v_exp_f32 v93, v93\n\t" \
    "v_exp_f32 v94, v94\n\t" \
    "v_exp_f32 v95, v95\n\t" \
    "v_cvt_pk_bf16_f32 v142, v92, v93\n\t" \
    "v_cvt_pk_bf16_f32 v143, v94, v95\n\t" \
    "v_mfma_f32_32x32x16_bf16 v[80:95], v[156:159], v[148:151], 0\n\t" \
    "ds_read_b128 v[172:175], %[va] offset:2304\n\t" \
    "v_exp_f32 v64, v64\n\t" \
    "v_exp_f32 v65, v65\n\t" \
    "v_exp_f32 v66, v66\n\t" \
    "v_cvt_pk_bf16_f32 v128, v64, v65\n\t" \
    "v_exp_f32 v67, v67\n\t" \
    "v_mfma_f32_32x32x8_bf16 v[80:95], v[164:165], v[154:155], v[80:95]\n\t" \
    "v_cvt_pk_bf16_f32 v129, v66, v67\n\t" \
    "v_exp_f32 v68, v68\n\t" \
    "v_exp_f32 v69, v69\n\t" \
    "v_exp_f32 v70, v70\n\t" \
    "v_cvt_pk_bf16_f32 v130, v68, v69\n\t" \
    "v_exp_f32 v71, v71\n\t" \
    "v_mfma_f32_32x32x16_bf16 v[112:127], v[176:179], v[136:139], v[112:127]\n\t" \
    "v_cvt_pk_bf16_f32 v131, v70, v71\n\t" \
    "v_exp_f32 v72, v72\n\t" \
    "v_exp_f32 v73, v73\n\t" \
    "v_exp_f32 v74, v74\n\t" \
    "v_cvt_pk_bf16_f32 v132, v72, v73\n\t" \
    "v_exp_f32 v75, v75\n\t" \
    "v_mfma_f32_32x32x16_bf16 v[112:127], v[180:183], v[140:143], v[112:127]\n\t" \
    "s_waitcnt lgkmcnt(0)\n\t" \
    "v_cvt_pk_bf16_f32 v133, v74, v75\n\t" \
    "v_exp_f32 v76, v76\n\t" \
    "v_exp_f32 v77, v77\n\t" \
    "v_exp_f32 v78, v78\n\t" \
    "v_exp_f32 v79, v79\n\t" \
    "v_cvt_pk_bf16_f32 v134, v76, v77\n\t" \
    "v_cvt_pk_bf16_f32 v135, v78, v79\n\t" \
    "v_mfma_f32_32x32x16_bf16 v[64:79], v[160:163], v[144:147], 0\n\t" \
    "ds_read_b128 v[156:159], %[va] offset:3328\n\t" \
    "v_exp_f32 v80, v80\n\t" \
    "v_exp_f32 v81, v81\n\t" \
    "v_exp_f32 v82, v82\n\t" \
    "v_cvt_pk_bf16_f32 v136, v80, v81\n\t" \
    "v_exp_f32 v83, v83\n\t" \
    "v_mfma_f32_32x32x8_bf16 v[64:79], v[166:167], v[152:153], v[64:79]\n\t" \
    "v_cvt_pk_bf16_f32 v137, v82, v83\n\t" \
    "v_exp_f32 v84, v84\n\t" \
    "v_exp_f32 v85, v85\n\t" \
    "v_exp_f32 v86, v86\n\t" \
    "v_cvt_pk_bf16_f32 v138, v84, v85\n\t" \
    "v_exp_f32 v87, v87\n\t" \
    "v_mfma_f32_32x32x16_bf16 v[96:111], v[168:171], v[128:131], v[96:111]\n\t" \
    "ds_read_b64 v[164:165], %[vb] offset:4352\n\t" \
    "v_cvt_pk_bf16_f32 v139, v86, v87\n\t" \
    "v_exp_f32 v88, v88\n\t" \
    "v_exp_f32 v89, v89\n\t" \
    "v_exp_f32 v90, v90\n\t" \
    "v_cvt_pk_bf16_f32 v140, v88, v89\n\t" \
    "v_exp_f32 v91, v91\n\t" \
    "v_mfma_f32_32x32x16_bf16 v[96:111], v[172:175], v[132:135], v[96:111]\n\t" \
    "ds_read_b128 v[176:179], %[va] offset:4608\n\t" \
    "v_cvt_pk_bf16_f32 v141, v90, v91\n\t" \
    "v_exp_f32 v92, v92\n\t" \
    "v_exp_f32 v93, v93\n\t" \
    "v_exp_f32 v94, v94\n\t" \
    "v_exp_f32 v95, v95\n\t" \
    "v_cvt_pk_bf16_f32 v142, v92, v93\n\t" \
    "v_cvt_pk_bf16_f32 v143, v94, v95\n\t" \
    "v_mfma_f32_32x32x16_bf16 v[80:95], v[160:163], v[148:151], 0\n\t" \
    "ds_read_b128 v[180:183], %[va] offset:5632\n\t" \
    "v_exp_f32 v64, v64\n\t" \
    "v_exp_f32 v65, v65\n\t" \
    "v_exp_f32 v66, v66\n\t" \
    "v_cvt_pk_bf16_f32 v128, v64, v65\n\t" \
    "v_exp_f32 v67, v67\n\t" \
    "v_mfma_f32_32x32x8_bf16 v[80:95], v[166:167], v[154:155], v[80:95]\n\t" \
    "v_cvt_pk_bf16_f32 v129, v66, v67\n\t" \
    "v_exp_f32 v68, v68\n\t" \
    "v_exp_f32 v69, v69\n\t" \
    "v_exp_f32 v70, v70\n\t" \
    "v_cvt_pk_bf16_f32 v130, v68, v69\n\t" \
    "v_exp_f32 v71, v71\n\t" \
    "v_mfma_f32_32x32x16_bf16 v[112:127], v[168:171], v[136:139], v[112:127]\n\t" \
    "v_cvt_pk_bf16_f32 v131, v70, v71\n\t" \
    "v_exp_f32 v72, v72\n\t" \
    "v_exp_f32 v73, v73\n\t" \
    "v_exp_f32 v74, v74\n\t" \
    "v_cvt_pk_bf16_f32 v132, v72, v73\n\t" \
    "v_exp_f32 v75, v75\n\t" \
    "v_mfma_f32_32x32x16_bf16 v[112:127], v[172:175], v[140:143], v[112:127]\n\t" \
    "s_waitcnt lgkmcnt(0)\n\t" \
    "v_cvt_pk_bf16_f32 v133, v74, v75\n\t" \
    "v_exp_f32 v76, v76\n\t" \
    "v_exp_f32 v77, v77\n\t" \
    "v_exp_f32 v78, v78\n\t" \
    "v_exp_f32 v79, v79\n\t" \
    "v_cvt_pk_bf16_f32 v134, v76, v77\n\t" \
    "v_cvt_pk_bf16_f32 v135, v78, v79\n\t" \
    "v_mfma_f32_32x32x16_bf16 v[64:79], v[156:159], v[144:147], 0\n\t" \
    "ds_read_b128 v[160:163], %[va] offset:6656\n\t" \
    "v_exp_f32 v80, v80\n\t" \
    "v_exp_f32 v81, v81\n\t" \
    "v_exp_f32 v82, v82\n\t" \
    "v_cvt_pk_bf16_f32 v136, v80, v81\n\t" \
    "v_exp_f32 v83, v83\n\t" \
    "v_mfma_f32_32x32x8_bf16 v[64:79], v[164:165], v[152:153], v[64:79]\n\t" \
    "v_cvt_pk_bf16_f32 v137, v82, v83\n\t" \
    "v_exp_f32 v84, v84\n\t" \
    "v_exp_f32 v85, v85\n\t" \
    "v_exp_f32 v86, v86\n\t" \
    "v_cvt_pk_bf16_f32 v138, v84, v85\n\t" \
    "v_exp_f32 v87, v87\n\t" \
    "v_mfma_f32_32x32x16_bf16 v[96:111], v[176:179], v[128:131], v[96:111]\n\t" \
    "ds_read_b64 v[166:167], %[vb] offset:7680\n\t" \
    "v_cvt_pk_bf16_f32 v139, v86, v87\n\t" \
    "v_exp_f32 v88, v88\n\t" \
    "v_exp_f32 v89, v89\n\t" \
    "v_exp_f32 v90, v90\n\t" \
    "v_cvt_pk_bf16_f32 v140, v88, v89\n\t" \
    "v_exp_f32 v91, v91\n\t" \
    "v_mfma_f32_32x32x16_bf16 v[96:111], v[180:183], v[132:135], v[96:111]\n\t" \
    "ds_read_b128 v[168:171], %[va] offset:7936\n\t" \
    "v_cvt_pk_bf16_f32 v141, v90, v91\n\t" \
    "v_exp_f32 v92, v92\n\t" \
    "v_exp_f32 v93, v93\n\t" \
    "v_exp_f32 v94, v94\n\t" \
    "v_exp_f32 v95, v95\n\t" \
    "v_cvt_pk_bf16_f32 v142, v92, v93\n\t" \
    "v_cvt_pk_bf16_f32 v143, v94, v95\n\t" \
    "v_mfma_f32_32x32x16_bf16 v[80:95], v[156:159], v[148:151], 0\n\t" \
    "ds_read_b128 v[172:175], %[va] offset:8960\n\t" \
    "v_exp_f32 v64, v64\n\t" \
    "v_exp_f32 v65, v65\n\t" \
    "v_exp_f32 v66, v66\n\t" \
    "v_cvt_pk_bf16_f32 v128, v64, v65\n\t" \
    "v_exp_f32 v67, v67\n\t" \
    "v_mfma_f32_32x32x8_bf16 v[80:95], v[164:165], v[154:155], v[80:95]\n\t" \
    "v_cvt_pk_bf16_f32 v129, v66, v67\n\t" \
    "v_exp_f32 v68, v68\n\t" \
    "v_exp_f32 v69, v69\n\t" \
    "v_exp_f32 v70, v70\n\t" \
    "v_cvt_pk_bf16_f32 v130, v68, v69\n\t" \
    "v_exp_f32 v71, v71\n\t" \
    "v_mfma_f32_32x32x16_bf16 v[112:127], v[176:179], v[136:139], v[112:127]\n\t" \
    "v_cvt_pk_bf16_f32 v131, v70, v71\n\t" \
    "v_exp_f32 v72, v72\n\t" \
    "v_exp_f32 v73, v73\n\t" \
    "v_exp_f32 v74, v74\n\t" \
    "v_cvt_pk_bf16_f32 v132, v72, v73\n\t" \
    "v_exp_f32 v75, v75\n\t" \
    "v_mfma_f32_32x32x16_bf16 v[112:127], v[180:183], v[140:143], v[112:127]\n\t" \
    "s_waitcnt lgkmcnt(0)\n\t" \
    "v_cvt_pk_bf16_f32 v133, v74, v75\n\t" \
    "v_exp_f32 v76, v76\n\t" \
    "v_exp_f32 v77, v77\n\t" \
    "v_exp_f32 v78, v78\n\t" \
    "v_exp_f32 v79, v79\n\t" \
    "v_cvt_pk_bf16_f32 v134, v76, v77\n\t" \
    "v_cvt_pk_bf16_f32 v135, v78, v79\n\t" \
    "v_mfma_f32_32x32x16_bf16 v[64:79], v[160:163], v[144:147], 0\n\t" \
    "ds_read_b128 v[156:159], %[va] offset:9984\n\t" \
    "v_exp_f32 v80, v80\n\t" \
    "v_exp_f32 v81, v81\n\t" \
    "v_exp_f32 v82, v82\n\t" \
    "v_cvt_pk_bf16_f32 v136, v80, v81\n\t" \
    "v_exp_f32 v83, v83\n\t" \
    "v_mfma_f32_32x32x8_bf16 v[64:79], v[166:167], v[152:153], v[64:79]\n\t" \
    "v_cvt_pk_bf16_f32 v137, v82, v83\n\t" \
    "v_exp_f32 v84, v84\n\t" \
    "v_exp_f32 v85, v85\n\t" \
    "v_exp_f32 v86, v86\n\t" \
    "v_cvt_pk_bf16_f32 v138, v84, v85\n\t" \
    "v_exp_f32 v87, v87\n\t" \
    "v_mfma_f32_32x32x16_bf16 v[96:111], v[168:171], v[128:131], v[96:111]\n\t" \
    "ds_read_b64 v[164:165], %[vb] offset:11008\n\t" \
    "v_cvt_pk_bf16_f32 v139, v86, v87\n\t" \
    "v_exp_f32 v88, v88\n\t" \
    "v_exp_f32 v89, v89\n\t" \
    "v_exp_f32 v90, v90\n\t" \
    "v_cvt_pk_bf16_f32 v140, v88, v89\n\t" \
    "v_exp_f32 v91, v91\n\t" \
    "v_mfma_f32_32x32x16_bf16 v[96:111], v[172:175], v[132:135], v[96:111]\n\t" \
    "ds_read_b128 v[176:179], %[va] offset:11264\n\t" \
    "v_cvt_pk_bf16_f32 v141, v90, v91\n\t" \
    "v_exp_f32 v92, v92\n\t" \
    "v_exp_f32 v93, v93\n\t" \
    "v_exp_f32 v94, v94\n\t" \
    "v_exp_f32 v95, v95\n\t" \
    "v_cvt_pk_bf16_f32 v142, v92, v93\n\t" \
    "v_cvt_pk_bf16_f32 v143, v94, v95\n\t" \
    "v_mfma_f32_32x32x16_bf16 v[80:95], v[160:163], v[148:151], 0\n\t" \
    "ds_read_b128 v[180:183], %[va] offset:12288\n\t" \
    "v_exp_f32 v64, v64\n\t" \
    "v_exp_f32 v65, v65\n\t" \
    "v_exp_f32 v66, v66\n\t" \
    "v_cvt_pk_bf16_f32 v128, v64, v65\n\t" \
    "v_exp_f32 v67, v67\n\t" \
    "v_mfma_f32_32x32x8_bf16 v[80:95], v[166:167], v[154:155], v[80:95]\n\t" \
    "v_cvt_pk_bf16_f32 v129, v66, v67\n\t" \
    "v_exp_f32 v68, v68\n\t" \
    "v_exp_f32 v69, v69\n\t" \
    "v_exp_f32 v70, v70\n\t" \
    "v_cvt_pk_bf16_f32 v130, v68, v69\n\t" \
    "v_exp_f32 v71, v71\n\t" \
    "v_mfma_f32_32x32x16_bf16 v[112:127], v[168:171], v[136:139], v[112:127]\n\t" \
    "s_sub_u32 %[cnt], %[cnt], 1\n\t" \
    "v_cvt_pk_bf16_f32 v131, v70, v71\n\t" \
    "v_exp_f32 v72, v72\n\t" \
    "v_exp_f32 v73, v73\n\t" \
    "v_exp_f32 v74, v74\n\t" \
    "v_cvt_pk_bf16_f32 v132, v72, v73\n\t" \
    "v_exp_f32 v75, v75\n\t" \
    "v_mfma_f32_32x32x16_bf16 v[112:127], v[172:175], v[140:143], v[112:127]\n\t" \
    "s_waitcnt lgkmcnt(0)\n\t" \
    "s_barrier\n\t" \
    "v_cvt_pk_bf16_f32 v133, v74, v75\n\t" \
    "v_exp_f32 v76, v76\n\t" \
    "v_exp_f32 v77, v77\n\t" \
    "v_exp_f32 v78, v78\n\t" \
    "v_exp_f32 v79, v79\n\t" \
    "v_cvt_pk_bf16_f32 v134, v76, v77\n\t" \
    "v_cvt_pk_bf16_f32 v135, v78, v79\n\t" \
    "v_xor_b32 %[va], 0x4000, %[va]\n\t" \
    "v_xor_b32 %[vb], 0x4000, %[vb]\n\t" \
    "s_cbranch_scc0 1b\n\t" \
    "v_exp_f32 v80, v80\n\t" \
    "v_exp_f32 v81, v81\n\t" \
    "v_exp_f32 v82, v82\n\t" \
    "v_cvt_pk_bf16_f32 v136, v80, v81\n\t" \
    "v_exp_f32 v83, v83\n\t" \
    "s_nop 0\n\t" \
    "v_cvt_pk_bf16_f32 v137, v82, v83\n\t" \
    "v_exp_f32 v84, v84\n\t" \
    "v_exp_f32 v85, v85\n\t" \
    "v_exp_f32 v86, v86\n\t" \
    "v_cvt_pk_bf16_f32 v138, v84, v85\n\t" \
    "v_exp_f32 v87, v87\n\t" \
    "v_mfma_f32_32x32x16_bf16 v[96:111], v[176:179], v[128:131], v[96:111]\n\t" \
    "v_cvt_pk_bf16_f32 v139, v86, v87\n\t" \
    "v_exp_f32 v88, v88\n\t" \
    "v_exp_f32 v89, v89\n\t" \
    "v_exp_f32 v90, v90\n\t" \
    "v_cvt_pk_bf16_f32 v140, v88, v89\n\t" \
    "v_exp_f32 v91, v91\n\t" \
    "v_mfma_f32_32x32x16_bf16 v[96:111], v[180:183], v[132:135], v[96:111]\n\t" \
    "v_cvt_pk_bf16_f32 v141, v90, v91\n\t" \
    "v_exp_f32 v92, v92\n\t" \
    "v_exp_f32 v93, v93\n\t" \
    "v_exp_f32 v94, v94\n\t" \
    "v_exp_f32 v95, v95\n\t" \
    "v_cvt_pk_bf16_f32 v142, v92, v93\n\t" \
    "v_cvt_pk_bf16_f32 v143, v94, v95\n\t" \
    "s_nop 1\n\t" \
    "v_mfma_f32_32x32x16_bf16 v[112:127], v[176:179], v[136:139], v[112:127]\n\t" \
    "v_mfma_f32_32x32x16_bf16 v[112:127], v[180:183], v[140:143], v[112:127]\n\t" \
    "s_nop 15\n\t"

#define WARM1 1
#define WARM2 1
constexpr int L_WO = 40960, L_X = 143360, M_LDS = 163840;
constexpr int KV_SLOT = 16384, KV_LREC = 3328;

DI int up_start(int c) { return 27 * (c >> 2) + 7 * (c & 3); }

DI void attn_slow(const char* kv, int lane, u32x4 q16, u32x2 q8, f32x16& o) {
    const int hi = lane >> 5, l31 = lane & 31;
    const unsigned fill = l31 == 20 ? 0x3f803f80u : 0u;
    o = f32x16{};
    float m = -3.0e38f;
#pragma unroll 1
    for (int t = 0; t < 64; ++t) {
        const char* r = kv + t * KVREC;
        const u32x4 k16 = *(const u32x4*)(r + lane * 16);
        const u32x2 k8 = *(const u32x2*)(r + 1024 + l31 * 8);
        u32x4 v0 = {fill, fill, fill, fill}, v1 = v0;
        if (l31 < 20) { v0 = *(const u32x4*)(r + 1280 + hi * 320 + l31 * 16); v1 = *(const u32x4*)(r + 1920 + hi * 320 + l31 * 16); }
        f32x16 s = mfma32k8(k8, q8, mfma32(k16, q16, f32x16{}));
        float mx = s[0];
#pragma unroll
        for (int i = 1; i < 16; ++i) mx = fmaxf(mx, s[i]);
        const float mn = fmaxf(m, swap32_max(mx));
        const float al = __builtin_amdgcn_exp2f(m - mn);
        m = mn;
        u32x4 p0, p1;
#pragma unroll
        for (int i = 0; i < 4; ++i) {
            p0[i] = pk2(__builtin_amdgcn_exp2f(s[2 * i] - mn), __builtin_amdgcn_exp2f(s[2 * i + 1] - mn));
            p1[i] = pk2(__builtin_amdgcn_exp2f(s[8 + 2 * i] - mn), __builtin_amdgcn_exp2f(s[9 + 2 * i] - mn));
        }
#pragma unroll
        for (int i = 0; i < 16; ++i) o[i] *= al;
        o = mfma32(v1, p1, mfma32(v0, p0, o));
    }
}

constexpr float GELU_C0 = -2.0f * 0.7978845608028654f * 1.4426950408889634f, GELU_C1 = 0.044715f * GELU_C0;

typedef __attribute__((address_space(1))) unsigned gu32;
typedef __attribute__((address_space(1))) unsigned long long gu64;
typedef unsigned long long u64;
DI void flag_set(char* ws, int off, u64 tag) { __hip_atomic_store((gu64*)(ws + off), tag, __ATOMIC_RELAXED, __HIP_MEMORY_SCOPE_AGENT); }
DI u64 flag_get(char* ws, int off) { return __hip_atomic_load((gu64*)(ws + off), __ATOMIC_RELAXED, __HIP_MEMORY_SCOPE_AGENT); }
DI void flag_wait(char* ws, int off, u64 tag) {
#pragma unroll 1
    for (int spin = 0; spin < (1 << 20); ++spin) {
        if (__all(flag_get(ws, off) == tag)) break;
        __builtin_amdgcn_s_sleep(2);
    }
}
DI void flag_wait2(char* ws, int off0, int off1, u64 tag) {
#pragma unroll 1
    for (int spin = 0; spin < (1 << 20); ++spin) {
        const u64 u = flag_get(ws, off0), v = flag_get(ws, off1);
        if (__all(u == tag && v == tag)) break;
        __builtin_amdgcn_s_sleep(2);
    }
}

constexpr int nld(int k, int first2, int per) { return k >= first2 ? 2 * per : per; }
constexpr int nld_o(int k) { return k % 5 == 4 ? 2 : 1; }

DI void attn_consumer(char* ws, lptr lds, int b, int w, int lane, u64 tag STARG) {
    const int hi = lane >> 5, l31 = lane & 31, tk = lane & 15;
    const unsigned vl = lane * 16;
    const int head = 4 * (b & 7) + ((b >> 3) & 3), tta = 8 * (b >> 5) + 2 * w;
    const char* qa = ws + WS_Q + (size_t)(head * 64 + tta) * QREC;
    const char* kv = ws + WS_KV + (size_t)head * 64 * KVREC;
    const unsigned v8 = 1024 + l31 * 8;
    u32x4 QA16, QB16, K16a, V0a = {}, V1a = {}, V0b = {}, V1b = {};
    u32x2 QA8, QB8, K8a;
    f32x16 SB, OA = {}, OB = {};
    u32x8 PA = {}, PB = {};
#pragma unroll
    for (int i = 0; i < 16; ++i) SB[i] = -1.0e30f;
    asm volatile("" : "+{v[80:95]}"(SB), "+{v[96:111]}"(OA), "+{v[112:127]}"(OB), "+{v[128:135]}"(PA), "+{v[136:143]}"(PB),
                      "+{v[168:171]}"(V0a), "+{v[172:175]}"(V1a), "+{v[176:179]}"(V0b), "+{v[180:183]}"(V1b));
    if (w == 0) {
        const int ca = (5 * head) >> 5, cb = (5 * head + 4) >> 5, cs = (lane & 16) ? cb : ca;
        flag_wait2(ws, FL_A + ((5 + 5 * hi + cs) * 16 + tk) * 128, FL_A + (cs * 16 + 2 * (b >> 5) + (lane & 1)) * 128, tag);
    }
    ST(5);
    lds_bar();
    ST(6);
    gld128s<0>(QA16, vl, qa); gld64s<0>(QA8, v8, qa);
    gld128s<QREC>(QB16, vl, qa); gld64s<QREC>(QB8, v8, qa);
    gld128s<0>(K16a, vl, kv); gld64s<0>(K8a, v8, kv);
    asm volatile("s_waitcnt vmcnt(0)" : "+v"(QA16), "+v"(QA8), "+v"(QB16), "+v"(QB8), "+v"(K16a), "+v"(K8a)::"memory");
    ST(7);
    if (hi) { QA8 = u32x2{0u, 0u}; QB8 = u32x2{0u, 0u}; }
    f32x16 SA = mfma32k8(K8a, QA8, mfma32(K16a, QA16, f32x16{}));
    f32x16 ST0 = mfma32k8(K8a, QB8, mfma32(K16a, QB16, f32x16{}));
    float ma = SA[0], mb = ST0[0];
#pragma unroll
    for (int i = 1; i < 16; ++i) { ma = fmaxf(ma, SA[i]); mb = fmaxf(mb, ST0[i]); }
    ma = swap32_max(ma); mb = swap32_max(mb);
    u32x2 RA8 = QA8, RB8 = QB8;
    if (hi) { RA8[0] = pk2(-ma, rbf(ma) - ma); RB8[0] = pk2(-mb, rbf(mb) - mb); K8a = u32x2{0x3f803f80u, 0x3f803f80u}; }
    unsigned va = (unsigned)(size_t)lds + vl, vb = (unsigned)(size_t)lds + (hi ? 576 : l31 * 8), cnt = 15;
    lds_bar();
    ST(8);
    asm volatile(ATTN_LOOP_ASM
                 : "+{v[64:79]}"(SA), "+{v[80:95]}"(SB), "+{v[96:111]}"(OA), "+{v[112:127]}"(OB), "+{v[128:135]}"(PA), "+{v[136:143]}"(PB),
                   "+{v[156:159]}"(K16a), "+{v[164:165]}"(K8a), "+{v[168:171]}"(V0a), "+{v[172:175]}"(V1a), "+{v[176:179]}"(V0b), "+{v[180:183]}"(V1b),
                   [va] "+v"(va), [vb] "+v"(vb), [cnt] "+s"(cnt)
                 : "{v[144:147]}"(QA16), "{v[148:151]}"(QB16), "{v[152:153]}"(RA8), "{v[154:155]}"(RB8)
                 : "memory", "scc", "v160", "v161", "v162", "v163", "v166", "v167");
    ST(9);
    bool ok = !hi || (OA[8] <= 1.8e19f && OB[8] <= 1.8e19f && OA[8] >= 1.0e-9f && OB[8] >= 1.0e-9f);
#pragma unroll
    for (int i = 0; i < 12; ++i) ok = ok && fabsf(OA[i]) <= 3.0e38f && fabsf(OB[i]) <= 3.0e38f;
    if (__builtin_expect(__any(!ok), 0)) {
        __builtin_amdgcn_fence(__ATOMIC_ACQUIRE, "agent");
        attn_slow(kv, lane, QA16, QA8, OA);
        attn_slow(kv, lane, QB16, QB8, OB);
    }
    const int u = 2 * (head & 7) + hi;
    const unsigned vo = (l31 >> 4) * 20480 + (4 * (head >> 3) + (u >> 2)) * 1024 + ((u & 3) * 16 + tk) * 16;
    const unsigned vs = (l31 >> 4) * 20480 + 16384 + head * 128 + tk * 8;
    sfor<0, 2>([&](auto T) {
        const f32x16& O = T.value == 0 ? OA : OB;
        auto r = __builtin_amdgcn_permlane32_swap(__float_as_uint(O[8]), __float_as_uint(O[8]), false, false);
        const float inv = __builtin_amdgcn_rcpf(__uint_as_float(r[1]));
        const char* ob = ws + WS_O + (size_t)(2 * tta + 2 * T.value) * 20480;
        gst128(vo, ob, u32x4{pk2(O[0] * inv, O[1] * inv), pk2(O[2] * inv, O[3] * inv), pk2(O[4] * inv, O[5] * inv), pk2(O[6] * inv, O[7] * inv)});
        if (hi == 0) gst64(vs, ob, u32x2{pk2(O[8] * inv, O[9] * inv), pk2(O[10] * inv, O[11] * inv)});
    });
}

#define MF16(d, a, b) "v_mfma_f32_16x16x32_bf16 %[" #d "], %[" #a "], %[" #b "], %[" #d "]\n\t"
#define DSR(d, p, o) "ds_read_b128 %[" #d "], %[" #p "] offset:%c[off]+" #o "\n\t"
template <int OFF, int VM>
DI void step5(f32x4 (&acc)[5], const u32x4 (&c)[5], u32x4 (&n)[5], u32x4 a, unsigned pa, unsigned pb, unsigned pc) {
    asm volatile("s_waitcnt vmcnt(%c[vm]) lgkmcnt(2)\n\t"
                 MF16(d0, c0, a) DSR(n0, pa, 0) DSR(n1, pa, 1024) MF16(d1, c1, a) DSR(n2, pb, 0) DSR(n3, pb, 1024) MF16(d2, c2, a) DSR(n4, pc, 0)
                 "s_waitcnt lgkmcnt(5)\n\t" MF16(d3, c3, a) MF16(d4, c4, a)
                 : [d0] "+v"(acc[0]), [d1] "+v"(acc[1]), [d2] "+v"(acc[2]), [d3] "+v"(acc[3]), [d4] "+v"(acc[4]),
                   [n0] "=&v"(n[0]), [n1] "=&v"(n[1]), [n2] "=&v"(n[2]), [n3] "=&v"(n[3]), [n4] "=&v"(n[4])
                 : [c0] "v"(c[0]), [c1] "v"(c[1]), [c2] "v"(c[2]), [c3] "v"(c[3]), [c4] "v"(c[4]), [a] "v"(a),
                   [pa] "v"(pa), [pb] "v"(pb), [pc] "v"(pc), [off] "i"(OFF), [vm] "i"(VM)
                 : "memory");
}
template <int VM>
DI void last5(f32x4 (&acc)[5], const u32x4 (&c)[5], u32x4 a) {
    asm volatile("s_waitcnt vmcnt(%c[vm]) lgkmcnt(0)\n\t" MF16(d0, c0, a) MF16(d1, c1, a) MF16(d2, c2, a) MF16(d3, c3, a) MF16(d4, c4, a) "s_nop 7"
                 : [d0] "+v"(acc[0]), [d1] "+v"(acc[1]), [d2] "+v"(acc[2]), [d3] "+v"(acc[3]), [d4] "+v"(acc[4])
                 : [c0] "v"(c[0]), [c1] "v"(c[1]), [c2] "v"(c[2]), [c3] "v"(c[3]), [c4] "v"(c[4]), [a] "v"(a), [vm] "i"(VM)
                 : "memory");
}
template <int OFF> DI void first5(u32x4 (&n)[5], unsigned pa, unsigned pb, unsigned pc) {
    asm volatile(DSR(n0, pa, 0) DSR(n1, pa, 1024) DSR(n2, pb, 0) DSR(n3, pb, 1024) DSR(n4, pc, 0)
                 : [n0] "=&v"(n[0]), [n1] "=&v"(n[1]), [n2] "=&v"(n[2]), [n3] "=&v"(n[3]), [n4] "=&v"(n[4])
                 : [pa] "v"(pa), [pb] "v"(pb), [pc] "v"(pc), [off] "i"(OFF) : "memory");
}
template <int OFF, int VM, bool RD>
DI void step7(f32x4 (&acc)[2][7], const u32x4 (&c)[7], u32x4 (&n)[7], u32x4 a, u32x4 b, unsigned p) {
    if constexpr (RD) {
        asm volatile("s_waitcnt vmcnt(%c[vm]) lgkmcnt(3)\n\t"
                     MF16(d0, c0, a) DSR(n0, p, 0) MF16(e0, c0, b) MF16(d1, c1, a) DSR(n1, p, 1024) MF16(e1, c1, b)
                     MF16(d2, c2, a) DSR(n2, p, 2048) MF16(e2, c2, b) MF16(d3, c3, a) DSR(n3, p, 3072) MF16(e3, c3, b)
                     : [d0] "+v"(acc[0][0]), [d1] "+v"(acc[0][1]), [d2] "+v"(acc[0][2]), [d3] "+v"(acc[0][3]),
                       [e0] "+v"(acc[1][0]), [e1] "+v"(acc[1][1]), [e2] "+v"(acc[1][2]), [e3] "+v"(acc[1][3]),
                       [n0] "=&v"(n[0]), [n1] "=&v"(n[1]), [n2] "=&v"(n[2]), [n3] "=&v"(n[3])
                     : [c0] "v"(c[0]), [c1] "v"(c[1]), [c2] "v"(c[2]), [c3] "v"(c[3]), [a] "v"(a), [b] "v"(b), [p] "v"(p), [off] "i"(OFF), [vm] "i"(VM)
                     : "memory");
        asm volatile("s_waitcnt lgkmcnt(4)\n\t"
                     MF16(d0, c0, a) DSR(n0, p, 4096) MF16(e0, c0, b) MF16(d1, c1, a) DSR(n1, p, 5120) MF16(e1, c1, b)
                     MF16(d2, c2, a) DSR(n2, p, 6144) MF16(e2, c2, b)
                     : [d0] "+v"(acc[0][4]), [d1] "+v"(acc[0][5]), [d2] "+v"(acc[0][6]), [e0] "+v"(acc[1][4]), [e1] "+v"(acc[1][5]), [e2] "+v"(acc[1][6]),
                       [n0] "=&v"(n[4]), [n1] "=&v"(n[5]), [n2] "=&v"(n[6])
                     : [c0] "v"(c[4]), [c1] "v"(c[5]), [c2] "v"(c[6]), [a] "v"(a), [b] "v"(b), [p] "v"(p), [off] "i"(OFF)
                     : "memory");
    } else {
        asm volatile("s_waitcnt vmcnt(%c[vm]) lgkmcnt(0)\n\t"
                     MF16(d0, c0, a) MF16(e0, c0, b) MF16(d1, c1, a) MF16(e1, c1, b) MF16(d2, c2, a) MF16(e2, c2, b) MF16(d3, c3, a) MF16(e3, c3, b)
                     : [d0] "+v"(acc[0][0]), [d1] "+v"(acc[0][1]), [d2] "+v"(acc[0][2]), [d3] "+v"(acc[0][3]),
                       [e0] "+v"(acc[1][0]), [e1] "+v"(acc[1][1]), [e2] "+v"(acc[1][2]), [e3] "+v"(acc[1][3])
                     : [c0] "v"(c[0]), [c1] "v"(c[1]), [c2] "v"(c[2]), [c3] "v"(c[3]), [a] "v"(a), [b] "v"(b), [vm] "i"(VM)
                     : "memory");
        asm volatile(MF16(d0, c0, a) MF16(e0, c0, b) MF16(d1, c1, a) MF16(e1, c1, b) MF16(d2, c2, a) MF16(e2, c2, b) "s_nop 7"
                     : [d0] "+v"(acc[0][4]), [d1] "+v"(acc[0][5]), [d2] "+v"(acc[0][6]), [e0] "+v"(acc[1][4]), [e1] "+v"(acc[1][5]), [e2] "+v"(acc[1][6])
                     : [c0] "v"(c[4]), [c1] "v"(c[5]), [c2] "v"(c[6]), [a] "v"(a), [b] "v"(b)
                     : "memory");
    }
}
template <int VM> DI u32x4 join2(u32x2& p, u32x2& q) {
    asm volatile("s_waitcnt vmcnt(%c2)" : "+v"(p), "+v"(q) : "i"(VM) : "memory");
    u32x4 a = {p[0], p[1], q[0], q[1]};
    asm volatile("s_nop 0" : "+v"(a));
    return a;
}

constexpr int L_CTR = L_X + 1024, L_ST = L_X + 2048;
#define H2_AT 2
#define UP_P 5
#define DN_P 6

DI void swap_halves(lptr lds, int w, int lane, f32x4 (&acc)[5]) {
    const int kh = w >> 2, tw = w & 3;
    const lptr mine = lds + L_X + tw * 5120 + kh * 2048 + lane * 16, other = lds + L_X + tw * 5120 + (1 - kh) * 2048 + lane * 16;
    *(LAS f32x4*)mine = acc[2];
    *(LAS f32x4*)(mine + 1024) = acc[3];
    if (kh) *(LAS f32x4*)(mine + 2048) = acc[4];
    lds_bar();
    acc[0] += *(LAS f32x4*)other;
    acc[1] += *(LAS f32x4*)(other + 1024);
    if (!kh) acc[4] += *(LAS f32x4*)(other + 2048);
}

DI void wo_phase(const u16* x, char* ws, lptr lds, int b, int w, int lane, u32x2 (&hk)[3], u32x4 (&held)[33], u64 tag STARG) {
    const int kh = w >> 2, tk = lane & 15, g = lane >> 4, cg = b >> 5;
    const int tt16 = 16 * (b & 7) + 4 * ((b >> 3) & 3) + (w & 3);
    const unsigned vl = lane * 16;
    u32x2 xr[3];
    const unsigned vxr = (16 * tt16 + tk) * 1280 + 8 * g;
    gld64<0>(xr[0], vxr, (const char*)x + cg * 160 + kh * 64); gld64<32>(xr[1], vxr, (const char*)x + cg * 160 + kh * 64);
    gld64<128>(xr[2], vxr, (const char*)x + cg * 160);
    ST(11);
    lds_bar();
    ST(12);
    const char* osrc = ws + WS_O + (size_t)tt16 * 20480;
    const unsigned vs = g * 256 + tk * 8;
    constexpr int P = 5;
    u32x4 of[8]; u32x2 os[2][2];
    auto ld = [&](auto K_) {
        constexpr int K = K_.value, G = K / 5;
        if constexpr (K % 5 < 4) gld128s<(K % 5) * 1024>(of[K - G], vl, osrc + (2 * kh + G) * 4096);
        else { gld64s<G * 1024>(os[G][0], vs, osrc + 16384 + kh * 2048); gld64s<G * 1024 + 128>(os[G][1], vs, osrc + 16384 + kh * 2048); }
    };
    sfor<0, P>(ld);
    const unsigned wb = (unsigned)(size_t)lds + L_WO + kh * 51200 + vl, pa = wb + kh * 2048, pb = wb + (1 - kh) * 2048, pc = wb + 4096;
    f32x4 acc[5] = {};
    u32x4 wf[2][5];
    sfor<0, 5>([&](auto I) { pin(acc[I.value]); });
    first5<0>(wf[0], pa, pb, pc);
    sfor<0, 10>([&](auto K_) {
        constexpr int K = K_.value;
        constexpr int N = [] { int n = 0; for (int j = K + 1; j < K + P && j < 10; ++j) n += nld_o(j); return n; }();
        u32x4 a;
        if constexpr (K % 5 < 4) a = of[K - K / 5]; else a = join2<N>(os[K / 5][0], os[K / 5][1]);
        if constexpr (K < 9) step5<(K + 1) * 5120, N>(acc, wf[K & 1], wf[(K + 1) & 1], a, pa, pb, pc);
        else last5<N>(acc, wf[K & 1], a);
        if constexpr (K + P < 10) ld(IC<K + P>{});
    });
    ST(13);
    swap_halves(lds, w, lane, acc);
    float s1 = 0.f, s2 = 0.f;
#pragma unroll
    for (int i = 0; i < 3; ++i) {
        pin(xr[i]);
        const f32x4 v = acc[i == 2 ? 4 : i];
        const unsigned a0 = pk2(v[0], v[1]), a1 = pk2(v[2], v[3]), x0 = xr[i][0], x1 = xr[i][1];
        unsigned h0 = pk2(bflo(x0) + bflo(a0), bfhi(x0) + bfhi(a0)), h1 = pk2(bflo(x1) + bflo(a1), bfhi(x1) + bfhi(a1));
        if (i == 2 && kh) { h0 = 0u; h1 = 0u; }
        hk[i] = u32x2{h0, h1};
        s1 = dot2(h0, 0x3f803f80u, dot2(h1, 0x3f803f80u, s1));
        s2 = dot2(h0, h0, dot2(h1, h1, s2));
    }
    const char* hb = ws + WS_H + (size_t)tt16 * 20480;
    gst128(vl, hb + (2 * cg + kh) * 1024, u32x4{hk[0][0], hk[0][1], hk[1][0], hk[1][1]});
    if (!kh) gst64(lane * 8, hb + 16384 + cg * 512, hk[2]);
    s1 = swap32_add(swap16_add(s1)); s2 = swap32_add(swap16_add(s2));
    if (g == 0) gst64(tk * 8, ws + WS_ST + (size_t)(tt16 * 16 + 2 * cg + kh) * 128, u32x2{__float_as_uint(s1), __float_as_uint(s2)});
    if (kh) {
        const lptr d = lds + (7 * (1 + w)) * 1024 + vl;
        sfor<0, 21>([&](auto I) { *(LAS u32x4*)(d + (28 * (I.value / 7) + I.value % 7) * 1024) = held[I.value]; });
        if (w < 7) sfor<21, 28>([&](auto I) { *(LAS u32x4*)(d + (84 + I.value % 7) * 1024) = held[I.value]; });
    }
    ST(14);
    wait_vm_bar<0>();
    if (w == 0) {
        if (lane == 0) flag_set(ws, FL_C + ((2 * (b & 7) + ((b >> 4) & 1)) * 16 + ((b >> 3) & 1) * 8 + cg) * 128, tag);
        flag_wait(ws, FL_C + ((2 * (b & 7) + ((b >> 3) & 1)) * 16 + tk) * 128, tag);
    }
}

DI void up_consumer(char* ws, lptr lds, int b, int w, int lane, u64 tag STARG) {
    const int tk = lane & 15, g = lane >> 4;
    const unsigned vl = lane * 16;
    ST(15);
    lds_bar();
    ST(16);
    const int t0 = 16 * (b & 7) + 8 * ((b >> 3) & 1) + 2 * w, c = b >> 4;
    const char* hsrc = ws + WS_H + (size_t)t0 * 20480;
    constexpr int P = UP_P;
    u32x4 hf[16][2]; u32x2 hs[4][2][2];
    auto ld = [&](auto K_) {
        constexpr int K = K_.value;
        sfor<0, 2>([&](auto T) {
            if constexpr (K < 16) gld128s<(K & 3) * 1024>(hf[K][T.value], vl, hsrc + T.value * 20480 + (K >> 2) * 4096);
            else { gld64s<(K - 16) * 1024>(hs[K - 16][T.value][0], lane * 8, hsrc + T.value * 20480 + 16384); gld64s<(K - 16) * 1024 + 512>(hs[K - 16][T.value][1], lane * 8, hsrc + T.value * 20480 + 16384); }
        });
    };
    sfor<0, P>(ld);
    f32x4 acc[2][7] = {};
    u32x4 wf[2][7];
    const unsigned p0 = (unsigned)(size_t)lds + vl;
    sfor<0, 7>([&](auto I) { wf[0][I.value] = lds128(lds + I.value * 1024 + vl); pin(acc[0][I.value]); pin(acc[1][I.value]); });
    sfor<0, 7>([&](auto I) { pin(wf[0][I.value]); });
    sfor<0, 20>([&](auto K_) {
        constexpr int K = K_.value, NX = K + 1, SEG = NX / 8;
        constexpr int N = [] { int n = 0; for (int j = K + 1; j < K + P && j < 20; ++j) n += nld(j, 16, 2); return n; }();
        u32x4 a0, a1;
        if constexpr (K < 16) { a0 = hf[K][0]; a1 = hf[K][1]; }
        else { a0 = join2<N>(hs[K - 16][0][0], hs[K - 16][0][1]); a1 = join2<N>(hs[K - 16][1][0], hs[K - 16][1][1]); }
        step7<(NX - 8 * SEG) * 7168, N, (K < 19)>(acc, wf[K & 1], wf[NX & 1], a0, a1, p0 + SEG * 57344);
        if constexpr (K + P < 20) ld(IC<K + P>{});
        if constexpr (K == 10 || K == 18) lds_bar();
    });
    ST(17);
    f32x4 cs[7], bv[7];
#pragma unroll
    for (int i = 0; i < 7; ++i) { cs[i] = *(LAS f32x4*)(lds + L_X + (16 * i + 4 * g) * 4); bv[i] = *(LAS f32x4*)(lds + L_X + 512 + (16 * i + 4 * g) * 4); }
    float mr[2], rs[2];
#pragma unroll
    for (int t = 0; t < 2; ++t) { const f32x2 v = *(LAS f32x2*)(lds + L_ST + (32 * w + 16 * t + tk) * 8); mr[t] = v[0]; rs[t] = v[1]; }
    u32x2 o[2][7];
    sfor<0, 7>([&](auto I) {
        constexpr int i = I.value;
        f32x2 a[4], z[4];
#pragma unroll
        for (int q = 0; q < 4; ++q) {
            const int t = q >> 1, e = 2 * (q & 1);
            a[q] = f32x2{acc[t][i][e], acc[t][i][e + 1]} * rs[t] + (f32x2{cs[i][e], cs[i][e + 1]} * mr[t] + f32x2{bv[i][e], bv[i][e + 1]});
        }
#pragma unroll
        for (int q = 0; q < 4; ++q) z[q] = a[q] * (a[q] * a[q] * GELU_C1 + GELU_C0);
#pragma unroll
        for (int q = 0; q < 4; ++q) z[q] = f32x2{__builtin_amdgcn_exp2f(z[q][0]), __builtin_amdgcn_exp2f(z[q][1])};
#pragma unroll
        for (int q = 0; q < 4; ++q) z[q] += 1.0f;
#pragma unroll
        for (int q = 0; q < 4; ++q) z[q] = f32x2{__builtin_amdgcn_rcpf(z[q][0]), __builtin_amdgcn_rcpf(z[q][1])};
#pragma unroll
        for (int q = 0; q < 4; ++q) { a[q] *= z[q]; o[q >> 1][i][q & 1] = pk2(a[q][0], a[q][1]); }
        pin(o[0][i]); pin(o[1][i]);
        SBAR();
        if constexpr (i & 1) sfor<0, 2>([&](auto T) {
            gst128(vl, ws + WS_G + (size_t)((t0 + T.value) * 54 + 3 * c + (i >> 1)) * 1024, u32x4{o[T.value][i - 1][0], o[T.value][i - 1][1], o[T.value][i][0], o[T.value][i][1]});
        });
    });
    if ((c & 3) != 3) sfor<0, 2>([&](auto T) { gst64(lane * 8, ws + WS_G + (size_t)((t0 + T.value) * 54 + 48) * 1024 + (3 * (c >> 2) + (c & 3)) * 512, o[T.value][6]); });
    ST(18);
    wait_vm<0>();
    unsigned old = 0;
    if (lane == 0) old = __hip_atomic_fetch_add((LAS unsigned*)(lds + L_CTR), 1u, __ATOMIC_RELAXED, __HIP_MEMORY_SCOPE_WORKGROUP);
    if (__builtin_amdgcn_readfirstlane(old) == 3u) {
        if (lane == 0) flag_set(ws, FL_D + ((2 * (b & 7) + ((b >> 3) & 1)) * 16 + c) * 128, tag);
        flag_wait(ws, FL_D + ((2 * (b & 7) + ((b >> 4) & 1)) * 16 + tk) * 128, tag);
    }
}

template <int KH>
DI void down_phase(const u16* b2, char* ws, u16* out, lptr lds, int b, int w, int lane, const u32x2 (&hk)[3], u32x4 (&held)[33] STARG) {
    const int tk = lane & 15, g = lane >> 4, cg = b >> 5;
    const int tt16 = 16 * (b & 7) + 4 * ((b >> 3) & 3) + (w & 3);
    const unsigned vl = lane * 16;
    u32x2 bv[3];
    gld64<KH * 64>(bv[0], 8 * g, (const char*)b2 + cg * 160); gld64<KH * 64 + 32>(bv[1], 8 * g, (const char*)b2 + cg * 160); gld64<128>(bv[2], 8 * g, (const char*)b2 + cg * 160);
    ST(19);
    if constexpr (KH) { wait_vm_bar<0>(); sfor<0, 33>([&](auto I) { pin(held[I.value]); }); } else lds_bar();
    ST(20);
    const char* gsrc = ws + WS_G + (size_t)tt16 * 54 * 1024;
    constexpr int P = DN_P;
    u32x4 gf[24]; u32x2 gs[3][2];
    auto ld = [&](auto S_) {
        constexpr int S = S_.value;
        if constexpr (S < 24) gld128s<(S & 3) * 1024>(gf[S], vl, gsrc + (6 * KH + (S >> 2)) * 4096);
        else { gld64s<(S - 24) * 1024>(gs[S - 24][0], lane * 8, gsrc + (48 + 3 * KH) * 1024); gld64s<(S - 24) * 1024 + 512>(gs[S - 24][1], lane * 8, gsrc + (48 + 3 * KH) * 1024); }
    };
    sfor<0, P>(ld);
    f32x4 acc[5] = {};
    u32x4 wf[2][5];
    const unsigned wb = (unsigned)(size_t)lds + KH * 71680 + vl, pa = wb + KH * 2048, pb = wb + (1 - KH) * 2048, pc = wb + 4096;
    sfor<0, 5>([&](auto I) { pin(acc[I.value]); });
    first5<0>(wf[0], pa, pb, pc);
    sfor<0, 27>([&](auto S_) {
        constexpr int S = S_.value, SL = (S + 1) % 14, UP = SL >= 12 ? 61440 : 0;
        constexpr int N = [] { int n = 0; for (int j = S + 1; j < S + P && j < 27; ++j) n += nld(j, 24, 1); return n; }();
        if constexpr (S == 6 || S == 13 || S == 20) lds_bar();
        if constexpr (KH && S == 6) {
            const lptr d = lds + ((w >> 1) & 1) * 71680 + (w & 1) * 17408 + vl;
            sfor<0, 18>([&](auto I) { *(LAS u32x4*)(d + I.value * 1024) = held[I.value]; });
        }
        if constexpr (KH && S == 13) {
            const lptr d = lds + ((w >> 1) & 1) * 71680 + 35840 + (w & 1) * 15360 + vl;
            sfor<0, 15>([&](auto I) { *(LAS u32x4*)(d + I.value * 1024) = held[18 + I.value]; });
        }
        u32x4 a;
        if constexpr (S < 24) a = gf[S]; else a = join2<N>(gs[S - 24][0], gs[S - 24][1]);
        if constexpr (S < 26) step5<SL * 5120 - UP, N>(acc, wf[S & 1], wf[(S + 1) & 1], a, pa + UP, pb + UP, pc + UP);
        else last5<N>(acc, wf[S & 1], a);
        if constexpr (S + P < 27) ld(IC<S + P>{});
    });
    ST(21);
    swap_halves(lds, w, lane, acc);
    char* ob = (char*)out + (size_t)(16 * tt16 + tk) * 1280 + cg * 160 + 8 * g;
#pragma unroll
    for (int i = 0; i < 3 - KH; ++i) {
        pin(bv[i]);
        const f32x4 v = acc[i == 2 ? 4 : i];
        const unsigned h0 = hk[i][0], h1 = hk[i][1];
        const unsigned y0 = pk2(v[0], v[1]), y1 = pk2(v[2], v[3]), b0 = bv[i][0], b1 = bv[i][1];
        const unsigned z0 = pk2(bflo(y0) + bflo(b0), bfhi(y0) + bfhi(b0)), z1 = pk2(bflo(y1) + bflo(b1), bfhi(y1) + bfhi(b1));
        *(u32x2*)(ob + (i == 2 ? 128 : KH * 64 + 32 * i)) = u32x2{pk2(bflo(h0) + bflo(z0), bfhi(h0) + bfhi(z0)), pk2(bflo(h1) + bflo(z1), bfhi(h1) + bfhi(z1))};
    }
}

DI void glds_run(unsigned vl, const char* src, int sstep, unsigned dst, int dstep, int n) {
#pragma unroll 1
    for (int i = 0; i < n; ++i) { glds_s(vl, src, dst); src += sstep; dst += dstep; }
}

constexpr bool kv_live(int i) { const int e = i % 208; return e < 80 || ((e - 80) & 31) < 20; }
constexpr unsigned kv_mask(int piece, int half) { unsigned m = 0; for (int l = 0; l < 32; ++l) m |= kv_live(piece * 64 + half * 32 + l) ? 1u << l : 0u; return m; }

template <int J>
DI void kv_loader(const char* kv, unsigned ldsb, int lane) {
    constexpr int P0 = J == 0 ? 0 : 7, NP = J == 0 ? 7 : 6;
    unsigned vo[NP];
#pragma unroll
    for (int p = 0; p < NP; ++p) {
        const int i = (P0 + p) * 64 + lane, r = i / 208, e = i - 208 * r, v = e - 80;
        vo[p] = r * KVREC + (e < 80 ? KVREC + e * 16 : 1280 + (v >> 5) * 320 + (v & 31) * 16);
    }
    auto issue = [&](int c) {
        const char* src = kv + c * 4 * KVREC;
        const unsigned dst = ldsb + (c & 1) * KV_SLOT;
        sfor<0, NP>([&](auto Pp) { glds_m<(P0 + Pp.value) * 1024, kv_mask(P0 + Pp.value, 0), kv_mask(P0 + Pp.value, 1)>(vo[Pp.value], src, dst); });
    };
    lds_bar();
    issue(0);
#pragma unroll 1
    for (int c = 0; c < 15; ++c) { wait_vm_bar<0>(); issue(c + 1); }
    wait_vm_bar<0>();
    lds_bar();
}

DI void retile_loader(const u16* wo, const u16* w1, const u16* w2, char* ws, lptr lds, int b, int j, int lane, u64 tag) {
    const unsigned ldsb = (unsigned)(size_t)lds, vl = lane * 16;
    const int first = j == 2 ? 0 : 3, nt = j == 2 ? 3 : 2;
    const int kind = b < 108 ? 1 : b < 148 ? 0 : 2;
    const char* src = kind == 0 ? (const char*)wo : kind == 1 ? (const char*)w1 : (const char*)w2;
    const int pitch = kind == 1 ? FF * 2 : DM * 2;
    const int rel = kind == 1 ? b : kind == 0 ? b - 108 : 5 * (b - 148), div = kind == 1 ? 27 : 10, quo = rel / div;
    auto task = [&](int i, int& kap, int& col64) {
        const int c = rel - quo * div + (kind == 2 ? i : 0), wrap = c >= 10 && kind == 2;
        col64 = wrap ? c - 10 : c;
        kap = kind == 2 ? quo + wrap : 5 * quo + i;
    };
    {
        const int l31 = lane & 31;
        const unsigned fill = l31 == 20 ? 0x3f803f80u : 0u;
        if (l31 >= 20) {
#pragma unroll
            for (int i = 0; i < 8; ++i) *(LAS u32x4*)(lds + (j - 2) * KV_SLOT + (i >> 1) * KV_LREC + 1280 + (i & 1) * 1024 + vl) = u32x4{fill, fill, fill, fill};
        }
    }
    {
        const int gl = lane >> 5, q = (lane >> 3) & 3, swz = ((lane & 7) ^ (2 * ((lane >> 4) & 3))) << 4;
#pragma unroll 1
        for (int i = first; i < first + nt; ++i) {
            int kap, col64;
            task(i, kap, col64);
            const int G = kap / 5, r = kap - 5 * G, c3 = kap / 3;
            int k0[2];
#pragma unroll
            for (int t = 0; t < 2; ++t) {
                const int lam = 2 * (kap - 48) + t, l3 = lam / 3;
                const int f1 = kap < 16 ? 5 * (kap >> 1) + 2 * (kap & 1) + t : 10 * (kap - 16) + 5 * t + 4;
                const int f2 = kap < 48 ? up_start(c3) + 2 * (kap - 3 * c3) + t : 27 * l3 + 7 * (lam - 3 * l3) + 6;
                const int r0 = r < 4 ? 160 * G + 40 * r + 8 * t : 160 * G + 20 * t + 16;
                k0[t] = kind == 0 ? r0 : 16 * (kind == 1 ? f1 : f2);
            }
            const int slo = kind == 0 && r == 4 ? 40 : 4, shi = kind == 0 ? (r == 4 ? 80 : 20) : 8;
            const unsigned v0 = (slo * gl + q) * pitch + swz;
            sfor<0, 4>([&](auto J) { glds<L_X + J.value * 1024>(v0, src + col64 * 128 + (k0[J.value >> 1] + (J.value & 1) * shi) * pitch, ldsb + i * 4096); });
        }
    }
    const int g = lane >> 4, q = (lane >> 2) & 3, p = lane & 3;
    lptr tp[4];
#pragma unroll
    for (int jj = 0; jj < 4; ++jj) { tp[jj] = lds + L_X + (4 * g + q) * 128 + 8 * (p & 1) + (((2 * jj + (p >> 1)) ^ (2 * (((4 * g + q) >> 1) & 3))) << 4); pin(tp[jj]); }
    lds_bar(); lds_bar(); lds_bar(); lds_bar();
    wait_vm<0>();
    f32x4 cs[4] = {};
#pragma unroll 1
    for (int i = first; i < first + 3; ++i) {
        if (i < 5) {
            int kap, col64;
            task(i, kap, col64);
            u32x4 f[4];
#pragma unroll
            for (int jj = 0; jj < 4; ++jj) {
                const u32x2 a = ldstr(tp[jj] + i * 4096), c = ldstr(tp[jj] + i * 4096 + 2048);
                f[jj] = u32x4{a[0], a[1], c[0], c[1]};
            }
#pragma unroll
            for (int jj = 0; jj < 4; ++jj) {
                const int ft = 4 * col64 + jj, f5 = ft / 5;
                const size_t piece = (kind == 0 ? WS_WO : kind == 1 ? WS_W1 : WS_W2) +
                                     (size_t)(kind == 1 ? ft * 20 + kap : (f5 * (kind == 0 ? 20 : 54) + kap) * 5 + ft - 5 * f5) * 1024;
                gst128(vl, ws + piece, f[jj]);
            }
            if (kind == 1) {
#pragma unroll
                for (int jj = 0; jj < 4; ++jj) cs[jj] = mfma16(f[jj], u32x4{0x3f803f80u, 0x3f803f80u, 0x3f803f80u, 0x3f803f80u}, cs[jj]);
            }
        }
        lds_bar();
    }
    asm volatile("s_nop 7" : "+v"(cs[0]), "+v"(cs[1]), "+v"(cs[2]), "+v"(cs[3]));
    if (kind == 1 && (lane & 15) == 0) {
#pragma unroll
        for (int jj = 0; jj < 4; ++jj)
            gst128(g * 16 + jj * 64, ws + WS_CS + (size_t)((2 * (b / 27) + j - 2) * FF + 64 * (b % 27)) * 4,
                   u32x4{__float_as_uint(cs[jj][0]), __float_as_uint(cs[jj][1]), __float_as_uint(cs[jj][2]), __float_as_uint(cs[jj][3])});
    }
    wait_vm_bar<0>();
    if (j == 3 && lane == 0) flag_set(ws, FL_R + b * 8, tag);
    lds_bar(); lds_bar();
    if (j == 2) {
        flag_wait2(ws, FL_R + lane * 8, FL_R + 512 + lane * 8, tag);
        flag_wait2(ws, FL_R + 1024 + lane * 8, FL_R + 1536 + lane * 8, tag);
        if (b == 0 && lane == 0) flag_set(ws, FL_E, tag);
    }
    lds_bar();
    const char* wsrc = ws + WS_WO + (size_t)(b >> 5) * 102400 + (j - 2) * 51200;
#pragma unroll 1
    for (int i = 0; i < 5; ++i) {
        glds_run(vl, wsrc + i * 10240, 1024, ldsb + L_WO + (j - 2) * 51200 + i * 10240, 1024, 10);
        const int ch = (b >> 3) + 32 * (2 * i + j - 2);
        if (WARM1 && ch < 270) warm64(lane * 128, ws + WS_W1 + (size_t)ch * 8192, ldsb + L_X + 4096);
        lds_bar();
    }
    lds_bar(); lds_bar();
}

__global__ __launch_bounds__(512, 2) void k_block(const u16* x, const u16* wqkv, const u16* wo, const u16* w1, const u16* b1, const u16* w2, const u16* b2, char* ws, u16* out) {
    extern __shared__ __attribute__((aligned(16))) char smem[];
    const lptr lds = (lptr)smem;
    const unsigned ldsb = (unsigned)(size_t)lds;
    const int b = blockIdx.x, w = __builtin_amdgcn_readfirstlane(threadIdx.x >> 6), lane = threadIdx.x & 63, j = w - 4;
    const unsigned vl = lane * 16;
    const int head = 4 * (b & 7) + ((b >> 3) & 3), c = b >> 4, ft0 = up_start(c);
    STDECL; STR(0);
    u32x2 seed;
    gld64s<0>(seed, 0, ws + FL_E);
    if (w == 7) {
        unsigned long long pc;
        asm volatile("s_getpc_b64 %0" : "=s"(pc));
        const int ch = (b >> 3) & 7;
        if (ch < 5) warm64(lane * 128, (const char*)(pc & ~255ull) + ch * 8192, ldsb + L_X + 4096);
    }
    const int tbq = 4 * ((b & 7) >> 1) + ((b >> 3) & 3), cgq = (b & 1) * 8 + (b >> 5);
    if (cgq < 15) qkv_phase(x, wqkv, ws, lds, tbq, cgq, w, lane STPASS);
    ST(4);
    wait_vm_bar<0>();
    pin(seed);
    const u64 tag = (((u64)(unsigned)__builtin_amdgcn_readfirstlane(seed[1]) << 32) | (u64)(unsigned)__builtin_amdgcn_readfirstlane(seed[0])) * 6364136223846793005ull + 1442695040888963407ull;
    if (cgq < 15 && threadIdx.x == 0) flag_set(ws, FL_A + (cgq * 16 + tbq) * 128, tag);
    auto w1_part = [&](int k0, int n) {
#pragma unroll 1
        for (int i = j; i < 7; i += 4)
            glds_run(vl, ws + WS_W1 + (size_t)((ft0 + (i == 6 && (c & 3) == 3 ? 5 : i)) * 20 + k0) * 1024, 1024, ldsb + (7 * k0 + i) * 1024, 7168, n);
    };
    float tcs = 0.f, tb1 = 0.f;
    u32x4 held[33];
    if (w < 4) {
        attn_consumer(ws, lds, b, w, lane, tag STPASS);
        ST(10);
        wait_vm_bar<0>();
        if (w == 0) {
            if (lane == 0) flag_set(ws, FL_B + ((b >> 5) * 32 + head) * 128, tag);
            flag_wait(ws, FL_B + ((b & 7) * 32 + (lane & 31)) * 128, tag);
        }
    } else {
        __builtin_amdgcn_s_setprio(3);
        const char* kv = ws + WS_KV + (size_t)head * 64 * KVREC;
        if (j == 0) kv_loader<0>(kv, ldsb, lane);
        else if (j == 1) kv_loader<1>(kv, ldsb, lane);
        else retile_loader(wo, w1, w2, ws, lds, b, j, lane, tag);
        __builtin_amdgcn_s_setprio(0);
        wait_vm<0>();
        w1_part(0, 5);
        sfor<0, 28>([&](auto I) {
            constexpr int m = I.value / 7, i = I.value % 7;
            if (m < 3 || j < 3) gld128s<0>(held[I.value], vl, ws + WS_W1 + (size_t)((ft0 + (i == 6 && (c & 3) == 3 ? 5 : i)) * 20 + 5 + j + 4 * m) * 1024);
        });
        lds_bar();
        if (j >= 2) {
            const int n = min(16 * ft0 + 64 * (j - 2) + lane, FF - 1);
#pragma unroll
            for (int i = 0; i < 8; ++i) tcs += __uint_as_float(__hip_atomic_load((gu32*)(ws + WS_CS + (size_t)(i * FF + n) * 4), __ATOMIC_RELAXED, __HIP_MEMORY_SCOPE_AGENT));
            tb1 = bflo(b1[n]);
            pin(tcs); pin(tb1);
        }
        wait_vm<0>();
        sfor<0, 28>([&](auto I) { pin(held[I.value]); });
    }
    u32x2 hk[3];
    wo_phase(x, ws, lds, b, w, lane, hk, held, tag STPASS);
    if (w < 4) {
        up_consumer(ws, lds, b, w, lane, tag STPASS);
        down_phase<0>(b2, ws, out, lds, b, w, lane, hk, held STPASS);
        ST(22); STWAIT; STR(23); STFLUSH(0);
    } else {
        const char* w2s = ws + WS_W2 + (size_t)(b >> 5) * 276480;
        auto hold_w2 = [&] {
            const char* ha = w2s + (j >> 1) * 122880 + 71680 + (j & 1) * 17408;
            sfor<0, 18>([&](auto I) { gld128s<(I.value & 3) * 1024>(held[I.value], vl, ha + (I.value >> 2) * 4096); });
            const char* hb = w2s + ((j & 1) ? 245760 + (j >> 1) * 15360 : (j >> 1) * 122880 + 107520);
            sfor<0, 15>([&](auto I) { gld128s<(I.value & 3) * 1024>(held[18 + I.value], vl, hb + (I.value >> 2) * 4096); });
        };
        lds_bar();
        {
            u32x2 st[2][4];
            const int t0 = 16 * (b & 7) + 8 * ((b >> 3) & 1) + 2 * j;
            sfor<0, 8>([&](auto I) { gld64s<(I.value >> 2) * 2048 + (I.value & 3) * 128>(st[I.value >> 2][I.value & 3], (lane >> 4) * 512 + (lane & 15) * 8, ws + WS_ST + (size_t)t0 * 2048); });
            wait_vm<0>();
            sfor<0, 2>([&](auto T) {
                float s1 = 0.f, s2 = 0.f;
                sfor<0, 4>([&](auto I) { pin(st[T.value][I.value]); const unsigned u0 = st[T.value][I.value][0], u1 = st[T.value][I.value][1]; s1 += __uint_as_float(u0); s2 += __uint_as_float(u1); });
                float m, rs;
                ln_stats(swap32_add(swap16_add(s1)), swap32_add(swap16_add(s2)), m, rs);
                if (lane < 16) *(LAS f32x2*)(lds + L_ST + (32 * j + 16 * T.value + lane) * 8) = f32x2{-m * rs, rs};
            });
        }
        if (j >= 2) { ((LAS float*)(lds + L_X))[64 * (j - 2) + lane] = tcs; ((LAS float*)(lds + L_X))[128 + 64 * (j - 2) + lane] = tb1; }
        if (j == 0 && lane == 0) *(LAS unsigned*)(lds + L_CTR) = 0u;
        lds_bar();
        glds_run(vl, w2s + j * 17408, 1024, ldsb + j * 17408, 1024, j == 3 ? 19 : 17);
        if (j < 2) glds_run(vl, w2s + 122880 + j * 5120, 1024, ldsb + 71680 + j * 5120, 1024, 5);
        lds_bar();
        glds_run(vl, w2s + 133120 + j * 15360, 1024, ldsb + 81920 + j * 15360, 1024, 15);
        hold_w2();
        down_phase<1>(b2, ws, out, lds, b, w, lane, hk, held STPASS);
        ST(22); STWAIT; STR(23); STFLUSH(0);
    }
}

extern "C" void kernel_launch(void* const* d_in, const int* in_sizes, int n_in, void* d_out, int out_size, void* d_ws, size_t ws_size, hipStream_t stream) {
    (void)hipFuncSetAttribute((const void*)k_block, hipFuncAttributeMaxDynamicSharedMemorySize, M_LDS);
    k_block<<<256, 512, M_LDS, stream>>>((const u16*)d_in[0], (const u16*)d_in[1], (const u16*)d_in[2], (const u16*)d_in[4], (const u16*)d_in[5],
                                         (const u16*)d_in[6], (const u16*)d_in[7], (char*)d_ws, (u16*)d_out);
}
```

```cpp
#define DIAG 2
#include <hip/hip_runtime.h>
#include <hip/hip_bf16.h>
#include <stdint.h>

typedef unsigned short u16;
typedef short bf16x8 __attribute__((ext_vector_type(8)));
typedef short s16x4 __attribute__((ext_vector_type(4)));
typedef float f32x16 __attribute__((ext_vector_type(16)));
typedef float f32x8 __attribute__((ext_vector_type(8)));
typedef float f32x4 __attribute__((ext_vector_type(4)));
typedef float f32x2 __attribute__((ext_vector_type(2)));
typedef unsigned u32x8 __attribute__((ext_vector_type(8)));
typedef unsigned u32x4 __attribute__((ext_vector_type(4)));
typedef unsigned u32x2 __attribute__((ext_vector_type(2)));
typedef __bf16 bf2 __attribute__((ext_vector_type(2)));
#define LAS __attribute__((address_space(3)))
#define DI __device__ __forceinline__
typedef LAS char* lptr;

constexpr int SEQ = 2048, DM = 640, NH = 32, FF = 1728;
constexpr int QREC = 1280, KVREC = 2560;

constexpr int FL_E = 0, FL_R = 2048, FL_A = 4096, FL_B = FL_A + 256 * 128, FL_C = FL_B + 256 * 128, FL_D = FL_C + 256 * 128;
constexpr size_t WS_Q = 262144;
constexpr size_t WS_KV = WS_Q + (size_t)NH * 64 * QREC;
constexpr size_t WS_O = WS_KV + (size_t)NH * 64 * KVREC + 4096;
constexpr size_t WS_H = WS_O + (size_t)128 * 20 * 1024;
constexpr size_t WS_G = WS_H + (size_t)128 * 20 * 1024;
constexpr size_t WS_WO = WS_G + (size_t)128 * 54 * 1024;
constexpr size_t WS_W1 = WS_WO + (size_t)8 * 20 * 5 * 1024;
constexpr size_t WS_W2 = WS_W1 + (size_t)108 * 20 * 1024;
constexpr size_t WS_ST = WS_W2 + (size_t)8 * 54 * 5 * 1024;
constexpr size_t WS_CS = WS_ST + (size_t)SEQ * 16 * 8;
constexpr size_t WS_END = WS_CS + (size_t)8 * FF * 4;

template <int V> using IC = std::integral_constant<int, V>;
#define SBAR() __builtin_amdgcn_sched_barrier(0)
template <int I, int N, class F> DI void sfor(F&& f) {
    if constexpr (I < N) { f(std::integral_constant<int, I>{}); sfor<I + 1, N>(f); }
}

DI unsigned pk2(float lo, float hi) { f32x2 v = {lo, hi}; bf2 b = __builtin_convertvector(v, bf2); return __builtin_bit_cast(unsigned, b); }
DI float bflo(unsigned u) { return __uint_as_float(u << 16); }
DI float bfhi(unsigned u) { return __uint_as_float(u & 0xffff0000u); }
DI float rbf(float x) { return bflo(pk2(x, x)); }
DI float dot2(unsigned a, unsigned b, float c) { return __builtin_amdgcn_fdot2_f32_bf16(__builtin_bit_cast(bf2, a), __builtin_bit_cast(bf2, b), c, false); }
DI float swap32_add(float v) { auto r = __builtin_amdgcn_permlane32_swap(__float_as_uint(v), __float_as_uint(v), false, false); return __uint_as_float(r[0]) + __uint_as_float(r[1]); }
DI float swap32_max(float v) { auto r = __builtin_amdgcn_permlane32_swap(__float_as_uint(v), __float_as_uint(v), false, false); return fmaxf(__uint_as_float(r[0]), __uint_as_float(r[1])); }
DI float swap16_add(float v) { auto r = __builtin_amdgcn_permlane16_swap(__float_as_uint(v), __float_as_uint(v), false, false); return __uint_as_float(r[0]) + __uint_as_float(r[1]); }
DI f32x16 mfma32(u32x4 a, u32x4 b, f32x16 c) { return __builtin_amdgcn_mfma_f32_32x32x16_bf16(__builtin_bit_cast(bf16x8, a), __builtin_bit_cast(bf16x8, b), c, 0, 0, 0); }
DI f32x16 mfma32k8(u32x2 a, u32x2 b, f32x16 c) { return __builtin_amdgcn_mfma_f32_32x32x8bf16_1k(__builtin_bit_cast(s16x4, a), __builtin_bit_cast(s16x4, b), c, 0, 0, 0); }
DI f32x4 mfma16(u32x4 a, u32x4 b, f32x4 c) { return __builtin_amdgcn_mfma_f32_16x16x32_bf16(__builtin_bit_cast(bf16x8, a), __builtin_bit_cast(bf16x8, b), c, 0, 0, 0); }
DI u32x4 lds128(lptr p) { return *(LAS u32x4*)p; }
DI u32x2 ldstr(lptr p) { return __builtin_bit_cast(u32x2, __builtin_amdgcn_ds_read_tr16_b64_v4i16((LAS s16x4*)p)); }

template <int OFF> DI void glds(unsigned voff, const void* sbase, unsigned ldsb) {
    asm volatile("s_add_u32 m0, %2, %3\n\ts_nop 0\n\tglobal_load_lds_dwordx4 %0, %1" ::"v"(voff), "s"(sbase), "s"(ldsb), "i"(OFF) : "memory", "scc");
}
template <int IMM> DI void gld128(u32x4& d, unsigned voff, const void* sbase) {
    asm volatile("global_load_dwordx4 %0, %1, %2 offset:%3" : "=v"(d) : "v"(voff), "s"(sbase), "i"(IMM) : "memory");
}
template <int IMM> DI void gld64(u32x2& d, unsigned voff, const void* sbase) {
    asm volatile("global_load_dwordx2 %0, %1, %2 offset:%3" : "=v"(d) : "v"(voff), "s"(sbase), "i"(IMM) : "memory");
}
template <int IMM> DI void gld32(unsigned& d, unsigned voff, const void* sbase) {
    asm volatile("global_load_dword %0, %1, %2 offset:%3" : "=v"(d) : "v"(voff), "s"(sbase), "i"(IMM) : "memory");
}
template <int IMM> DI void gldu16(unsigned& d, unsigned voff, const void* sbase) {
    asm volatile("global_load_ushort %0, %1, %2 offset:%3" : "=v"(d) : "v"(voff), "s"(sbase), "i"(IMM) : "memory");
}
DI void glds_s(unsigned voff, const void* sbase, unsigned ldsa) {
    asm volatile("s_mov_b32 m0, %2\n\ts_nop 0\n\tglobal_load_lds_dwordx4 %0, %1 sc1" ::"v"(voff), "s"(sbase), "s"(ldsa) : "memory");
}
template <int OFF, unsigned LO, unsigned HI> DI void glds_m(unsigned voff, const void* sbase, unsigned ldsb) {
    asm volatile("s_add_u32 m0, %2, %3\n\ts_mov_b32 exec_lo, %4\n\ts_mov_b32 exec_hi, %5\n\tglobal_load_lds_dwordx4 %0, %1 sc1\n\ts_mov_b64 exec, -1"
                 ::"v"(voff), "s"(sbase), "s"(ldsb), "i"(OFF), "i"(LO), "i"(HI) : "memory", "scc");
}
template <int IMM> DI void gld128s(u32x4& d, unsigned voff, const void* sbase) {
    asm volatile("global_load_dwordx4 %0, %1, %2 offset:%3 sc1" : "=v"(d) : "v"(voff), "s"(sbase), "i"(IMM) : "memory");
}
template <int IMM> DI void gld64s(u32x2& d, unsigned voff, const void* sbase) {
    asm volatile("global_load_dwordx2 %0, %1, %2 offset:%3 sc1" : "=v"(d) : "v"(voff), "s"(sbase), "i"(IMM) : "memory");
}
DI void gst128(unsigned voff, const void* sbase, u32x4 v) {
    asm volatile("global_store_dwordx4 %0, %1, %2 sc1\n\ts_nop 1" ::"v"(voff), "v"(v), "s"(sbase) : "memory");
}
DI void gst64(unsigned voff, const void* sbase, u32x2 v) {
    asm volatile("global_store_dwordx2 %0, %1, %2 sc1" ::"v"(voff), "v"(v), "s"(sbase) : "memory");
}
DI void warm64(unsigned voff, const void* sbase, unsigned ldsa) {
    asm volatile("s_mov_b32 m0, %2\n\ts_nop 0\n\tglobal_load_lds_dword %0, %1 sc1" ::"v"(voff), "s"(sbase), "s"(ldsa) : "memory");
}
template <int N> DI void wait_vm() { asm volatile("s_waitcnt vmcnt(%0)" ::"i"(N) : "memory"); }
template <int N> DI void wait_vm_bar() { asm volatile("s_waitcnt vmcnt(%0) lgkmcnt(0)\n\ts_barrier" ::"i"(N) : "memory"); }
DI void lds_bar() { asm volatile("s_waitcnt lgkmcnt(0)\n\ts_barrier" ::: "memory"); }
template <class T> DI void pin(T& v) { asm volatile("" : "+v"(v)); }

#define STARG , unsigned long long (&str)[2]
#define STPASS , str
#define STWAIT
#define STDECL unsigned long long str[2] = {}
#define ST(i) do { if constexpr ((i) == DIAG) asm volatile("s_memrealtime %0\n\ts_waitcnt lgkmcnt(0)" : "=s"(str[1])::"memory"); } while (0)
#define STR(i) do { if constexpr ((i) == 0) asm volatile("s_memrealtime %0\n\ts_waitcnt lgkmcnt(0)" : "=s"(str[0])::"memory"); else ST(i); } while (0)
#define STFLUSH(k) do { if (w == 0 && str[1]) { const unsigned long long t0_ = __builtin_amdgcn_s_memrealtime(), d_ = 10ull * (str[1] - str[0]); \
    while (__builtin_amdgcn_s_memrealtime() - t0_ < d_) __builtin_amdgcn_s_sleep(2); } } while (0)

DI void ln_stats(float s1, float s2, float& m, float& rs) {
    const float mean = s1 * (1.0f / DM);
    const float var = fmaxf(s2 * (1.0f / DM) - mean * mean, 0.0f);
    m = rbf(mean);
    rs = __builtin_amdgcn_rcpf(rbf(__builtin_amdgcn_sqrtf(rbf(rbf(var) + 1e-5f))));
}

#define QKV_LOOP_QK \
    "s_nop 1\n\t" \
    "1:\n\t" \
    "s_waitcnt lgkmcnt(0)\n\t" \
    "v_mfma_f32_32x32x16_bf16 v[64:79], v[40:43], v[32:35], v[64:79]\n\t" \
    "ds_read_b128 v[48:51], %[x1] offset:0\n\t" \
    "ds_read_b64_tr_b16 v[56:57], %[w0] offset:4096\n\t" \
    "ds_read_b64_tr_b16 v[58:59], %[w0] offset:5120\n\t" \
    "v_lshlrev_b32 v14, 16, v32\n\t" \
    "v_and_b32 v15, 0xffff0000, v32\n\t" \
    "v_mfma_f32_32x32x16_bf16 v[80:95], v[44:47], v[32:35], v[80:95]\n\t" \
    "ds_read_b64_tr_b16 v[60:61], %[w1] offset:4096\n\t" \
    "ds_read_b64_tr_b16 v[62:63], %[w1] offset:5120\n\t" \
    "v_fmac_f32 v18, v14, v14\n\t" \
    "v_fmac_f32 v19, v15, v15\n\t" \
    "v_lshlrev_b32 v16, 16, v33\n\t" \
    "v_and_b32 v17, 0xffff0000, v33\n\t" \
    "v_mfma_f32_16x16x32_bf16 v[24:27], v[20:23], v[32:35], v[24:27]\n\t" \
    "ds_read_b128 v[52:55], %[y1] offset:0\n\t" \
    "v_fmac_f32 v18, v16, v16\n\t" \
    "v_mfma_f32_32x32x16_bf16 v[96:111], v[40:43], v[36:39], v[96:111]\n\t" \
    "v_fmac_f32 v19, v17, v17\n\t" \
    "v_lshlrev_b32 v14, 16, v34\n\t" \
    "v_and_b32 v15, 0xffff0000, v34\n\t" \
    "v_fmac_f32 v18, v14, v14\n\t" \
    "v_fmac_f32 v19, v15, v15\n\t" \
    "v_mfma_f32_32x32x16_bf16 v[112:127], v[44:47], v[36:39], v[112:127]\n\t" \
    "v_lshlrev_b32 v16, 16, v35\n\t" \
    "v_and_b32 v17, 0xffff0000, v35\n\t" \
    "v_fmac_f32 v18, v16, v16\n\t" \
    "v_fmac_f32 v19, v17, v17\n\t" \
    "v_mfma_f32_16x16x32_bf16 v[28:31], v[20:23], v[40:43], v[28:31]\n\t" \
    "s_waitcnt lgkmcnt(0)\n\t" \
    "v_mfma_f32_32x32x16_bf16 v[64:79], v[56:59], v[48:51], v[64:79]\n\t" \
    "ds_read_b128 v[32:35], %[x2] offset:0\n\t" \
    "ds_read_b64_tr_b16 v[40:41], %[w0] offset:8192\n\t" \
    "ds_read_b64_tr_b16 v[42:43], %[w0] offset:9216\n\t" \
    "v_lshlrev_b32 v14, 16, v48\n\t" \
    "v_and_b32 v15, 0xffff0000, v48\n\t" \
    "v_mfma_f32_32x32x16_bf16 v[80:95], v[60:63], v[48:51], v[80:95]\n\t" \
    "ds_read_b64_tr_b16 v[44:45], %[w1] offset:8192\n\t" \
    "ds_read_b64_tr_b16 v[46:47], %[w1] offset:9216\n\t" \
    "v_fmac_f32 v18, v14, v14\n\t" \
    "v_fmac_f32 v19, v15, v15\n\t" \
    "v_lshlrev_b32 v16, 16, v49\n\t" \
    "v_and_b32 v17, 0xffff0000, v49\n\t" \
    "v_mfma_f32_16x16x32_bf16 v[24:27], v[20:23], v[48:51], v[24:27]\n\t" \
    "ds_read_b128 v[36:39], %[y2] offset:0\n\t" \
    "v_fmac_f32 v18, v16, v16\n\t" \
    "v_mfma_f32_32x32x16_bf16 v[96:111], v[56:59], v[52:55], v[96:111]\n\t" \
    "v_fmac_f32 v19, v17, v17\n\t" \
    "v_lshlrev_b32 v14, 16, v50\n\t" \
    "v_and_b32 v15, 0xffff0000, v50\n\t" \
    "v_fmac_f32 v18, v14, v14\n\t" \
    "v_fmac_f32 v19, v15, v15\n\t" \
    "v_mfma_f32_32x32x16_bf16 v[112:127], v[60:63], v[52:55], v[112:127]\n\t" \
    "v_lshlrev_b32 v16, 16, v51\n\t" \
    "v_and_b32 v17, 0xffff0000, v51\n\t" \
    "v_fmac_f32 v18, v16, v16\n\t" \
    "v_fmac_f32 v19, v17, v17\n\t" \
    "v_mfma_f32_16x16x32_bf16 v[28:31], v[20:23], v[56:59], v[28:31]\n\t" \
    "s_waitcnt lgkmcnt(0)\n\t" \
    "v_mfma_f32_32x32x16_bf16 v[64:79], v[40:43], v[32:35], v[64:79]\n\t" \
    "ds_read_b128 v[48:51], %[x3] offset:0\n\t" \
    "ds_read_b64_tr_b16 v[56:57], %[w0] offset:12288\n\t" \
    "ds_read_b64_tr_b16 v[58:59], %[w0] offset:13312\n\t" \
    "v_lshlrev_b32 v14, 16, v32\n\t" \
    "v_and_b32 v15, 0xffff0000, v32\n\t" \
    "v_mfma_f32_32x32x16_bf16 v[80:95], v[44:47], v[32:35], v[80:95]\n\t" \
    "ds_read_b64_tr_b16 v[60:61], %[w1] offset:12288\n\t" \
    "ds_read_b64_tr_b16 v[62:63], %[w1] offset:13312\n\t" \
    "v_fmac_f32 v18, v14, v14\n\t" \
    "v_fmac_f32 v19, v15, v15\n\t" \
    "v_lshlrev_b32 v16, 16, v33\n\t" \
    "v_and_b32 v17, 0xffff0000, v33\n\t" \
    "v_mfma_f32_16x16x32_bf16 v[24:27], v[20:23], v[32:35], v[24:27]\n\t" \
    "ds_read_b128 v[52:55], %[y3] offset:0\n\t" \
    "v_fmac_f32 v18, v16, v16\n\t" \
    "v_mfma_f32_32x32x16_bf16 v[96:111], v[40:43], v[36:39], v[96:111]\n\t" \
    "v_fmac_f32 v19, v17, v17\n\t" \
    "v_lshlrev_b32 v14, 16, v34\n\t" \
    "v_and_b32 v15, 0xffff0000, v34\n\t" \
    "v_fmac_f32 v18, v14, v14\n\t" \
    "v_fmac_f32 v19, v15, v15\n\t" \
    "v_mfma_f32_32x32x16_bf16 v[112:127], v[44:47], v[36:39], v[112:127]\n\t" \
    "v_lshlrev_b32 v16, 16, v35\n\t" \
    "v_and_b32 v17, 0xffff0000, v35\n\t" \
    "v_fmac_f32 v18, v16, v16\n\t" \
    "v_fmac_f32 v19, v17, v17\n\t" \
    "v_mfma_f32_16x16x32_bf16 v[28:31], v[20:23], v[40:43], v[28:31]\n\t" \
    "s_waitcnt lgkmcnt(0)\n\t" \
    "s_barrier\n\t" \
    "v_mfma_f32_32x32x16_bf16 v[64:79], v[56:59], v[48:51], v[64:79]\n\t" \
    "ds_read_b128 v[32:35], %[x0] offset:32768\n\t" \
    "ds_read_b64_tr_b16 v[40:41], %[w0] offset:32768\n\t" \
    "ds_read_b64_tr_b16 v[42:43], %[w0] offset:33792\n\t" \
    "v_lshlrev_b32 v14, 16, v48\n\t" \
    "v_and_b32 v15, 0xffff0000, v48\n\t" \
    "v_mfma_f32_32x32x16_bf16 v[80:95], v[60:63], v[48:51], v[80:95]\n\t" \
    "ds_read_b64_tr_b16 v[44:45], %[w1] offset:32768\n\t" \
    "ds_read_b64_tr_b16 v[46:47], %[w1] offset:33792\n\t" \
    "v_fmac_f32 v18, v14, v14\n\t" \
    "v_fmac_f32 v19, v15, v15\n\t" \
    "v_lshlrev_b32 v16, 16, v49\n\t" \
    "v_and_b32 v17, 0xffff0000, v49\n\t" \
    "v_mfma_f32_16x16x32_bf16 v[24:27], v[20:23], v[48:51], v[24:27]\n\t" \
    "ds_read_b128 v[36:39], %[y0] offset:32768\n\t" \
    "v_fmac_f32 v18, v16, v16\n\t" \
    "v_mfma_f32_32x32x16_bf16 v[96:111], v[56:59], v[52:55], v[96:111]\n\t" \
    "v_fmac_f32 v19, v17, v17\n\t" \
    "v_lshlrev_b32 v14, 16, v50\n\t" \
    "v_and_b32 v15, 0xffff0000, v50\n\t" \
    "v_fmac_f32 v18, v14, v14\n\t" \
    "v_fmac_f32 v19, v15, v15\n\t" \
    "v_mfma_f32_32x32x16_bf16 v[112:127], v[60:63], v[52:55], v[112:127]\n\t" \
    "v_lshlrev_b32 v16, 16, v51\n\t" \
    "v_and_b32 v17, 0xffff0000, v51\n\t" \
    "v_fmac_f32 v18, v16, v16\n\t" \
    "v_fmac_f32 v19, v17, v17\n\t" \
    "v_mfma_f32_16x16x32_bf16 v[28:31], v[20:23], v[56:59], v[28:31]\n\t" \
    "s_waitcnt lgkmcnt(0)\n\t" \
    "v_mfma_f32_32x32x16_bf16 v[64:79], v[40:43], v[32:35], v[64:79]\n\t" \
    "ds_read_b128 v[48:51], %[x1] offset:32768\n\t" \
    "ds_read_b64_tr_b16 v[56:57], %[w0] offset:36864\n\t" \
    "ds_read_b64_tr_b16 v[58:59], %[w0] offset:37888\n\t" \
    "v_lshlrev_b32 v14, 16, v32\n\t" \
    "v_and_b32 v15, 0xffff0000, v32\n\t" \
    "v_mfma_f32_32x32x16_bf16 v[80:95], v[44:47], v[32:35], v[80:95]\n\t" \
    "ds_read_b64_tr_b16 v[60:61], %[w1] offset:36864\n\t" \
    "ds_read_b64_tr_b16 v[62:63], %[w1] offset:37888\n\t" \
    "v_fmac_f32 v18, v14, v14\n\t" \
    "v_fmac_f32 v19, v15, v15\n\t" \
    "v_lshlrev_b32 v16, 16, v33\n\t" \
    "v_and_b32 v17, 0xffff0000, v33\n\t" \
    "v_mfma_f32_16x16x32_bf16 v[24:27], v[20:23], v[32:35], v[24:27]\n\t" \
    "ds_read_b128 v[52:55], %[y1] offset:32768\n\t" \
    "v_fmac_f32 v18, v16, v16\n\t" \
    "v_mfma_f32_32x32x16_bf16 v[96:111], v[40:43], v[36:39], v[96:111]\n\t" \
    "v_fmac_f32 v19, v17, v17\n\t" \
    "v_lshlrev_b32 v14, 16, v34\n\t" \
    "v_and_b32 v15, 0xffff0000, v34\n\t" \
    "v_fmac_f32 v18, v14, v14\n\t" \
    "v_fmac_f32 v19, v15, v15\n\t" \
    "v_mfma_f32_32x32x16_bf16 v[112:127], v[44:47], v[36:39], v[112:127]\n\t" \
    "v_lshlrev_b32 v16, 16, v35\n\t" \
    "v_and_b32 v17, 0xffff0000, v35\n\t" \
    "v_fmac_f32 v18, v16, v16\n\t" \
    "v_fmac_f32 v19, v17, v17\n\t" \
    "v_mfma_f32_16x16x32_bf16 v[28:31], v[20:23], v[40:43], v[28:31]\n\t" \
    "v_xor_b32 %[x0], 0x10000, %[x0]\n\t" \
    "v_xor_b32 %[y0], 0x10000, %[y0]\n\t" \
    "s_waitcnt lgkmcnt(0)\n\t" \
    "v_mfma_f32_32x32x16_bf16 v[64:79], v[56:59], v[48:51], v[64:79]\n\t" \
    "ds_read_b128 v[32:35], %[x2] offset:32768\n\t" \
    "ds_read_b64_tr_b16 v[40:41], %[w0] offset:40960\n\t" \
    "ds_read_b64_tr_b16 v[42:43], %[w0] offset:41984\n\t" \
    "v_lshlrev_b32 v14, 16, v48\n\t" \
    "v_and_b32 v15, 0xffff0000, v48\n\t" \
    "v_mfma_f32_32x32x16_bf16 v[80:95], v[60:63], v[48:51], v[80:95]\n\t" \
    "ds_read_b64_tr_b16 v[44:45], %[w1] offset:40960\n\t" \
    "ds_read_b64_tr_b16 v[46:47], %[w1] offset:41984\n\t" \
    "v_fmac_f32 v18, v14, v14\n\t" \
    "v_fmac_f32 v19, v15, v15\n\t" \
    "v_lshlrev_b32 v16, 16, v49\n\t" \
    "v_and_b32 v17, 0xffff0000, v49\n\t" \
    "v_mfma_f32_16x16x32_bf16 v[24:27], v[20:23], v[48:51], v[24:27]\n\t" \
    "ds_read_b128 v[36:39], %[y2] offset:32768\n\t" \
    "v_fmac_f32 v18, v16, v16\n\t" \
    "v_mfma_f32_32x32x16_bf16 v[96:111], v[56:59], v[52:55], v[96:111]\n\t" \
    "v_fmac_f32 v19, v17, v17\n\t" \
    "v_lshlrev_b32 v14, 16, v50\n\t" \
    "v_and_b32 v15, 0xffff0000, v50\n\t" \
    "v_fmac_f32 v18, v14, v14\n\t" \
    "v_fmac_f32 v19, v15, v15\n\t" \
    "v_mfma_f32_32x32x16_bf16 v[112:127], v[60:63], v[52:55], v[112:127]\n\t" \
    "v_lshlrev_b32 v16, 16, v51\n\t" \
    "v_and_b32 v17, 0xffff0000, v51\n\t" \
    "v_fmac_f32 v18, v16, v16\n\t" \
    "v_fmac_f32 v19, v17, v17\n\t" \
    "v_mfma_f32_16x16x32_bf16 v[28:31], v[20:23], v[56:59], v[28:31]\n\t" \
    "v_xor_b32 %[x1], 0x10000, %[x1]\n\t" \
    "v_xor_b32 %[y1], 0x10000, %[y1]\n\t" \
    "s_waitcnt lgkmcnt(0)\n\t" \
    "v_mfma_f32_32x32x16_bf16 v[64:79], v[40:43], v[32:35], v[64:79]\n\t" \
    "ds_read_b128 v[48:51], %[x3] offset:32768\n\t" \
    "ds_read_b64_tr_b16 v[56:57], %[w0] offset:45056\n\t" \
    "ds_read_b64_tr_b16 v[58:59], %[w0] offset:46080\n\t" \
    "v_lshlrev_b32 v14, 16, v32\n\t" \
    "v_and_b32 v15, 0xffff0000, v32\n\t" \
    "v_mfma_f32_32x32x16_bf16 v[80:95], v[44:47], v[32:35], v[80:95]\n\t" \
    "ds_read_b64_tr_b16 v[60:61], %[w1] offset:45056\n\t" \
    "ds_read_b64_tr_b16 v[62:63], %[w1] offset:46080\n\t" \
    "v_fmac_f32 v18, v14, v14\n\t" \
    "v_fmac_f32 v19, v15, v15\n\t" \
    "v_lshlrev_b32 v16, 16, v33\n\t" \
    "v_and_b32 v17, 0xffff0000, v33\n\t" \
    "v_mfma_f32_16x16x32_bf16 v[24:27], v[20:23], v[32:35], v[24:27]\n\t" \
    "ds_read_b128 v[52:55], %[y3] offset:32768\n\t" \
    "v_fmac_f32 v18, v16, v16\n\t" \
    "v_mfma_f32_32x32x16_bf16 v[96:111], v[40:43], v[36:39], v[96:111]\n\t" \
    "v_fmac_f32 v19, v17, v17\n\t" \
    "v_lshlrev_b32 v14, 16, v34\n\t" \
    "v_and_b32 v15, 0xffff0000, v34\n\t" \
    "v_fmac_f32 v18, v14, v14\n\t" \
    "v_fmac_f32 v19, v15, v15\n\t" \
    "v_mfma_f32_32x32x16_bf16 v[112:127], v[44:47], v[36:39], v[112:127]\n\t" \
    "v_lshlrev_b32 v16, 16, v35\n\t" \
    "v_and_b32 v17, 0xffff0000, v35\n\t" \
    "v_fmac_f32 v18, v16, v16\n\t" \
    "v_fmac_f32 v19, v17, v17\n\t" \
    "v_mfma_f32_16x16x32_bf16 v[28:31], v[20:23], v[40:43], v[28:31]\n\t" \
    "v_xor_b32 %[w0], 0x10000, %[w0]\n\t" \
    "v_xor_b32 %[w1], 0x10000, %[w1]\n\t" \
    "s_waitcnt lgkmcnt(0)\n\t" \
    "s_barrier\n\t" \
    "v_mfma_f32_32x32x16_bf16 v[64:79], v[56:59], v[48:51], v[64:79]\n\t" \
    "ds_read_b128 v[32:35], %[x0] offset:0\n\t" \
    "ds_read_b64_tr_b16 v[40:41], %[w0] offset:0\n\t" \
    "ds_read_b64_tr_b16 v[42:43], %[w0] offset:1024\n\t" \
    "v_lshlrev_b32 v14, 16, v48\n\t" \
    "v_and_b32 v15, 0xffff0000, v48\n\t" \
    "v_mfma_f32_32x32x16_bf16 v[80:95], v[60:63], v[48:51], v[80:95]\n\t" \
    "ds_read_b64_tr_b16 v[44:45], %[w1] offset:0\n\t" \
    "ds_read_b64_tr_b16 v[46:47], %[w1] offset:1024\n\t" \
    "v_fmac_f32 v18, v14, v14\n\t" \
    "v_fmac_f32 v19, v15, v15\n\t" \
    "v_lshlrev_b32 v16, 16, v49\n\t" \
    "v_and_b32 v17, 0xffff0000, v49\n\t" \
    "v_mfma_f32_16x16x32_bf16 v[24:27], v[20:23], v[48:51], v[24:27]\n\t" \
    "ds_read_b128 v[36:39], %[y0] offset:0\n\t" \
    "v_fmac_f32 v18, v16, v16\n\t" \
    "v_mfma_f32_32x32x16_bf16 v[96:111], v[56:59], v[52:55], v[96:111]\n\t" \
    "v_fmac_f32 v19, v17, v17\n\t" \
    "v_lshlrev_b32 v14, 16, v50\n\t" \
    "v_and_b32 v15, 0xffff0000, v50\n\t" \
    "v_fmac_f32 v18, v14, v14\n\t" \
    "v_fmac_f32 v19, v15, v15\n\t" \
    "v_mfma_f32_32x32x16_bf16 v[112:127], v[60:63], v[52:55], v[112:127]\n\t" \
    "v_lshlrev_b32 v16, 16, v51\n\t" \
    "v_and_b32 v17, 0xffff0000, v51\n\t" \
    "v_fmac_f32 v18, v16, v16\n\t" \
    "v_fmac_f32 v19, v17, v17\n\t" \
    "v_mfma_f32_16x16x32_bf16 v[28:31], v[20:23], v[56:59], v[28:31]\n\t" \
    "v_xor_b32 %[x2], 0x10000, %[x2]\n\t" \
    "v_xor_b32 %[y2], 0x10000, %[y2]\n\t" \
    "v_xor_b32 %[x3], 0x10000, %[x3]\n\t" \
    "v_xor_b32 %[y3], 0x10000, %[y3]\n\t" \
    "s_sub_u32 %[cnt], %[cnt], 1\n\t" \
    "s_cbranch_scc0 1b\n\t" \
    "s_waitcnt lgkmcnt(0)\n\t" \
    "s_nop 15\n\t"
#define QKV_LOOP_V \
    "s_nop 1\n\t" \
    "1:\n\t" \
    "s_waitcnt lgkmcnt(0)\n\t" \
    "v_mfma_f32_32x32x16_bf16 v[64:79], v[32:35], v[40:43], v[64:79]\n\t" \
    "ds_read_b128 v[48:51], %[x1] offset:0\n\t" \
    "ds_read_b64_tr_b16 v[56:57], %[w0] offset:4096\n\t" \
    "ds_read_b64_tr_b16 v[58:59], %[w0] offset:5120\n\t" \
    "v_lshlrev_b32 v14, 16, v32\n\t" \
    "v_and_b32 v15, 0xffff0000, v32\n\t" \
    "v_mfma_f32_32x32x16_bf16 v[80:95], v[32:35], v[44:47], v[80:95]\n\t" \
    "ds_read_b64_tr_b16 v[60:61], %[w1] offset:4096\n\t" \
    "ds_read_b64_tr_b16 v[62:63], %[w1] offset:5120\n\t" \
    "v_fmac_f32 v18, v14, v14\n\t" \
    "v_fmac_f32 v19, v15, v15\n\t" \
    "v_lshlrev_b32 v16, 16, v33\n\t" \
    "v_and_b32 v17, 0xffff0000, v33\n\t" \
    "v_mfma_f32_16x16x32_bf16 v[24:27], v[20:23], v[32:35], v[24:27]\n\t" \
    "ds_read_b128 v[52:55], %[y1] offset:0\n\t" \
    "v_fmac_f32 v18, v16, v16\n\t" \
    "v_mfma_f32_32x32x16_bf16 v[96:111], v[36:39], v[40:43], v[96:111]\n\t" \
    "v_fmac_f32 v19, v17, v17\n\t" \
    "v_lshlrev_b32 v14, 16, v34\n\t" \
    "v_and_b32 v15, 0xffff0000, v34\n\t" \
    "v_fmac_f32 v18, v14, v14\n\t" \
    "v_fmac_f32 v19, v15, v15\n\t" \
    "v_mfma_f32_32x32x16_bf16 v[112:127], v[36:39], v[44:47], v[112:127]\n\t" \
    "v_lshlrev_b32 v16, 16, v35\n\t" \
    "v_and_b32 v17, 0xffff0000, v35\n\t" \
    "v_fmac_f32 v18, v16, v16\n\t" \
    "v_fmac_f32 v19, v17, v17\n\t" \
    "v_mfma_f32_16x16x32_bf16 v[28:31], v[20:23], v[40:43], v[28:31]\n\t" \
    "s_waitcnt lgkmcnt(0)\n\t" \
    "v_mfma_f32_32x32x16_bf16 v[64:79], v[48:51], v[56:59], v[64:79]\n\t" \
    "ds_read_b128 v[32:35], %[x2] offset:0\n\t" \
    "ds_read_b64_tr_b16 v[40:41], %[w0] offset:8192\n\t" \
    "ds_read_b64_tr_b16 v[42:43], %[w0] offset:9216\n\t" \
    "v_lshlrev_b32 v14, 16, v48\n\t" \
    "v_and_b32 v15, 0xffff0000, v48\n\t" \
    "v_mfma_f32_32x32x16_bf16 v[80:95], v[48:51], v[60:63], v[80:95]\n\t" \
    "ds_read_b64_tr_b16 v[44:45], %[w1] offset:8192\n\t" \
    "ds_read_b64_tr_b16 v[46:47], %[w1] offset:9216\n\t" \
    "v_fmac_f32 v18, v14, v14\n\t" \
    "v_fmac_f32 v19, v15, v15\n\t" \
    "v_lshlrev_b32 v16, 16, v49\n\t" \
    "v_and_b32 v17, 0xffff0000, v49\n\t" \
    "v_mfma_f32_16x16x32_bf16 v[24:27], v[20:23], v[48:51], v[24:27]\n\t" \
    "ds_read_b128 v[36:39], %[y2] offset:0\n\t" \
    "v_fmac_f32 v18, v16, v16\n\t" \
    "v_mfma_f32_32x32x16_bf16 v[96:111], v[52:55], v[56:59], v[96:111]\n\t" \
    "v_fmac_f32 v19, v17, v17\n\t" \
    "v_lshlrev_b32 v14, 16, v50\n\t" \
    "v_and_b32 v15, 0xffff0000, v50\n\t" \
    "v_fmac_f32 v18, v14, v14\n\t" \
    "v_fmac_f32 v19, v15, v15\n\t" \
    "v_mfma_f32_32x32x16_bf16 v[112:127], v[52:55], v[60:63], v[112:127]\n\t" \
    "v_lshlrev_b32 v16, 16, v51\n\t" \
    "v_and_b32 v17, 0xffff0000, v51\n\t" \
    "v_fmac_f32 v18, v16, v16\n\t" \
    "v_fmac_f32 v19, v17, v17\n\t" \
    "v_mfma_f32_16x16x32_bf16 v[28:31], v[20:23], v[56:59], v[28:31]\n\t" \
    "s_waitcnt lgkmcnt(0)\n\t" \
    "v_mfma_f32_32x32x16_bf16 v[64:79], v[32:35], v[40:43], v[64:79]\n\t" \
    "ds_read_b128 v[48:51], %[x3] offset:0\n\t" \
    "ds_read_b64_tr_b16 v[56:57], %[w0] offset:12288\n\t" \
    "ds_read_b64_tr_b16 v[58:59], %[w0] offset:13312\n\t" \
    "v_lshlrev_b32 v14, 16, v32\n\t" \
    "v_and_b32 v15, 0xffff0000, v32\n\t" \
    "v_mfma_f32_32x32x16_bf16 v[80:95], v[32:35], v[44:47], v[80:95]\n\t" \
    "ds_read_b64_tr_b16 v[60:61], %[w1] offset:12288\n\t" \
    "ds_read_b64_tr_b16 v[62:63], %[w1] offset:13312\n\t" \
    "v_fmac_f32 v18, v14, v14\n\t" \
    "v_fmac_f32 v19, v15, v15\n\t" \
    "v_lshlrev_b32 v16, 16, v33\n\t" \
    "v_and_b32 v17, 0xffff0000, v33\n\t" \
    "v_mfma_f32_16x16x32_bf16 v[24:27], v[20:23], v[32:35], v[24:27]\n\t" \
    "ds_read_b128 v[52:55], %[y3] offset:0\n\t" \
    "v_fmac_f32 v18, v16, v16\n\t" \
    "v_mfma_f32_32x32x16_bf16 v[96:111], v[36:39], v[40:43], v[96:111]\n\t" \
    "v_fmac_f32 v19, v17, v17\n\t" \
    "v_lshlrev_b32 v14, 16, v34\n\t" \
    "v_and_b32 v15, 0xffff0000, v34\n\t" \
    "v_fmac_f32 v18, v14, v14\n\t" \
    "v_fmac_f32 v19, v15, v15\n\t" \
    "v_mfma_f32_32x32x16_bf16 v[112:127], v[36:39], v[44:47], v[112:127]\n\t" \
    "v_lshlrev_b32 v16, 16, v35\n\t" \
    "v_and_b32 v17, 0xffff0000, v35\n\t" \
    "v_fmac_f32 v18, v16, v16\n\t" \
    "v_fmac_f32 v19, v17, v17\n\t" \
    "v_mfma_f32_16x16x32_bf16 v[28:31], v[20:23], v[40:43], v[28:31]\n\t" \
    "s_waitcnt lgkmcnt(0)\n\t" \
    "s_barrier\n\t" \
    "v_mfma_f32_32x32x16_bf16 v[64:79], v[48:51], v[56:59], v[64:79]\n\t" \
    "ds_read_b128 v[32:35], %[x0] offset:32768\n\t" \
    "ds_read_b64_tr_b16 v[40:41], %[w0] offset:32768\n\t" \
    "ds_read_b64_tr_b16 v[42:43], %[w0] offset:33792\n\t" \
    "v_lshlrev_b32 v14, 16, v48\n\t" \
    "v_and_b32 v15, 0xffff0000, v48\n\t" \
    "v_mfma_f32_32x32x16_bf16 v[80:95], v[48:51], v[60:63], v[80:95]\n\t" \
    "ds_read_b64_tr_b16 v[44:45], %[w1] offset:32768\n\t" \
    "ds_read_b64_tr_b16 v[46:47], %[w1] offset:33792\n\t" \
    "v_fmac_f32 v18, v14, v14\n\t" \
    "v_fmac_f32 v19, v15, v15\n\t" \
    "v_lshlrev_b32 v16, 16, v49\n\t" \
    "v_and_b32 v17, 0xffff0000, v49\n\t" \
    "v_mfma_f32_16x16x32_bf16 v[24:27], v[20:23], v[48:51], v[24:27]\n\t" \
    "ds_read_b128 v[36:39], %[y0] offset:32768\n\t" \
    "v_fmac_f32 v18, v16, v16\n\t" \
    "v_mfma_f32_32x32x16_bf16 v[96:111], v[52:55], v[56:59], v[96:111]\n\t" \
    "v_fmac_f32 v19, v17, v17\n\t" \
    "v_lshlrev_b32 v14, 16, v50\n\t" \
    "v_and_b32 v15, 0xffff0000, v50\n\t" \
    "v_fmac_f32 v18, v14, v14\n\t" \
    "v_fmac_f32 v19, v15, v15\n\t" \
    "v_mfma_f32_32x32x16_bf16 v[112:127], v[52:55], v[60:63], v[112:127]\n\t" \
    "v_lshlrev_b32 v16, 16, v51\n\t" \
    "v_and_b32 v17, 0xffff0000, v51\n\t" \
    "v_fmac_f32 v18, v16, v16\n\t" \
    "v_fmac_f32 v19, v17, v17\n\t" \
    "v_mfma_f32_16x16x32_bf16 v[28:31], v[20:23], v[56:59], v[28:31]\n\t" \
    "s_waitcnt lgkmcnt(0)\n\t" \
    "v_mfma_f32_32x32x16_bf16 v[64:79], v[32:35], v[40:43], v[64:79]\n\t" \
    "ds_read_b128 v[48:51], %[x1] offset:32768\n\t" \
    "ds_read_b64_tr_b16 v[56:57], %[w0] offset:36864\n\t" \
    "ds_read_b64_tr_b16 v[58:59], %[w0] offset:37888\n\t" \
    "v_lshlrev_b32 v14, 16, v32\n\t" \
    "v_and_b32 v15, 0xffff0000, v32\n\t" \
    "v_mfma_f32_32x32x16_bf16 v[80:95], v[32:35], v[44:47], v[80:95]\n\t" \
    "ds_read_b64_tr_b16 v[60:61], %[w1] offset:36864\n\t" \
    "ds_read_b64_tr_b16 v[62:63], %[w1] offset:37888\n\t" \
    "v_fmac_f32 v18, v14, v14\n\t" \
    "v_fmac_f32 v19, v15, v15\n\t" \
    "v_lshlrev_b32 v16, 16, v33\n\t" \
    "v_and_b32 v17, 0xffff0000, v33\n\t" \
    "v_mfma_f32_16x16x32_bf16 v[24:27], v[20:23], v[32:35], v[24:27]\n\t" \
    "ds_read_b128 v[52:55], %[y1] offset:32768\n\t" \
    "v_fmac_f32 v18, v16, v16\n\t" \
    "v_mfma_f32_32x32x16_bf16 v[96:111], v[36:39], v[40:43], v[96:111]\n\t" \
    "v_fmac_f32 v19, v17, v17\n\t" \
    "v_lshlrev_b32 v14, 16, v34\n\t" \
    "v_and_b32 v15, 0xffff0000, v34\n\t" \
    "v_fmac_f32 v18, v14, v14\n\t" \
    "v_fmac_f32 v19, v15, v15\n\t" \
    "v_mfma_f32_32x32x16_bf16 v[112:127], v[36:39], v[44:47], v[112:127]\n\t" \
    "v_lshlrev_b32 v16, 16, v35\n\t" \
    "v_and_b32 v17, 0xffff0000, v35\n\t" \
    "v_fmac_f32 v18, v16, v16\n\t" \
    "v_fmac_f32 v19, v17, v17\n\t" \
    "v_mfma_f32_16x16x32_bf16 v[28:31], v[20:23], v[40:43], v[28:31]\n\t" \
    "v_xor_b32 %[x0], 0x10000, %[x0]\n\t" \
    "v_xor_b32 %[y0], 0x10000, %[y0]\n\t" \
    "s_waitcnt lgkmcnt(0)\n\t" \
    "v_mfma_f32_32x32x16_bf16 v[64:79], v[48:51], v[56:59], v[64:79]\n\t" \
    "ds_read_b128 v[32:35], %[x2] offset:32768\n\t" \
    "ds_read_b64_tr_b16 v[40:41], %[w0] offset:40960\n\t" \
    "ds_read_b64_tr_b16 v[42:43], %[w0] offset:41984\n\t" \
    "v_lshlrev_b32 v14, 16, v48\n\t" \
    "v_and_b32 v15, 0xffff0000, v48\n\t" \
    "v_mfma_f32_32x32x16_bf16 v[80:95], v[48:51], v[60:63], v[80:95]\n\t" \
    "ds_read_b64_tr_b16 v[44:45], %[w1] offset:40960\n\t" \
    "ds_read_b64_tr_b16 v[46:47], %[w1] offset:41984\n\t" \
    "v_fmac_f32 v18, v14, v14\n\t" \
    "v_fmac_f32 v19, v15, v15\n\t" \
    "v_lshlrev_b32 v16, 16, v49\n\t" \
    "v_and_b32 v17, 0xffff0000, v49\n\t" \
    "v_mfma_f32_16x16x32_bf16 v[24:27], v[20:23], v[48:51], v[24:27]\n\t" \
    "ds_read_b128 v[36:39], %[y2] offset:32768\n\t" \
    "v_fmac_f32 v18, v16, v16\n\t" \
    "v_mfma_f32_32x32x16_bf16 v[96:111], v[52:55], v[56:59], v[96:111]\n\t" \
    "v_fmac_f32 v19, v17, v17\n\t" \
    "v_lshlrev_b32 v14, 16, v50\n\t" \
    "v_and_b32 v15, 0xffff0000, v50\n\t" \
    "v_fmac_f32 v18, v14, v14\n\t" \
    "v_fmac_f32 v19, v15, v15\n\t" \
    "v_mfma_f32_32x32x16_bf16 v[112:127], v[52:55], v[60:63], v[112:127]\n\t" \
    "v_lshlrev_b32 v16, 16, v51\n\t" \
    "v_and_b32 v17, 0xffff0000, v51\n\t" \
    "v_fmac_f32 v18, v16, v16\n\t" \
    "v_fmac_f32 v19, v17, v17\n\t" \
    "v_mfma_f32_16x16x32_bf16 v[28:31], v[20:23], v[56:59], v[28:31]\n\t" \
    "v_xor_b32 %[x1], 0x10000, %[x1]\n\t" \
    "v_xor_b32 %[y1], 0x10000, %[y1]\n\t" \
    "s_waitcnt lgkmcnt(0)\n\t" \
    "v_mfma_f32_32x32x16_bf16 v[64:79], v[32:35], v[40:43], v[64:79]\n\t" \
    "ds_read_b128 v[48:51], %[x3] offset:32768\n\t" \
    "ds_read_b64_tr_b16 v[56:57], %[w0] offset:45056\n\t" \
    "ds_read_b64_tr_b16 v[58:59], %[w0] offset:46080\n\t" \
    "v_lshlrev_b32 v14, 16, v32\n\t" \
    "v_and_b32 v15, 0xffff0000, v32\n\t" \
    "v_mfma_f32_32x32x16_bf16 v[80:95], v[32:35], v[44:47], v[80:95]\n\t" \
    "ds_read_b64_tr_b16 v[60:61], %[w1] offset:45056\n\t" \
    "ds_read_b64_tr_b16 v[62:63], %[w1] offset:46080\n\t" \
    "v_fmac_f32 v18, v14, v14\n\t" \
    "v_fmac_f32 v19, v15, v15\n\t" \
    "v_lshlrev_b32 v16, 16, v33\n\t" \
    "v_and_b32 v17, 0xffff0000, v33\n\t" \
    "v_mfma_f32_16x16x32_bf16 v[24:27], v[20:23], v[32:35], v[24:27]\n\t" \
    "ds_read_b128 v[52:55], %[y3] offset:32768\n\t" \
    "v_fmac_f32 v18, v16, v16\n\t" \
    "v_mfma_f32_32x32x16_bf16 v[96:111], v[36:39], v[40:43], v[96:111]\n\t" \
    "v_fmac_f32 v19, v17, v17\n\t" \
    "v_lshlrev_b32 v14, 16, v34\n\t" \
    "v_and_b32 v15, 0xffff0000, v34\n\t" \
    "v_fmac_f32 v18, v14, v14\n\t" \
    "v_fmac_f32 v19, v15, v15\n\t" \
    "v_mfma_f32_32x32x16_bf16 v[112:127], v[36:39], v[44:47], v[112:127]\n\t" \
    "v_lshlrev_b32 v16, 16, v35\n\t" \
    "v_and_b32 v17, 0xffff0000, v35\n\t" \
    "v_fmac_f32 v18, v16, v16\n\t" \
    "v_fmac_f32 v19, v17, v17\n\t" \
    "v_mfma_f32_16x16x32_bf16 v[28:31], v[20:23], v[40:43], v[28:31]\n\t" \
    "v_xor_b32 %[w0], 0x10000, %[w0]\n\t" \
    "v_xor_b32 %[w1], 0x10000, %[w1]\n\t" \
    "s_waitcnt lgkmcnt(0)\n\t" \
    "s_barrier\n\t" \
    "v_mfma_f32_32x32x16_bf16 v[64:79], v[48:51], v[56:59], v[64:79]\n\t" \
    "ds_read_b128 v[32:35], %[x0] offset:0\n\t" \
    "ds_read_b64_tr_b16 v[40:41], %[w0] offset:0\n\t" \
    "ds_read_b64_tr_b16 v[42:43], %[w0] offset:1024\n\t" \
    "v_lshlrev_b32 v14, 16, v48\n\t" \
    "v_and_b32 v15, 0xffff0000, v48\n\t" \
    "v_mfma_f32_32x32x16_bf16 v[80:95], v[48:51], v[60:63], v[80:95]\n\t" \
    "ds_read_b64_tr_b16 v[44:45], %[w1] offset:0\n\t" \
    "ds_read_b64_tr_b16 v[46:47], %[w1] offset:1024\n\t" \
    "v_fmac_f32 v18, v14, v14\n\t" \
    "v_fmac_f32 v19, v15, v15\n\t" \
    "v_lshlrev_b32 v16, 16, v49\n\t" \
    "v_and_b32 v17, 0xffff0000, v49\n\t" \
    "v_mfma_f32_16x16x32_bf16 v[24:27], v[20:23], v[48:51], v[24:27]\n\t" \
    "ds_read_b128 v[36:39], %[y0] offset:0\n\t" \
    "v_fmac_f32 v18, v16, v16\n\t" \
    "v_mfma_f32_32x32x16_bf16 v[96:111], v[52:55], v[56:59], v[96:111]\n\t" \
    "v_fmac_f32 v19, v17, v17\n\t" \
    "v_lshlrev_b32 v14, 16, v50\n\t" \
    "v_and_b32 v15, 0xffff0000, v50\n\t" \
    "v_fmac_f32 v18, v14, v14\n\t" \
    "v_fmac_f32 v19, v15, v15\n\t" \
    "v_mfma_f32_32x32x16_bf16 v[112:127], v[52:55], v[60:63], v[112:127]\n\t" \
    "v_lshlrev_b32 v16, 16, v51\n\t" \
    "v_and_b32 v17, 0xffff0000, v51\n\t" \
    "v_fmac_f32 v18, v16, v16\n\t" \
    "v_fmac_f32 v19, v17, v17\n\t" \
    "v_mfma_f32_16x16x32_bf16 v[28:31], v[20:23], v[56:59], v[28:31]\n\t" \
    "v_xor_b32 %[x2], 0x10000, %[x2]\n\t" \
    "v_xor_b32 %[y2], 0x10000, %[y2]\n\t" \
    "v_xor_b32 %[x3], 0x10000, %[x3]\n\t" \
    "v_xor_b32 %[y3], 0x10000, %[y3]\n\t" \
    "s_sub_u32 %[cnt], %[cnt], 1\n\t" \
    "s_cbranch_scc0 1b\n\t" \
    "s_waitcnt lgkmcnt(0)\n\t" \
    "s_nop 15\n\t"

constexpr int A_STAGE = 32768, A_IMG = 2 * A_STAGE, A_TAB = 4 * A_STAGE, A_END = A_TAB + 4096;

constexpr int quad_role(int head, int dq) { return head == 19 ? (dq == 0 ? 4 : dq - 1) : head == 25 ? (dq == 2 ? 4 : dq > 2 ? dq - 1 : dq) : dq; }
constexpr unsigned role_mask(int cgm, bool single) {
    unsigned m = 0;
    for (int n = 0; n < 32; ++n) { const int q = 32 * cgm + n, r = quad_role(q / 5, q % 5); m |= (single ? r == 4 : (r & 1) != 0) ? 1u << n : 0u; }
    return m;
}
template <bool S> DI unsigned role_bits(int cgm) {
    return cgm == 0 ? role_mask(0, S) : cgm == 1 ? role_mask(1, S) : cgm == 2 ? role_mask(2, S) : cgm == 3 ? role_mask(3, S) : role_mask(4, S);
}

template <bool ISV>
DI void qkv_consumer(char* ws, lptr lds, int tb, int cg, int w, int lane STARG) {
    const int th = w >> 1, fh = w & 1, hi = lane >> 5, l31 = lane & 31;
    unsigned xp[2][4], wp[2];
    const unsigned ldsb = (unsigned)(size_t)lds;
#pragma unroll
    for (int ks = 0; ks < 4; ++ks) {
        const int c = (((2 * ks + hi) ^ ((l31 >> 1) & 7)) << 4);
        xp[0][ks] = ldsb + (64 * th + 32 * fh + l31) * 128 + c;
        xp[1][ks] = ldsb + (64 * th + 32 * (1 - fh) + l31) * 128 + c;
    }
    {
        const int q = (lane >> 2) & 3, p = lane & 3, cb = (lane >> 4) & 1;
#pragma unroll
        for (int f = 0; f < 2; ++f) {
            const int ft = 2 * fh + (f == 0 ? th : 1 - th);
            wp[f] = ldsb + 16384 + (8 * hi + q) * 256 + (((4 * (ft ^ q)) + 2 * cb + (p >> 1)) << 4) + 8 * (p & 1);
        }
    }
    int oa[2][4];
    if constexpr (!ISV) {
        const int cgm = cg < 5 ? cg : cg - 5;
        const unsigned ms = role_bits<true>(cgm) >> (16 * fh + hi), mo = role_bits<false>(cgm) >> (16 * fh + hi);
        const int o0 = A_IMG + (2 * th + fh) * 8192 + (16 * fh + hi) * 256 + l31 * 16;
#pragma unroll
        for (int f = 0; f < 2; ++f)
#pragma unroll
            for (int g = 0; g < 4; ++g) {
                const int n = 8 * (f == 0 ? th : 1 - th) + 2 * g;
                oa[f][g] = o0 + n * 256 - ((ms >> n) & 1) * (l31 * 8) - ((mo >> n) & 1) * 248;
                pin(oa[f][g]);
            }
    } else {
#pragma unroll
        for (int f = 0; f < 2; ++f) {
            const int col = 128 * (cg - 10) + 64 * fh + 32 * (f == 0 ? th : 1 - th) + l31, head = (col * 3277) >> 16;
            oa[f][0] = (head * 64 + 4 * tb + 2 * th) * KVREC + 1280 + hi * 320 + (col - 20 * head) * 16;
            pin(oa[f][0]);
        }
    }
    const unsigned selv = (lane & 15) == ((lane >> 4) & 1) ? 0x3f803f80u : 0u;
    u32x4 sel = {selv, selv, selv, selv};
    f32x16 acc[2][2] = {};
    f32x4 sx = {}, sw = {};
    float s2a = 0.f, s2b = 0.f;
    u32x4 x00, x01, w00, w01, x10, x11, w10, w11;
    unsigned cnt = 4;
    asm volatile("" : "+{v[64:79]}"(acc[0][0]), "+{v[80:95]}"(acc[0][1]), "+{v[96:111]}"(acc[1][0]), "+{v[112:127]}"(acc[1][1]), "+{v[24:27]}"(sx), "+{v[28:31]}"(sw), "+{v[20:23]}"(sel));
    ST(1);
    lds_bar();
    ST(2);
    {
        x00 = lds128((lptr)(size_t)xp[0][0]); x01 = lds128((lptr)(size_t)xp[1][0]);
        const u32x2 a = ldstr((lptr)(size_t)wp[0]), b = ldstr((lptr)(size_t)(wp[0] + 1024)), c = ldstr((lptr)(size_t)wp[1]), d = ldstr((lptr)(size_t)(wp[1] + 1024));
        w00 = u32x4{a[0], a[1], b[0], b[1]}; w01 = u32x4{c[0], c[1], d[0], d[1]};
    }
#define QKV_OPERANDS \
        : "+{v[64:79]}"(acc[0][0]), "+{v[80:95]}"(acc[0][1]), "+{v[96:111]}"(acc[1][0]), "+{v[112:127]}"(acc[1][1]), "+{v[24:27]}"(sx), "+{v[28:31]}"(sw), \
          "+{v18}"(s2a), "+{v19}"(s2b), "+{v[32:35]}"(x00), "+{v[36:39]}"(x01), "+{v[40:43]}"(w00), "+{v[44:47]}"(w01), \
          [x0] "+v"(xp[0][0]), [x1] "+v"(xp[0][1]), [x2] "+v"(xp[0][2]), [x3] "+v"(xp[0][3]), \
          [y0] "+v"(xp[1][0]), [y1] "+v"(xp[1][1]), [y2] "+v"(xp[1][2]), [y3] "+v"(xp[1][3]), [w0] "+v"(wp[0]), [w1] "+v"(wp[1]), [cnt] "+s"(cnt) \
        : "{v[20:23]}"(sel) \
        : "memory", "scc", "v14", "v15", "v16", "v17", "v48", "v49", "v50", "v51", "v52", "v53", "v54", "v55", "v56", "v57", "v58", "v59", "v60", "v61", "v62", "v63"
    if constexpr (ISV) asm volatile(QKV_LOOP_V QKV_OPERANDS);
    else asm volatile(QKV_LOOP_QK QKV_OPERANDS);
    ST(3);
    LAS float* T1 = (LAS float*)(lds + A_TAB);
    LAS float* T2 = T1 + 128;
    LAS float* TC = T1 + 256;
    LAS f32x2* TS = (LAS f32x2*)(T1 + 384) + w * 64;
    const float s2 = swap32_add(s2a + s2b);
    if (hi == 0) T2[64 * th + 32 * fh + l31] = s2;
    if (lane < 16) {
        T1[64 * th + 32 * fh + lane] = sx[0]; T1[64 * th + 32 * fh + 16 + lane] = sx[1];
        TC[64 * fh + 32 * th + lane] = sw[0]; TC[64 * fh + 32 * th + 16 + lane] = sw[1];
    }
    lds_bar();
    if constexpr (!ISV) {
        f32x4 c4[2][4];
        float t1[2], t2[2];
#pragma unroll
        for (int t = 0; t < 2; ++t) { t1[t] = T1[64 * th + 32 * (t == 0 ? fh : 1 - fh) + l31]; t2[t] = T2[64 * th + 32 * (t == 0 ? fh : 1 - fh) + l31]; }
#pragma unroll
        for (int f = 0; f < 2; ++f)
#pragma unroll
            for (int g = 0; g < 4; ++g) c4[f][g] = *(LAS f32x4*)(TC + 64 * fh + 32 * (f == 0 ? th : 1 - th) + 8 * g + 4 * hi);
        const float sc = cg < 5 ? 0.22360679774997896f * 1.4426950408889634f : 1.0f;
        const int flip = fh ? -8192 : 8192;
#pragma unroll
        for (int t = 0; t < 2; ++t) {
            float m, rs;
            ln_stats(t1[t], t2[t], m, rs);
            rs *= sc;
            const float mr = -m * rs;
#pragma unroll
            for (int f = 0; f < 2; ++f)
#pragma unroll
                for (int g = 0; g < 4; ++g) {
                    const f32x4 v = f32x4{acc[t][f][4 * g], acc[t][f][4 * g + 1], acc[t][f][4 * g + 2], acc[t][f][4 * g + 3]} * rs + c4[f][g] * mr;
                    *(LAS u32x2*)(lds + oa[f][g] + t * flip) = u32x2{pk2(v[0], v[1]), pk2(v[2], v[3])};
                }
        }
    } else {
        {
            float m, rs;
            ln_stats(T1[64 * th + lane], T2[64 * th + lane], m, rs);
            TS[lane] = f32x2{-m * rs, rs};
        }
        const float c0 = TC[64 * fh + 32 * th + l31], c1 = TC[64 * fh + 32 * (1 - th) + l31];
        asm volatile("s_waitcnt lgkmcnt(0)" ::: "memory");
#pragma unroll
        for (int t = 0; t < 2; ++t) {
            const int ta = t == 0 ? fh : 1 - fh;
            f32x4 sa[4], sb[4];
#pragma unroll
            for (int g = 0; g < 4; ++g) { sa[g] = *(LAS f32x4*)(TS + 32 * ta + 8 * g + 4 * hi); sb[g] = *(LAS f32x4*)(TS + 32 * ta + 8 * g + 4 * hi + 2); }
#pragma unroll
            for (int f = 0; f < 2; ++f) {
                const float c = f == 0 ? c0 : c1;
#pragma unroll
                for (int s = 0; s < 2; ++s) {
                    u32x4 o;
#pragma unroll
                    for (int g2 = 0; g2 < 2; ++g2) {
                        const int g = 2 * s + g2;
                        o[2 * g2] = pk2(fmaf(acc[t][f][4 * g], sa[g][1], sa[g][0] * c), fmaf(acc[t][f][4 * g + 1], sa[g][3], sa[g][2] * c));
                        o[2 * g2 + 1] = pk2(fmaf(acc[t][f][4 * g + 2], sb[g][1], sb[g][0] * c), fmaf(acc[t][f][4 * g + 3], sb[g][3], sb[g][2] * c));
                    }
                    gst128(oa[f][0], ws + WS_KV + ta * KVREC + s * 640, o);
                }
            }
        }
    }
}

DI void qkv_loader(const u16* x, const u16* wqkv, lptr lds, int tb, int cg, int j, int lane) {
    unsigned vx[4];
#pragma unroll
    for (int i = 0; i < 4; ++i) {
        const int row = 32 * j + 8 * i + (lane >> 3);
        vx[i] = row * 1280 + (((lane & 7) ^ ((row >> 1) & 7)) << 4);
    }
    unsigned vw[4];
#pragma unroll
    for (int i = 0; i < 4; ++i) vw[i] = (16 * j + 4 * i + (lane >> 4)) * 3840 + (((lane & 15) ^ ((lane >> 4) << 2)) << 4);
    const char* xs = (const char*)x + (size_t)tb * 128 * 1280;
    const char* wsrc = (const char*)wqkv + cg * 256;
    const unsigned ldw = (unsigned)(size_t)lds + j * 4096;
#define A_ROT 0
#define A_DEPTH 2
    const int rot = A_ROT ? (cg + 3 * tb) % 10 : 0;
    auto issue = [&](int s) {
        const unsigned dst = ldw + (s & 3) * A_STAGE;
        const int k = s + rot >= 10 ? s + rot - 10 : s + rot;
        sfor<0, 4>([&](auto I) { glds<I.value * 1024>(vx[I.value], xs + k * 128, dst); glds<16384 + I.value * 1024>(vw[I.value], wsrc + (size_t)k * 64 * 3840, dst); });
    };
    issue(0); issue(1);
    wait_vm_bar<8>();
#pragma unroll 1
    for (int s = 2; s < A_DEPTH; ++s) issue(s);
#pragma unroll 1
    for (int s = A_DEPTH; s < 10; ++s) { issue(s); wait_vm_bar<8 * (A_DEPTH - 1)>(); }
    sfor<0, A_DEPTH - 1>([&](auto I) { wait_vm_bar<8 * (A_DEPTH - 2 - I.value)>(); });
    lds_bar();
    lds_bar();
}

DI void qkv_phase(const u16* x, const u16* wqkv, char* ws, lptr lds, int tb, int cg, int w, int lane STARG) {
    if (w >= 4) qkv_loader(x, wqkv, lds, tb, cg, w - 4, lane);
    else if (cg >= 10) qkv_consumer<true>(ws, lds, tb, cg, w, lane STPASS);
    else qkv_consumer<false>(ws, lds, tb, cg, w, lane STPASS);
    if (cg < 10) {
        const int cgm = cg < 5 ? cg : cg - 5, rec = cg < 5 ? QREC : KVREC;
        const char* dst = ws + (cg < 5 ? WS_Q : WS_KV) + (size_t)4 * tb * rec;
        const int nq = 4 * w + (lane >> 4), gq = 32 * cgm + nq, head = (gq * 205) >> 10, role = quad_role(head, gq - 5 * head);
        const unsigned off = head * 64 * rec + role * 256 + (lane & 15) * 16;
        lds_bar();
        u32x4 v[4];
#pragma unroll
        for (int t = 0; t < 4; ++t) v[t] = lds128(lds + A_IMG + t * 8192 + w * 1024 + lane * 16);
#pragma unroll
        for (int t = 0; t < 4; ++t) gst128(off, dst + t * rec, v[t]);
    }
}

#define ATTN_LOOP_ASM \
    "s_nop 4\n\t" \
    "1:\n\t" \
    "v_mfma_f32_32x32x16_bf16 v[64:79], v[156:159], v[144:147], 0\n\t" \
    "ds_read_b128 v[160:163], %[va] offset:0\n\t" \
    "v_exp_f32 v80, v80\n\t" \
    "v_exp_f32 v81, v81\n\t" \
    "v_exp_f32 v82, v82\n\t" \
    "v_cvt_pk_bf16_f32 v136, v80, v81\n\t" \
    "v_exp_f32 v83, v83\n\t" \
    "v_mfma_f32_32x32x8_bf16 v[64:79], v[164:165], v[152:153], v[64:79]\n\t" \
    "v_cvt_pk_bf16_f32 v137, v82, v83\n\t" \
    "v_exp_f32 v84, v84\n\t" \
    "v_exp_f32 v85, v85\n\t" \
    "v_exp_f32 v86, v86\n\t" \
    "v_cvt_pk_bf16_f32 v138, v84, v85\n\t" \
    "v_exp_f32 v87, v87\n\t" \
    "v_mfma_f32_32x32x16_bf16 v[96:111], v[176:179], v[128:131], v[96:111]\n\t" \
    "ds_read_b64 v[166:167], %[vb] offset:1024\n\t" \
    "v_cvt_pk_bf16_f32 v139, v86, v87\n\t" \
    "v_exp_f32 v88, v88\n\t" \
    "v_exp_f32 v89, v89\n\t" \
    "v_exp_f32 v90, v90\n\t" \
    "v_cvt_pk_bf16_f32 v140, v88, v89\n\t" \
    "v_exp_f32 v91, v91\n\t" \
    "v_mfma_f32_32x32x16_bf16 v[96:111], v[180:183], v[132:135], v[96:111]\n\t" \
    "ds_read_b128 v[168:171], %[va] offset:1280\n\t" \
    "v_cvt_pk_bf16_f32 v141, v90, v91\n\t" \
    "v_exp_f32 v92, v92\n\t" \
    "v_exp_f32 v93, v93\n\t" \
    "v_exp_f32 v94, v94\n\t" \
    "v_exp_f32 v95, v95\n\t" \
    "v_cvt_pk_bf16_f32 v142, v92, v93\n\t" \
    "v_cvt_pk_bf16_f32 v143, v94, v95\n\t" \
    "v_mfma_f32_32x32x16_bf16 v[80:95], v[156:159], v[148:151], 0\n\t" \
    "ds_read_b128 v[172:175], %[va] offset:2304\n\t" \
    "v_exp_f32 v64, v64\n\t" \
    "v_exp_f32 v65, v65\n\t" \
    "v_exp_f32 v66, v66\n\t" \
    "v_cvt_pk_bf16_f32 v128, v64, v65\n\t" \
    "v_exp_f32 v67, v67\n\t" \
    "v_mfma_f32_32x32x8_bf16 v[80:95], v[164:165], v[154:155], v[80:95]\n\t" \
    "v_cvt_pk_bf16_f32 v129, v66, v67\n\t" \
    "v_exp_f32 v68, v68\n\t" \
    "v_exp_f32 v69, v69\n\t" \
    "v_exp_f32 v70, v70\n\t" \
    "v_cvt_pk_bf16_f32 v130, v68, v69\n\t" \
    "v_exp_f32 v71, v71\n\t" \
    "v_mfma_f32_32x32x16_bf16 v[112:127], v[176:179], v[136:139], v[112:127]\n\t" \
    "v_cvt_pk_bf16_f32 v131, v70, v71\n\t" \
    "v_exp_f32 v72, v72\n\t" \
    "v_exp_f32 v73, v73\n\t" \
    "v_exp_f32 v74, v74\n\t" \
    "v_cvt_pk_bf16_f32 v132, v72, v73\n\t" \
    "v_exp_f32 v75, v75\n\t" \
    "v_mfma_f32_32x32x16_bf16 v[112:127], v[180:183], v[140:143], v[112:127]\n\t" \
    "s_waitcnt lgkmcnt(0)\n\t" \
    "v_cvt_pk_bf16_f32 v133, v74, v75\n\t" \
    "v_exp_f32 v76, v76\n\t" \
    "v_exp_f32 v77, v77\n\t" \
    "v_exp_f32 v78, v78\n\t" \
    "v_exp_f32 v79, v79\n\t" \
    "v_cvt_pk_bf16_f32 v134, v76, v77\n\t" \
    "v_cvt_pk_bf16_f32 v135, v78, v79\n\t" \
    "v_mfma_f32_32x32x16_bf16 v[64:79], v[160:163], v[144:147], 0\n\t" \
    "ds_read_b128 v[156:159], %[va] offset:3328\n\t" \
    "v_exp_f32 v80, v80\n\t" \
    "v_exp_f32 v81, v81\n\t" \
    "v_exp_f32 v82, v82\n\t" \
    "v_cvt_pk_bf16_f32 v136, v80, v81\n\t" \
    "v_exp_f32 v83, v83\n\t" \
    "v_mfma_f32_32x32x8_bf16 v[64:79], v[166:167], v[152:153], v[64:79]\n\t" \
    "v_cvt_pk_bf16_f32 v137, v82, v83\n\t" \
    "v_exp_f32 v84, v84\n\t" \
    "v_exp_f32 v85, v85\n\t" \
    "v_exp_f32 v86, v86\n\t" \
    "v_cvt_pk_bf16_f32 v138, v84, v85\n\t" \
    "v_exp_f32 v87, v87\n\t" \
    "v_mfma_f32_32x32x16_bf16 v[96:111], v[168:171], v[128:131], v[96:111]\n\t" \
    "ds_read_b64 v[164:165], %[vb] offset:4352\n\t" \
    "v_cvt_pk_bf16_f32 v139, v86, v87\n\t" \
    "v_exp_f32 v88, v88\n\t" \
    "v_exp_f32 v89, v89\n\t" \
    "v_exp_f32 v90, v90\n\t" \
    "v_cvt_pk_bf16_f32 v140, v88, v89\n\t" \
    "v_exp_f32 v91, v91\n\t" \
    "v_mfma_f32_32x32x16_bf16 v[96:111], v[172:175], v[132:135], v[96:111]\n\t" \
    "ds_read_b128 v[176:179], %[va] offset:4608\n\t" \
    "v_cvt_pk_bf16_f32 v141, v90, v91\n\t" \
    "v_exp_f32 v92, v92\n\t" \
    "v_exp_f32 v93, v93\n\t" \
    "v_exp_f32 v94, v94\n\t" \
    "v_exp_f32 v95, v95\n\t" \
    "v_cvt_pk_bf16_f32 v142, v92, v93\n\t" \
    "v_cvt_pk_bf16_f32 v143, v94, v95\n\t" \
    "v_mfma_f32_32x32x16_bf16 v[80:95], v[160:163], v[148:151], 0\n\t" \
    "ds_read_b128 v[180:183], %[va] offset:5632\n\t" \
    "v_exp_f32 v64, v64\n\t" \
    "v_exp_f32 v65, v65\n\t" \
    "v_exp_f32 v66, v66\n\t" \
    "v_cvt_pk_bf16_f32 v128, v64, v65\n\t" \
    "v_exp_f32 v67, v67\n\t" \
    "v_mfma_f32_32x32x8_bf16 v[80:95], v[166:167], v[154:155], v[80:95]\n\t" \
    "v_cvt_pk_bf16_f32 v129, v66, v67\n\t" \
    "v_exp_f32 v68, v68\n\t" \
    "v_exp_f32 v69, v69\n\t" \
    "v_exp_f32 v70, v70\n\t" \
    "v_cvt_pk_bf16_f32 v130, v68, v69\n\t" \
    "v_exp_f32 v71, v71\n\t" \
    "v_mfma_f32_32x32x16_bf16 v[112:127], v[168:171], v[136:139], v[112:127]\n\t" \
    "v_cvt_pk_bf16_f32 v131, v70, v71\n\t" \
    "v_exp_f32 v72, v72\n\t" \
    "v_exp_f32 v73, v73\n\t" \
    "v_exp_f32 v74, v74\n\t" \
    "v_cvt_pk_bf16_f32 v132, v72, v73\n\t" \
    "v_exp_f32 v75, v75\n\t" \
    "v_mfma_f32_32x32x16_bf16 v[112:127], v[172:175], v[140:143], v[112:127]\n\t" \
    "s_waitcnt lgkmcnt(0)\n\t" \
    "v_cvt_pk_bf16_f32 v133, v74, v75\n\t" \
    "v_exp_f32 v76, v76\n\t" \
    "v_exp_f32 v77, v77\n\t" \
    "v_exp_f32 v78, v78\n\t" \
    "v_exp_f32 v79, v79\n\t" \
    "v_cvt_pk_bf16_f32 v134, v76, v77\n\t" \
    "v_cvt_pk_bf16_f32 v135, v78, v79\n\t" \
    "v_mfma_f32_32x32x16_bf16 v[64:79], v[156:159], v[144:147], 0\n\t" \
    "ds_read_b128 v[160:163], %[va] offset:6656\n\t" \
    "v_exp_f32 v80, v80\n\t" \
    "v_exp_f32 v81, v81\n\t" \
    "v_exp_f32 v82, v82\n\t" \
    "v_cvt_pk_bf16_f32 v136, v80, v81\n\t" \
    "v_exp_f32 v83, v83\n\t" \
    "v_mfma_f32_32x32x8_bf16 v[64:79], v[164:165], v[152:153], v[64:79]\n\t" \
    "v_cvt_pk_bf16_f32 v137, v82, v83\n\t" \
    "v_exp_f32 v84, v84\n\t" \
    "v_exp_f32 v85, v85\n\t" \
    "v_exp_f32 v86, v86\n\t" \
    "v_cvt_pk_bf16_f32 v138, v84, v85\n\t" \
    "v_exp_f32 v87, v87\n\t" \
    "v_mfma_f32_32x32x16_bf16 v[96:111], v[176:179], v[128:131], v[96:111]\n\t" \
    "ds_read_b64 v[166:167], %[vb] offset:7680\n\t" \
    "v_cvt_pk_bf16_f32 v139, v86, v87\n\t" \
    "v_exp_f32 v88, v88\n\t" \
    "v_exp_f32 v89, v89\n\t" \
    "v_exp_f32 v90, v90\n\t" \
    "v_cvt_pk_bf16_f32 v140, v88, v89\n\t" \
    "v_exp_f32 v91, v91\n\t" \
    "v_mfma_f32_32x32x16_bf16 v[96:111], v[180:183], v[132:135], v[96:111]\n\t" \
    "ds_read_b128 v[168:171], %[va] offset:7936\n\t" \
    "v_cvt_pk_bf16_f32 v141, v90, v91\n\t" \
    "v_exp_f32 v92, v92\n\t" \
    "v_exp_f32 v93, v93\n\t" \
    "v_exp_f32 v94, v94\n\t" \
    "v_exp_f32 v95, v95\n\t" \
    "v_cvt_pk_bf16_f32 v142, v92, v93\n\t" \
    "v_cvt_pk_bf16_f32 v143, v94, v95\n\t" \
    "v_mfma_f32_32x32x16_bf16 v[80:95], v[156:159], v[148:151], 0\n\t" \
    "ds_read_b128 v[172:175], %[va] offset:8960\n\t" \
    "v_exp_f32 v64, v64\n\t" \
    "v_exp_f32 v65, v65\n\t" \
    "v_exp_f32 v66, v66\n\t" \
    "v_cvt_pk_bf16_f32 v128, v64, v65\n\t" \
    "v_exp_f32 v67, v67\n\t" \
    "v_mfma_f32_32x32x8_bf16 v[80:95], v[164:165], v[154:155], v[80:95]\n\t" \
    "v_cvt_pk_bf16_f32 v129, v66, v67\n\t" \
    "v_exp_f32 v68, v68\n\t" \
    "v_exp_f32 v69, v69\n\t" \
    "v_exp_f32 v70, v70\n\t" \
    "v_cvt_pk_bf16_f32 v130, v68, v69\n\t" \
    "v_exp_f32 v71, v71\n\t" \
    "v_mfma_f32_32x32x16_bf16 v[112:127], v[176:179], v[136:139], v[112:127]\n\t" \
    "v_cvt_pk_bf16_f32 v131, v70, v71\n\t" \
    "v_exp_f32 v72, v72\n\t" \
    "v_exp_f32 v73, v73\n\t" \
    "v_exp_f32 v74, v74\n\t" \
    "v_cvt_pk_bf16_f32 v132, v72, v73\n\t" \
    "v_exp_f32 v75, v75\n\t" \
    "v_mfma_f32_32x32x16_bf16 v[112:127], v[180:183], v[140:143], v[112:127]\n\t" \
    "s_waitcnt lgkmcnt(0)\n\t" \
    "v_cvt_pk_bf16_f32 v133, v74, v75\n\t" \
    "v_exp_f32 v76, v76\n\t" \
    "v_exp_f32 v77, v77\n\t" \
    "v_exp_f32 v78, v78\n\t" \
    "v_exp_f32 v79, v79\n\t" \
    "v_cvt_pk_bf16_f32 v134, v76, v77\n\t" \
    "v_cvt_pk_bf16_f32 v135, v78, v79\n\t" \
    "v_mfma_f32_32x32x16_bf16 v[64:79], v[160:163], v[144:147], 0\n\t" \
    "ds_read_b128 v[156:159], %[va] offset:9984\n\t" \
    "v_exp_f32 v80, v80\n\t" \
    "v_exp_f32 v81, v81\n\t" \
    "v_exp_f32 v82, v82\n\t" \
    "v_cvt_pk_bf16_f32 v136, v80, v81\n\t" \
    "v_exp_f32 v83, v83\n\t" \
    "v_mfma_f32_32x32x8_bf16 v[64:79], v[166:167], v[152:153], v[64:79]\n\t" \
    "v_cvt_pk_bf16_f32 v137, v82, v83\n\t" \
    "v_exp_f32 v84, v84\n\t" \
    "v_exp_f32 v85, v85\n\t" \
    "v_exp_f32 v86, v86\n\t" \
    "v_cvt_pk_bf16_f32 v138, v84, v85\n\t" \
    "v_exp_f32 v87, v87\n\t" \
    "v_mfma_f32_32x32x16_bf16 v[96:111], v[168:171], v[128:131], v[96:111]\n\t" \
    "ds_read_b64 v[164:165], %[vb] offset:11008\n\t" \
    "v_cvt_pk_bf16_f32 v139, v86, v87\n\t" \
    "v_exp_f32 v88, v88\n\t" \
    "v_exp_f32 v89, v89\n\t" \
    "v_exp_f32 v90, v90\n\t" \
    "v_cvt_pk_bf16_f32 v140, v88, v89\n\t" \
    "v_exp_f32 v91, v91\n\t" \
    "v_mfma_f32_32x32x16_bf16 v[96:111], v[172:175], v[132:135], v[96:111]\n\t" \
    "ds_read_b128 v[176:179], %[va] offset:11264\n\t" \
    "v_cvt_pk_bf16_f32 v141, v90, v91\n\t" \
    "v_exp_f32 v92, v92\n\t" \
    "v_exp_f32 v93, v93\n\t" \
    "v_exp_f32 v94, v94\n\t" \
    "v_exp_f32 v95, v95\n\t" \
    "v_cvt_pk_bf16_f32 v142, v92, v93\n\t" \
    "v_cvt_pk_bf16_f32 v143, v94, v95\n\t" \
    "v_mfma_f32_32x32x16_bf16 v[80:95], v[160:163], v[148:151], 0\n\t" \
    "ds_read_b128 v[180:183], %[va] offset:12288\n\t" \
    "v_exp_f32 v64, v64\n\t" \
    "v_exp_f32 v65, v65\n\t" \
    "v_exp_f32 v66, v66\n\t" \
    "v_cvt_pk_bf16_f32 v128, v64, v65\n\t" \
    "v_exp_f32 v67, v67\n\t" \
    "v_mfma_f32_32x32x8_bf16 v[80:95], v[166:167], v[154:155], v[80:95]\n\t" \
    "v_cvt_pk_bf16_f32 v129, v66, v67\n\t" \
    "v_exp_f32 v68, v68\n\t" \
    "v_exp_f32 v69, v69\n\t" \
    "v_exp_f32 v70, v70\n\t" \
    "v_cvt_pk_bf16_f32 v130, v68, v69\n\t" \
    "v_exp_f32 v71, v71\n\t" \
    "v_mfma_f32_32x32x16_bf16 v[112:127], v[168:171], v[136:139], v[112:127]\n\t" \
    "s_sub_u32 %[cnt], %[cnt], 1\n\t" \
    "v_cvt_pk_bf16_f32 v131, v70, v71\n\t" \
    "v_exp_f32 v72, v72\n\t" \
    "v_exp_f32 v73, v73\n\t" \
    "v_exp_f32 v74, v74\n\t" \
    "v_cvt_pk_bf16_f32 v132, v72, v73\n\t" \
    "v_exp_f32 v75, v75\n\t" \
    "v_mfma_f32_32x32x16_bf16 v[112:127], v[172:175], v[140:143], v[112:127]\n\t" \
    "s_waitcnt lgkmcnt(0)\n\t" \
    "s_barrier\n\t" \
    "v_cvt_pk_bf16_f32 v133, v74, v75\n\t" \
    "v_exp_f32 v76, v76\n\t" \
    "v_exp_f32 v77, v77\n\t" \
    "v_exp_f32 v78, v78\n\t" \
    "v_exp_f32 v79, v79\n\t" \
    "v_cvt_pk_bf16_f32 v134, v76, v77\n\t" \
    "v_cvt_pk_bf16_f32 v135, v78, v79\n\t" \
    "v_xor_b32 %[va], 0x4000, %[va]\n\t" \
    "v_xor_b32 %[vb], 0x4000, %[vb]\n\t" \
    "s_cbranch_scc0 1b\n\t" \
    "v_exp_f32 v80, v80\n\t" \
    "v_exp_f32 v81, v81\n\t" \
    "v_exp_f32 v82, v82\n\t" \
    "v_cvt_pk_bf16_f32 v136, v80, v81\n\t" \
    "v_exp_f32 v83, v83\n\t" \
    "s_nop 0\n\t" \
    "v_cvt_pk_bf16_f32 v137, v82, v83\n\t" \
    "v_exp_f32 v84, v84\n\t" \
    "v_exp_f32 v85, v85\n\t" \
    "v_exp_f32 v86, v86\n\t" \
    "v_cvt_pk_bf16_f32 v138, v84, v85\n\t" \
    "v_exp_f32 v87, v87\n\t" \
    "v_mfma_f32_32x32x16_bf16 v[96:111], v[176:179], v[128:131], v[96:111]\n\t" \
    "v_cvt_pk_bf16_f32 v139, v86, v87\n\t" \
    "v_exp_f32 v88, v88\n\t" \
    "v_exp_f32 v89, v89\n\t" \
    "v_exp_f32 v90, v90\n\t" \
    "v_cvt_pk_bf16_f32 v140, v88, v89\n\t" \
    "v_exp_f32 v91, v91\n\t" \
    "v_mfma_f32_32x32x16_bf16 v[96:111], v[180:183], v[132:135], v[96:111]\n\t" \
    "v_cvt_pk_bf16_f32 v141, v90, v91\n\t" \
    "v_exp_f32 v92, v92\n\t" \
    "v_exp_f32 v93, v93\n\t" \
    "v_exp_f32 v94, v94\n\t" \
    "v_exp_f32 v95, v95\n\t" \
    "v_cvt_pk_bf16_f32 v142, v92, v93\n\t" \
    "v_cvt_pk_bf16_f32 v143, v94, v95\n\t" \
    "s_nop 1\n\t" \
    "v_mfma_f32_32x32x16_bf16 v[112:127], v[176:179], v[136:139], v[112:127]\n\t" \
    "v_mfma_f32_32x32x16_bf16 v[112:127], v[180:183], v[140:143], v[112:127]\n\t" \
    "s_nop 15\n\t"

#define WARM1 1
#define WARM2 1
constexpr int L_WO = 40960, L_X = 143360, M_LDS = 163840;
constexpr int KV_SLOT = 16384, KV_LREC = 3328;

DI int up_start(int c) { return 27 * (c >> 2) + 7 * (c & 3); }

DI void attn_slow(const char* kv, int lane, u32x4 q16, u32x2 q8, f32x16& o) {
    const int hi = lane >> 5, l31 = lane & 31;
    const unsigned fill = l31 == 20 ? 0x3f803f80u : 0u;
    o = f32x16{};
    float m = -3.0e38f;
#pragma unroll 1
    for (int t = 0; t < 64; ++t) {
        const char* r = kv + t * KVREC;
        const u32x4 k16 = *(const u32x4*)(r + lane * 16);
        const u32x2 k8 = *(const u32x2*)(r + 1024 + l31 * 8);
        u32x4 v0 = {fill, fill, fill, fill}, v1 = v0;
        if (l31 < 20) { v0 = *(const u32x4*)(r + 1280 + hi * 320 + l31 * 16); v1 = *(const u32x4*)(r + 1920 + hi * 320 + l31 * 16); }
        f32x16 s = mfma32k8(k8, q8, mfma32(k16, q16, f32x16{}));
        float mx = s[0];
#pragma unroll
        for (int i = 1; i < 16; ++i) mx = fmaxf(mx, s[i]);
        const float mn = fmaxf(m, swap32_max(mx));
        const float al = __builtin_amdgcn_exp2f(m - mn);
        m = mn;
        u32x4 p0, p1;
#pragma unroll
        for (int i = 0; i < 4; ++i) {
            p0[i] = pk2(__builtin_amdgcn_exp2f(s[2 * i] - mn), __builtin_amdgcn_exp2f(s[2 * i + 1] - mn));
            p1[i] = pk2(__builtin_amdgcn_exp2f(s[8 + 2 * i] - mn), __builtin_amdgcn_exp2f(s[9 + 2 * i] - mn));
        }
#pragma unroll
        for (int i = 0; i < 16; ++i) o[i] *= al;
        o = mfma32(v1, p1, mfma32(v0, p0, o));
    }
}

constexpr float GELU_C0 = -2.0f * 0.7978845608028654f * 1.4426950408889634f, GELU_C1 = 0.044715f * GELU_C0;

typedef __attribute__((address_space(1))) unsigned gu32;
typedef __attribute__((address_space(1))) unsigned long long gu64;
typedef unsigned long long u64;
DI void flag_set(char* ws, int off, u64 tag) { __hip_atomic_store((gu64*)(ws + off), tag, __ATOMIC_RELAXED, __HIP_MEMORY_SCOPE_AGENT); }
DI u64 flag_get(char* ws, int off) { return __hip_atomic_load((gu64*)(ws + off), __ATOMIC_RELAXED, __HIP_MEMORY_SCOPE_AGENT); }
DI void flag_wait(char* ws, int off, u64 tag) {
#pragma unroll 1
    for (int spin = 0; spin < (1 << 20); ++spin) {
        if (__all(flag_get(ws, off) == tag)) break;
        __builtin_amdgcn_s_sleep(2);
    }
}
DI void flag_wait2(char* ws, int off0, int off1, u64 tag) {
#pragma unroll 1
    for (int spin = 0; spin < (1 << 20); ++spin) {
        const u64 u = flag_get(ws, off0), v = flag_get(ws, off1);
        if (__all(u == tag && v == tag)) break;
        __builtin_amdgcn_s_sleep(2);
    }
}

constexpr int nld(int k, int first2, int per) { return k >= first2 ? 2 * per : per; }
constexpr int nld_o(int k) { return k % 5 == 4 ? 2 : 1; }

DI void attn_consumer(char* ws, lptr lds, int b, int w, int lane, u64 tag STARG) {
    const int hi = lane >> 5, l31 = lane & 31, tk = lane & 15;
    const unsigned vl = lane * 16;
    const int head = 4 * (b & 7) + ((b >> 3) & 3), tta = 8 * (b >> 5) + 2 * w;
    const char* qa = ws + WS_Q + (size_t)(head * 64 + tta) * QREC;
    const char* kv = ws + WS_KV + (size_t)head * 64 * KVREC;
    const unsigned v8 = 1024 + l31 * 8;
    u32x4 QA16, QB16, K16a, V0a = {}, V1a = {}, V0b = {}, V1b = {};
    u32x2 QA8, QB8, K8a;
    f32x16 SB, OA = {}, OB = {};
    u32x8 PA = {}, PB = {};
#pragma unroll
    for (int i = 0; i < 16; ++i) SB[i] = -1.0e30f;
    asm volatile("" : "+{v[80:95]}"(SB), "+{v[96:111]}"(OA), "+{v[112:127]}"(OB), "+{v[128:135]}"(PA), "+{v[136:143]}"(PB),
                      "+{v[168:171]}"(V0a), "+{v[172:175]}"(V1a), "+{v[176:179]}"(V0b), "+{v[180:183]}"(V1b));
    if (w == 0) {
        const int ca = (5 * head) >> 5, cb = (5 * head + 4) >> 5, cs = (lane & 16) ? cb : ca;
        flag_wait2(ws, FL_A + ((5 + 5 * hi + cs) * 16 + tk) * 128, FL_A + (cs * 16 + 2 * (b >> 5) + (lane & 1)) * 128, tag);
    }
    ST(5);
    lds_bar();
    ST(6);
    gld128s<0>(QA16, vl, qa); gld64s<0>(QA8, v8, qa);
    gld128s<QREC>(QB16, vl, qa); gld64s<QREC>(QB8, v8, qa);
    gld128s<0>(K16a, vl, kv); gld64s<0>(K8a, v8, kv);
    asm volatile("s_waitcnt vmcnt(0)" : "+v"(QA16), "+v"(QA8), "+v"(QB16), "+v"(QB8), "+v"(K16a), "+v"(K8a)::"memory");
    ST(7);
    if (hi) { QA8 = u32x2{0u, 0u}; QB8 = u32x2{0u, 0u}; }
    f32x16 SA = mfma32k8(K8a, QA8, mfma32(K16a, QA16, f32x16{}));
    f32x16 ST0 = mfma32k8(K8a, QB8, mfma32(K16a, QB16, f32x16{}));
    float ma = SA[0], mb = ST0[0];
#pragma unroll
    for (int i = 1; i < 16; ++i) { ma = fmaxf(ma, SA[i]); mb = fmaxf(mb, ST0[i]); }
    ma = swap32_max(ma); mb = swap32_max(mb);
    u32x2 RA8 = QA8, RB8 = QB8;
    if (hi) { RA8[0] = pk2(-ma, rbf(ma) - ma); RB8[0] = pk2(-mb, rbf(mb) - mb); K8a = u32x2{0x3f803f80u, 0x3f803f80u}; }
    unsigned va = (unsigned)(size_t)lds + vl, vb = (unsigned)(size_t)lds + (hi ? 576 : l31 * 8), cnt = 15;
    lds_bar();
    ST(8);
    asm volatile(ATTN_LOOP_ASM
                 : "+{v[64:79]}"(SA), "+{v[80:95]}"(SB), "+{v[96:111]}"(OA), "+{v[112:127]}"(OB), "+{v[128:135]}"(PA), "+{v[136:143]}"(PB),
                   "+{v[156:159]}"(K16a), "+{v[164:165]}"(K8a), "+{v[168:171]}"(V0a), "+{v[172:175]}"(V1a), "+{v[176:179]}"(V0b), "+{v[180:183]}"(V1b),
                   [va] "+v"(va), [vb] "+v"(vb), [cnt] "+s"(cnt)
                 : "{v[144:147]}"(QA16), "{v[148:151]}"(QB16), "{v[152:153]}"(RA8), "{v[154:155]}"(RB8)
                 : "memory", "scc", "v160", "v161", "v162", "v163", "v166", "v167");
    ST(9);
    bool ok = !hi || (OA[8] <= 1.8e19f && OB[8] <= 1.8e19f && OA[8] >= 1.0e-9f && OB[8] >= 1.0e-9f);
#pragma unroll
    for (int i = 0; i < 12; ++i) ok = ok && fabsf(OA[i]) <= 3.0e38f && fabsf(OB[i]) <= 3.0e38f;
    if (__builtin_expect(__any(!ok), 0)) {
        __builtin_amdgcn_fence(__ATOMIC_ACQUIRE, "agent");
        attn_slow(kv, lane, QA16, QA8, OA);
        attn_slow(kv, lane, QB16, QB8, OB);
    }
    const int u = 2 * (head & 7) + hi;
    const unsigned vo = (l31 >> 4) * 20480 + (4 * (head >> 3) + (u >> 2)) * 1024 + ((u & 3) * 16 + tk) * 16;
    const unsigned vs = (l31 >> 4) * 20480 + 16384 + head * 128 + tk * 8;
    sfor<0, 2>([&](auto T) {
        const f32x16& O = T.value == 0 ? OA : OB;
        auto r = __builtin_amdgcn_permlane32_swap(__float_as_uint(O[8]), __float_as_uint(O[8]), false, false);
        const float inv = __builtin_amdgcn_rcpf(__uint_as_float(r[1]));
        const char* ob = ws + WS_O + (size_t)(2 * tta + 2 * T.value) * 20480;
        gst128(vo, ob, u32x4{pk2(O[0] * inv, O[1] * inv), pk2(O[2] * inv, O[3] * inv), pk2(O[4] * inv, O[5] * inv), pk2(O[6] * inv, O[7] * inv)});
        if (hi == 0) gst64(vs, ob, u32x2{pk2(O[8] * inv, O[9] * inv), pk2(O[10] * inv, O[11] * inv)});
    });
}

#define MF16(d, a, b) "v_mfma_f32_16x16x32_bf16 %[" #d "], %[" #a "], %[" #b "], %[" #d "]\n\t"
#define DSR(d, p, o) "ds_read_b128 %[" #d "], %[" #p "] offset:%c[off]+" #o "\n\t"
template <int OFF, int VM>
DI void step5(f32x4 (&acc)[5], const u32x4 (&c)[5], u32x4 (&n)[5], u32x4 a, unsigned pa, unsigned pb, unsigned pc) {
    asm volatile("s_waitcnt vmcnt(%c[vm]) lgkmcnt(2)\n\t"
                 MF16(d0, c0, a) DSR(n0, pa, 0) DSR(n1, pa, 1024) MF16(d1, c1, a) DSR(n2, pb, 0) DSR(n3, pb, 1024) MF16(d2, c2, a) DSR(n4, pc, 0)
                 "s_waitcnt lgkmcnt(5)\n\t" MF16(d3, c3, a) MF16(d4, c4, a)
                 : [d0] "+v"(acc[0]), [d1] "+v"(acc[1]), [d2] "+v"(acc[2]), [d3] "+v"(acc[3]), [d4] "+v"(acc[4]),
                   [n0] "=&v"(n[0]), [n1] "=&v"(n[1]), [n2] "=&v"(n[2]), [n3] "=&v"(n[3]), [n4] "=&v"(n[4])
                 : [c0] "v"(c[0]), [c1] "v"(c[1]), [c2] "v"(c[2]), [c3] "v"(c[3]), [c4] "v"(c[4]), [a] "v"(a),
                   [pa] "v"(pa), [pb] "v"(pb), [pc] "v"(pc), [off] "i"(OFF), [vm] "i"(VM)
                 : "memory");
}
template <int VM>
DI void last5(f32x4 (&acc)[5], const u32x4 (&c)[5], u32x4 a) {
    asm volatile("s_waitcnt vmcnt(%c[vm]) lgkmcnt(0)\n\t" MF16(d0, c0, a) MF16(d1, c1, a) MF16(d2, c2, a) MF16(d3, c3, a) MF16(d4, c4, a) "s_nop 7"
                 : [d0] "+v"(acc[0]), [d1] "+v"(acc[1]), [d2] "+v"(acc[2]), [d3] "+v"(acc[3]), [d4] "+v"(acc[4])
                 : [c0] "v"(c[0]), [c1] "v"(c[1]), [c2] "v"(c[2]), [c3] "v"(c[3]), [c4] "v"(c[4]), [a] "v"(a), [vm] "i"(VM)
                 : "memory");
}
template <int OFF> DI void first5(u32x4 (&n)[5], unsigned pa, unsigned pb, unsigned pc) {
    asm volatile(DSR(n0, pa, 0) DSR(n1, pa, 1024) DSR(n2, pb, 0) DSR(n3, pb, 1024) DSR(n4, pc, 0)
                 : [n0] "=&v"(n[0]), [n1] "=&v"(n[1]), [n2] "=&v"(n[2]), [n3] "=&v"(n[3]), [n4] "=&v"(n[4])
                 : [pa] "v"(pa), [pb] "v"(pb), [pc] "v"(pc), [off] "i"(OFF) : "memory");
}
template <int OFF, int VM, bool RD>
DI void step7(f32x4 (&acc)[2][7], const u32x4 (&c)[7], u32x4 (&n)[7], u32x4 a, u32x4 b, unsigned p) {
    if constexpr (RD) {
        asm volatile("s_waitcnt vmcnt(%c[vm]) lgkmcnt(3)\n\t"
                     MF16(d0, c0, a) DSR(n0, p, 0) MF16(e0, c0, b) MF16(d1, c1, a) DSR(n1, p, 1024) MF16(e1, c1, b)
                     MF16(d2, c2, a) DSR(n2, p, 2048) MF16(e2, c2, b) MF16(d3, c3, a) DSR(n3, p, 3072) MF16(e3, c3, b)
                     : [d0] "+v"(acc[0][0]), [d1] "+v"(acc[0][1]), [d2] "+v"(acc[0][2]), [d3] "+v"(acc[0][3]),
                       [e0] "+v"(acc[1][0]), [e1] "+v"(acc[1][1]), [e2] "+v"(acc[1][2]), [e3] "+v"(acc[1][3]),
                       [n0] "=&v"(n[0]), [n1] "=&v"(n[1]), [n2] "=&v"(n[2]), [n3] "=&v"(n[3])
                     : [c0] "v"(c[0]), [c1] "v"(c[1]), [c2] "v"(c[2]), [c3] "v"(c[3]), [a] "v"(a), [b] "v"(b), [p] "v"(p), [off] "i"(OFF), [vm] "i"(VM)
                     : "memory");
        asm volatile("s_waitcnt lgkmcnt(4)\n\t"
                     MF16(d0, c0, a) DSR(n0, p, 4096) MF16(e0, c0, b) MF16(d1, c1, a) DSR(n1, p, 5120) MF16(e1, c1, b)
                     MF16(d2, c2, a) DSR(n2, p, 6144) MF16(e2, c2, b)
                     : [d0] "+v"(acc[0][4]), [d1] "+v"(acc[0][5]), [d2] "+v"(acc[0][6]), [e0] "+v"(acc[1][4]), [e1] "+v"(acc[1][5]), [e2] "+v"(acc[1][6]),
                       [n0] "=&v"(n[4]), [n1] "=&v"(n[5]), [n2] "=&v"(n[6])
                     : [c0] "v"(c[4]), [c1] "v"(c[5]), [c2] "v"(c[6]), [a] "v"(a), [b] "v"(b), [p] "v"(p), [off] "i"(OFF)
                     : "memory");
    } else {
        asm volatile("s_waitcnt vmcnt(%c[vm]) lgkmcnt(0)\n\t"
                     MF16(d0, c0, a) MF16(e0, c0, b) MF16(d1, c1, a) MF16(e1, c1, b) MF16(d2, c2, a) MF16(e2, c2, b) MF16(d3, c3, a) MF16(e3, c3, b)
                     : [d0] "+v"(acc[0][0]), [d1] "+v"(acc[0][1]), [d2] "+v"(acc[0][2]), [d3] "+v"(acc[0][3]),
                       [e0] "+v"(acc[1][0]), [e1] "+v"(acc[1][1]), [e2] "+v"(acc[1][2]), [e3] "+v"(acc[1][3])
                     : [c0] "v"(c[0]), [c1] "v"(c[1]), [c2] "v"(c[2]), [c3] "v"(c[3]), [a] "v"(a), [b] "v"(b), [vm] "i"(VM)
                     : "memory");
        asm volatile(MF16(d0, c0, a) MF16(e0, c0, b) MF16(d1, c1, a) MF16(e1, c1, b) MF16(d2, c2, a) MF16(e2, c2, b) "s_nop 7"
                     : [d0] "+v"(acc[0][4]), [d1] "+v"(acc[0][5]), [d2] "+v"(acc[0][6]), [e0] "+v"(acc[1][4]), [e1] "+v"(acc[1][5]), [e2] "+v"(acc[1][6])
                     : [c0] "v"(c[4]), [c1] "v"(c[5]), [c2] "v"(c[6]), [a] "v"(a), [b] "v"(b)
                     : "memory");
    }
}
template <int VM> DI u32x4 join2(u32x2& p, u32x2& q) {
    asm volatile("s_waitcnt vmcnt(%c2)" : "+v"(p), "+v"(q) : "i"(VM) : "memory");
    u32x4 a = {p[0], p[1], q[0], q[1]};
    asm volatile("s_nop 0" : "+v"(a));
    return a;
}

constexpr int L_CTR = L_X + 1024, L_ST = L_X + 2048;
#define H2_AT 2
#define UP_P 5
#define DN_P 6

DI void swap_halves(lptr lds, int w, int lane, f32x4 (&acc)[5]) {
    const int kh = w >> 2, tw = w & 3;
    const lptr mine = lds + L_X + tw * 5120 + kh * 2048 + lane * 16, other = lds + L_X + tw * 5120 + (1 - kh) * 2048 + lane * 16;
    *(LAS f32x4*)mine = acc[2];
    *(LAS f32x4*)(mine + 1024) = acc[3];
    if (kh) *(LAS f32x4*)(mine + 2048) = acc[4];
    lds_bar();
    acc[0] += *(LAS f32x4*)other;
    acc[1] += *(LAS f32x4*)(other + 1024);
    if (!kh) acc[4] += *(LAS f32x4*)(other + 2048);
}

DI void wo_phase(const u16* x, char* ws, lptr lds, int b, int w, int lane, u32x2 (&hk)[3], u32x4 (&held)[33], u64 tag STARG) {
    const int kh = w >> 2, tk = lane & 15, g = lane >> 4, cg = b >> 5;
    const int tt16 = 16 * (b & 7) + 4 * ((b >> 3) & 3) + (w & 3);
    const unsigned vl = lane * 16;
    u32x2 xr[3];
    const unsigned vxr = (16 * tt16 + tk) * 1280 + 8 * g;
    gld64<0>(xr[0], vxr, (const char*)x + cg * 160 + kh * 64); gld64<32>(xr[1], vxr, (const char*)x + cg * 160 + kh * 64);
    gld64<128>(xr[2], vxr, (const char*)x + cg * 160);
    ST(11);
    lds_bar();
    ST(12);
    const char* osrc = ws + WS_O + (size_t)tt16 * 20480;
    const unsigned vs = g * 256 + tk * 8;
    constexpr int P = 5;
    u32x4 of[8]; u32x2 os[2][2];
    auto ld = [&](auto K_) {
        constexpr int K = K_.value, G = K / 5;
        if constexpr (K % 5 < 4) gld128s<(K % 5) * 1024>(of[K - G], vl, osrc + (2 * kh + G) * 4096);
        else { gld64s<G * 1024>(os[G][0], vs, osrc + 16384 + kh * 2048); gld64s<G * 1024 + 128>(os[G][1], vs, osrc + 16384 + kh * 2048); }
    };
    sfor<0, P>(ld);
    const unsigned wb = (unsigned)(size_t)lds + L_WO + kh * 51200 + vl, pa = wb + kh * 2048, pb = wb + (1 - kh) * 2048, pc = wb + 4096;
    f32x4 acc[5] = {};
    u32x4 wf[2][5];
    sfor<0, 5>([&](auto I) { pin(acc[I.value]); });
    first5<0>(wf[0], pa, pb, pc);
    sfor<0, 10>([&](auto K_) {
        constexpr int K = K_.value;
        constexpr int N = [] { int n = 0; for (int j = K + 1; j < K + P && j < 10; ++j) n += nld_o(j); return n; }();
        u32x4 a;
        if constexpr (K % 5 < 4) a = of[K - K / 5]; else a = join2<N>(os[K / 5][0], os[K / 5][1]);
        if constexpr (K < 9) step5<(K + 1) * 5120, N>(acc, wf[K & 1], wf[(K + 1) & 1], a, pa, pb, pc);
        else last5<N>(acc, wf[K & 1], a);
        if constexpr (K + P < 10) ld(IC<K + P>{});
    });
    ST(13);
    swap_halves(lds, w, lane, acc);
    float s1 = 0.f, s2 = 0.f;
#pragma unroll
    for (int i = 0; i < 3; ++i) {
        pin(xr[i]);
        const f32x4 v = acc[i == 2 ? 4 : i];
        const unsigned a0 = pk2(v[0], v[1]), a1 = pk2(v[2], v[3]), x0 = xr[i][0], x1 = xr[i][1];
        unsigned h0 = pk2(bflo(x0) + bflo(a0), bfhi(x0) + bfhi(a0)), h1 = pk2(bflo(x1) + bflo(a1), bfhi(x1) + bfhi(a1));
        if (i == 2 && kh) { h0 = 0u; h1 = 0u; }
        hk[i] = u32x2{h0, h1};
        s1 = dot2(h0, 0x3f803f80u, dot2(h1, 0x3f803f80u, s1));
        s2 = dot2(h0, h0, dot2(h1, h1, s2));
    }
    const char* hb = ws + WS_H + (size_t)tt16 * 20480;
    gst128(vl, hb + (2 * cg + kh) * 1024, u32x4{hk[0][0], hk[0][1], hk[1][0], hk[1][1]});
    if (!kh) gst64(lane * 8, hb + 16384 + cg * 512, hk[2]);
    s1 = swap32_add(swap16_add(s1)); s2 = swap32_add(swap16_add(s2));
    if (g == 0) gst64(tk * 8, ws + WS_ST + (size_t)(tt16 * 16 + 2 * cg + kh) * 128, u32x2{__float_as_uint(s1), __float_as_uint(s2)});
    if (kh) {
        const lptr d = lds + (7 * (1 + w)) * 1024 + vl;
        sfor<0, 21>([&](auto I) { *(LAS u32x4*)(d + (28 * (I.value / 7) + I.value % 7) * 1024) = held[I.value]; });
        if (w < 7) sfor<21, 28>([&](auto I) { *(LAS u32x4*)(d + (84 + I.value % 7) * 1024) = held[I.value]; });
    }
    ST(14);
    wait_vm_bar<0>();
    if (w == 0) {
        if (lane == 0) flag_set(ws, FL_C + ((2 * (b & 7) + ((b >> 4) & 1)) * 16 + ((b >> 3) & 1) * 8 + cg) * 128, tag);
        flag_wait(ws, FL_C + ((2 * (b & 7) + ((b >> 3) & 1)) * 16 + tk) * 128, tag);
    }
}

DI void up_consumer(char* ws, lptr lds, int b, int w, int lane, u64 tag STARG) {
    const int tk = lane & 15, g = lane >> 4;
    const unsigned vl = lane * 16;
    ST(15);
    lds_bar();
    ST(16);
    const int t0 = 16 * (b & 7) + 8 * ((b >> 3) & 1) + 2 * w, c = b >> 4;
    const char* hsrc = ws + WS_H + (size_t)t0 * 20480;
    constexpr int P = UP_P;
    u32x4 hf[16][2]; u32x2 hs[4][2][2];
    auto ld = [&](auto K_) {
        constexpr int K = K_.value;
        sfor<0, 2>([&](auto T) {
            if constexpr (K < 16) gld128s<(K & 3) * 1024>(hf[K][T.value], vl, hsrc + T.value * 20480 + (K >> 2) * 4096);
            else { gld64s<(K - 16) * 1024>(hs[K - 16][T.value][0], lane * 8, hsrc + T.value * 20480 + 16384); gld64s<(K - 16) * 1024 + 512>(hs[K - 16][T.value][1], lane * 8, hsrc + T.value * 20480 + 16384); }
        });
    };
    sfor<0, P>(ld);
    f32x4 acc[2][7] = {};
    u32x4 wf[2][7];
    const unsigned p0 = (unsigned)(size_t)lds + vl;
    sfor<0, 7>([&](auto I) { wf[0][I.value] = lds128(lds + I.value * 1024 + vl); pin(acc[0][I.value]); pin(acc[1][I.value]); });
    sfor<0, 7>([&](auto I) { pin(wf[0][I.value]); });
    sfor<0, 20>([&](auto K_) {
        constexpr int K = K_.value, NX = K + 1, SEG = NX / 8;
        constexpr int N = [] { int n = 0; for (int j = K + 1; j < K + P && j < 20; ++j) n += nld(j, 16, 2); return n; }();
        u32x4 a0, a1;
        if constexpr (K < 16) { a0 = hf[K][0]; a1 = hf[K][1]; }
        else { a0 = join2<N>(hs[K - 16][0][0], hs[K - 16][0][1]); a1 = join2<N>(hs[K - 16][1][0], hs[K - 16][1][1]); }
        step7<(NX - 8 * SEG) * 7168, N, (K < 19)>(acc, wf[K & 1], wf[NX & 1], a0, a1, p0 + SEG * 57344);
        if constexpr (K + P < 20) ld(IC<K + P>{});
        if constexpr (K == 10 || K == 18) lds_bar();
    });
    ST(17);
    f32x4 cs[7], bv[7];
#pragma unroll
    for (int i = 0; i < 7; ++i) { cs[i] = *(LAS f32x4*)(lds + L_X + (16 * i + 4 * g) * 4); bv[i] = *(LAS f32x4*)(lds + L_X + 512 + (16 * i + 4 * g) * 4); }
    float mr[2], rs[2];
#pragma unroll
    for (int t = 0; t < 2; ++t) { const f32x2 v = *(LAS f32x2*)(lds + L_ST + (32 * w + 16 * t + tk) * 8); mr[t] = v[0]; rs[t] = v[1]; }
    u32x2 o[2][7];
    sfor<0, 7>([&](auto I) {
        constexpr int i = I.value;
        f32x2 a[4], z[4];
#pragma unroll
        for (int q = 0; q < 4; ++q) {
            const int t = q >> 1, e = 2 * (q & 1);
            a[q] = f32x2{acc[t][i][e], acc[t][i][e + 1]} * rs[t] + (f32x2{cs[i][e], cs[i][e + 1]} * mr[t] + f32x2{bv[i][e], bv[i][e + 1]});
        }
#pragma unroll
        for (int q = 0; q < 4; ++q) z[q] = a[q] * (a[q] * a[q] * GELU_C1 + GELU_C0);
#pragma unroll
        for (int q = 0; q < 4; ++q) z[q] = f32x2{__builtin_amdgcn_exp2f(z[q][0]), __builtin_amdgcn_exp2f(z[q][1])};
#pragma unroll
        for (int q = 0; q < 4; ++q) z[q] += 1.0f;
#pragma unroll
        for (int q = 0; q < 4; ++q) z[q] = f32x2{__builtin_amdgcn_rcpf(z[q][0]), __builtin_amdgcn_rcpf(z[q][1])};
#pragma unroll
        for (int q = 0; q < 4; ++q) { a[q] *= z[q]; o[q >> 1][i][q & 1] = pk2(a[q][0], a[q][1]); }
        pin(o[0][i]); pin(o[1][i]);
        SBAR();
        if constexpr (i & 1) sfor<0, 2>([&](auto T) {
            gst128(vl, ws + WS_G + (size_t)((t0 + T.value) * 54 + 3 * c + (i >> 1)) * 1024, u32x4{o[T.value][i - 1][0], o[T.value][i - 1][1], o[T.value][i][0], o[T.value][i][1]});
        });
    });
    if ((c & 3) != 3) sfor<0, 2>([&](auto T) { gst64(lane * 8, ws + WS_G + (size_t)((t0 + T.value) * 54 + 48) * 1024 + (3 * (c >> 2) + (c & 3)) * 512, o[T.value][6]); });
    ST(18);
    wait_vm<0>();
    unsigned old = 0;
    if (lane == 0) old = __hip_atomic_fetch_add((LAS unsigned*)(lds + L_CTR), 1u, __ATOMIC_RELAXED, __HIP_MEMORY_SCOPE_WORKGROUP);
    if (__builtin_amdgcn_readfirstlane(old) == 3u) {
        if (lane == 0) flag_set(ws, FL_D + ((2 * (b & 7) + ((b >> 3) & 1)) * 16 + c) * 128, tag);
        flag_wait(ws, FL_D + ((2 * (b & 7) + ((b >> 4) & 1)) * 16 + tk) * 128, tag);
    }
}

template <int KH>
DI void down_phase(const u16* b2, char* ws, u16* out, lptr lds, int b, int w, int lane, const u32x2 (&hk)[3], u32x4 (&held)[33] STARG) {
    const int tk = lane & 15, g = lane >> 4, cg = b >> 5;
    const int tt16 = 16 * (b & 7) + 4 * ((b >> 3) & 3) + (w & 3);
    const unsigned vl = lane * 16;
    u32x2 bv[3];
    gld64<KH * 64>(bv[0], 8 * g, (const char*)b2 + cg * 160); gld64<KH * 64 + 32>(bv[1], 8 * g, (const char*)b2 + cg * 160); gld64<128>(bv[2], 8 * g, (const char*)b2 + cg * 160);
    ST(19);
    if constexpr (KH) { wait_vm_bar<0>(); sfor<0, 33>([&](auto I) { pin(held[I.value]); }); } else lds_bar();
    ST(20);
    const char* gsrc = ws + WS_G + (size_t)tt16 * 54 * 1024;
    constexpr int P = DN_P;
    u32x4 gf[24]; u32x2 gs[3][2];
    auto ld = [&](auto S_) {
        constexpr int S = S_.value;
        if constexpr (S < 24) gld128s<(S & 3) * 1024>(gf[S], vl, gsrc + (6 * KH + (S >> 2)) * 4096);
        else { gld64s<(S - 24) * 1024>(gs[S - 24][0], lane * 8, gsrc + (48 + 3 * KH) * 1024); gld64s<(S - 24) * 1024 + 512>(gs[S - 24][1], lane * 8, gsrc + (48 + 3 * KH) * 1024); }
    };
    sfor<0, P>(ld);
    f32x4 acc[5] = {};
    u32x4 wf[2][5];
    const unsigned wb = (unsigned)(size_t)lds + KH * 71680 + vl, pa = wb + KH * 2048, pb = wb + (1 - KH) * 2048, pc = wb + 4096;
    sfor<0, 5>([&](auto I) { pin(acc[I.value]); });
    first5<0>(wf[0], pa, pb, pc);
    sfor<0, 27>([&](auto S_) {
        constexpr int S = S_.value, SL = (S + 1) % 14, UP = SL >= 12 ? 61440 : 0;
        constexpr int N = [] { int n = 0; for (int j = S + 1; j < S + P && j < 27; ++j) n += nld(j, 24, 1); return n; }();
        if constexpr (S == 6 || S == 13 || S == 20) lds_bar();
        if constexpr (KH && S == 6) {
            const lptr d = lds + ((w >> 1) & 1) * 71680 + (w & 1) * 17408 + vl;
            sfor<0, 18>([&](auto I) { *(LAS u32x4*)(d + I.value * 1024) = held[I.value]; });
        }
        if constexpr (KH && S == 13) {
            const lptr d = lds + ((w >> 1) & 1) * 71680 + 35840 + (w & 1) * 15360 + vl;
            sfor<0, 15>([&](auto I) { *(LAS u32x4*)(d + I.value * 1024) = held[18 + I.value]; });
        }
        u32x4 a;
        if constexpr (S < 24) a = gf[S]; else a = join2<N>(gs[S - 24][0], gs[S - 24][1]);
        if constexpr (S < 26) step5<SL * 5120 - UP, N>(acc, wf[S & 1], wf[(S + 1) & 1], a, pa + UP, pb + UP, pc + UP);
        else last5<N>(acc, wf[S & 1], a);
        if constexpr (S + P < 27) ld(IC<S + P>{});
    });
    ST(21);
    swap_halves(lds, w, lane, acc);
    char* ob = (char*)out + (size_t)(16 * tt16 + tk) * 1280 + cg * 160 + 8 * g;
#pragma unroll
    for (int i = 0; i < 3 - KH; ++i) {
        pin(bv[i]);
        const f32x4 v = acc[i == 2 ? 4 : i];
        const unsigned h0 = hk[i][0], h1 = hk[i][1];
        const unsigned y0 = pk2(v[0], v[1]), y1 = pk2(v[2], v[3]), b0 = bv[i][0], b1 = bv[i][1];
        const unsigned z0 = pk2(bflo(y0) + bflo(b0), bfhi(y0) + bfhi(b0)), z1 = pk2(bflo(y1) + bflo(b1), bfhi(y1) + bfhi(b1));
        *(u32x2*)(ob + (i == 2 ? 128 : KH * 64 + 32 * i)) = u32x2{pk2(bflo(h0) + bflo(z0), bfhi(h0) + bfhi(z0)), pk2(bflo(h1) + bflo(z1), bfhi(h1) + bfhi(z1))};
    }
}

DI void glds_run(unsigned vl, const char* src, int sstep, unsigned dst, int dstep, int n) {
#pragma unroll 1
    for (int i = 0; i < n; ++i) { glds_s(vl, src, dst); src += sstep; dst += dstep; }
}

constexpr bool kv_live(int i) { const int e = i % 208; return e < 80 || ((e - 80) & 31) < 20; }
constexpr unsigned kv_mask(int piece, int half) { unsigned m = 0; for (int l = 0; l < 32; ++l) m |= kv_live(piece * 64 + half * 32 + l) ? 1u << l : 0u; return m; }

template <int J>
DI void kv_loader(const char* kv, unsigned ldsb, int lane) {
    constexpr int P0 = J == 0 ? 0 : 7, NP = J == 0 ? 7 : 6;
    unsigned vo[NP];
#pragma unroll
    for (int p = 0; p < NP; ++p) {
        const int i = (P0 + p) * 64 + lane, r = i / 208, e = i - 208 * r, v = e - 80;
        vo[p] = r * KVREC + (e < 80 ? KVREC + e * 16 : 1280 + (v >> 5) * 320 + (v & 31) * 16);
    }
    auto issue = [&](int c) {
        const char* src = kv + c * 4 * KVREC;
        const unsigned dst = ldsb + (c & 1) * KV_SLOT;
        sfor<0, NP>([&](auto Pp) { glds_m<(P0 + Pp.value) * 1024, kv_mask(P0 + Pp.value, 0), kv_mask(P0 + Pp.value, 1)>(vo[Pp.value], src, dst); });
    };
    lds_bar();
    issue(0);
#pragma unroll 1
    for (int c = 0; c < 15; ++c) { wait_vm_bar<0>(); issue(c + 1); }
    wait_vm_bar<0>();
    lds_bar();
}

DI void retile_loader(const u16* wo, const u16* w1, const u16* w2, char* ws, lptr lds, int b, int j, int lane, u64 tag) {
    const unsigned ldsb = (unsigned)(size_t)lds, vl = lane * 16;
    const int first = j == 2 ? 0 : 3, nt = j == 2 ? 3 : 2;
    const int kind = b < 108 ? 1 : b < 148 ? 0 : 2;
    const char* src = kind == 0 ? (const char*)wo : kind == 1 ? (const char*)w1 : (const char*)w2;
    const int pitch = kind == 1 ? FF * 2 : DM * 2;
    const int rel = kind == 1 ? b : kind == 0 ? b - 108 : 5 * (b - 148), div = kind == 1 ? 27 : 10, quo = rel / div;
    auto task = [&](int i, int& kap, int& col64) {
        const int c = rel - quo * div + (kind == 2 ? i : 0), wrap = c >= 10 && kind == 2;
        col64 = wrap ? c - 10 : c;
        kap = kind == 2 ? quo + wrap : 5 * quo + i;
    };
    {
        const int l31 = lane & 31;
        const unsigned fill = l31 == 20 ? 0x3f803f80u : 0u;
        if (l31 >= 20) {
#pragma unroll
            for (int i = 0; i < 8; ++i) *(LAS u32x4*)(lds + (j - 2) * KV_SLOT + (i >> 1) * KV_LREC + 1280 + (i & 1) * 1024 + vl) = u32x4{fill, fill, fill, fill};
        }
    }
    {
        const int gl = lane >> 5, q = (lane >> 3) & 3, swz = ((lane & 7) ^ (2 * ((lane >> 4) & 3))) << 4;
#pragma unroll 1
        for (int i = first; i < first + nt; ++i) {
            int kap, col64;
            task(i, kap, col64);
            const int G = kap / 5, r = kap - 5 * G, c3 = kap / 3;
            int k0[2];
#pragma unroll
            for (int t = 0; t < 2; ++t) {
                const int lam = 2 * (kap - 48) + t, l3 = lam / 3;
                const int f1 = kap < 16 ? 5 * (kap >> 1) + 2 * (kap & 1) + t : 10 * (kap - 16) + 5 * t + 4;
                const int f2 = kap < 48 ? up_start(c3) + 2 * (kap - 3 * c3) + t : 27 * l3 + 7 * (lam - 3 * l3) + 6;
                const int r0 = r < 4 ? 160 * G + 40 * r + 8 * t : 160 * G + 20 * t + 16;
                k0[t] = kind == 0 ? r0 : 16 * (kind == 1 ? f1 : f2);
            }
            const int slo = kind == 0 && r == 4 ? 40 : 4, shi = kind == 0 ? (r == 4 ? 80 : 20) : 8;
            const unsigned v0 = (slo * gl + q) * pitch + swz;
            sfor<0, 4>([&](auto J) { glds<L_X + J.value * 1024>(v0, src + col64 * 128 + (k0[J.value >> 1] + (J.value & 1) * shi) * pitch, ldsb + i * 4096); });
        }
    }
    const int g = lane >> 4, q = (lane >> 2) & 3, p = lane & 3;
    lptr tp[4];
#pragma unroll
    for (int jj = 0; jj < 4; ++jj) { tp[jj] = lds + L_X + (4 * g + q) * 128 + 8 * (p & 1) + (((2 * jj + (p >> 1)) ^ (2 * (((4 * g + q) >> 1) & 3))) << 4); pin(tp[jj]); }
    lds_bar(); lds_bar(); lds_bar(); lds_bar();
    wait_vm<0>();
    f32x4 cs[4] = {};
#pragma unroll 1
    for (int i = first; i < first + 3; ++i) {
        if (i < 5) {
            int kap, col64;
            task(i, kap, col64);
            u32x4 f[4];
#pragma unroll
            for (int jj = 0; jj < 4; ++jj) {
                const u32x2 a = ldstr(tp[jj] + i * 4096), c = ldstr(tp[jj] + i * 4096 + 2048);
                f[jj] = u32x4{a[0], a[1], c[0], c[1]};
            }
#pragma unroll
            for (int jj = 0; jj < 4; ++jj) {
                const int ft = 4 * col64 + jj, f5 = ft / 5;
                const size_t piece = (kind == 0 ? WS_WO : kind == 1 ? WS_W1 : WS_W2) +
                                     (size_t)(kind == 1 ? ft * 20 + kap : (f5 * (kind == 0 ? 20 : 54) + kap) * 5 + ft - 5 * f5) * 1024;
                gst128(vl, ws + piece, f[jj]);
            }
            if (kind == 1) {
#pragma unroll
                for (int jj = 0; jj < 4; ++jj) cs[jj] = mfma16(f[jj], u32x4{0x3f803f80u, 0x3f803f80u, 0x3f803f80u, 0x3f803f80u}, cs[jj]);
            }
        }
        lds_bar();
    }
    asm volatile("s_nop 7" : "+v"(cs[0]), "+v"(cs[1]), "+v"(cs[2]), "+v"(cs[3]));
    if (kind == 1 && (lane & 15) == 0) {
#pragma unroll
        for (int jj = 0; jj < 4; ++jj)
            gst128(g * 16 + jj * 64, ws + WS_CS + (size_t)((2 * (b / 27) + j - 2) * FF + 64 * (b % 27)) * 4,
                   u32x4{__float_as_uint(cs[jj][0]), __float_as_uint(cs[jj][1]), __float_as_uint(cs[jj][2]), __float_as_uint(cs[jj][3])});
    }
    wait_vm_bar<0>();
    if (j == 3 && lane == 0) flag_set(ws, FL_R + b * 8, tag);
    lds_bar(); lds_bar();
    if (j == 2) {
        flag_wait2(ws, FL_R + lane * 8, FL_R + 512 + lane * 8, tag);
        flag_wait2(ws, FL_R + 1024 + lane * 8, FL_R + 1536 + lane * 8, tag);
        if (b == 0 && lane == 0) flag_set(ws, FL_E, tag);
    }
    lds_bar();
    const char* wsrc = ws + WS_WO + (size_t)(b >> 5) * 102400 + (j - 2) * 51200;
#pragma unroll 1
    for (int i = 0; i < 5; ++i) {
        glds_run(vl, wsrc + i * 10240, 1024, ldsb + L_WO + (j - 2) * 51200 + i * 10240, 1024, 10);
        const int ch = (b >> 3) + 32 * (2 * i + j - 2);
        if (WARM1 && ch < 270) warm64(lane * 128, ws + WS_W1 + (size_t)ch * 8192, ldsb + L_X + 4096);
        lds_bar();
    }
    lds_bar(); lds_bar();
}

__global__ __launch_bounds__(512, 2) void k_block(const u16* x, const u16* wqkv, const u16* wo, const u16* w1, const u16* b1, const u16* w2, const u16* b2, char* ws, u16* out) {
    extern __shared__ __attribute__((aligned(16))) char smem[];
    const lptr lds = (lptr)smem;
    const unsigned ldsb = (unsigned)(size_t)lds;
    const int b = blockIdx.x, w = __builtin_amdgcn_readfirstlane(threadIdx.x >> 6), lane = threadIdx.x & 63, j = w - 4;
    const unsigned vl = lane * 16;
    const int head = 4 * (b & 7) + ((b >> 3) & 3), c = b >> 4, ft0 = up_start(c);
    STDECL; STR(0);
    u32x2 seed;
    gld64s<0>(seed, 0, ws + FL_E);
    const int tbq = 4 * ((b & 7) >> 1) + ((b >> 3) & 3), cgq = (b & 1) * 8 + (b >> 5);
    if (cgq < 15) qkv_phase(x, wqkv, ws, lds, tbq, cgq, w, lane STPASS);
    ST(4);
    wait_vm_bar<0>();
    pin(seed);
    const u64 tag = (((u64)(unsigned)__builtin_amdgcn_readfirstlane(seed[1]) << 32) | (u64)(unsigned)__builtin_amdgcn_readfirstlane(seed[0])) * 6364136223846793005ull + 1442695040888963407ull;
    if (cgq < 15 && threadIdx.x == 0) flag_set(ws, FL_A + (cgq * 16 + tbq) * 128, tag);
    auto w1_part = [&](int k0, int n) {
#pragma unroll 1
        for (int i = j; i < 7; i += 4)
            glds_run(vl, ws + WS_W1 + (size_t)((ft0 + (i == 6 && (c & 3) == 3 ? 5 : i)) * 20 + k0) * 1024, 1024, ldsb + (7 * k0 + i) * 1024, 7168, n);
    };
    float tcs = 0.f, tb1 = 0.f;
    u32x4 held[33];
    if (w < 4) {
        attn_consumer(ws, lds, b, w, lane, tag STPASS);
        ST(10);
        wait_vm_bar<0>();
        if (w == 0) {
            if (lane == 0) flag_set(ws, FL_B + ((b >> 5) * 32 + head) * 128, tag);
            flag_wait(ws, FL_B + ((b & 7) * 32 + (lane & 31)) * 128, tag);
        }
    } else {
        __builtin_amdgcn_s_setprio(3);
        const char* kv = ws + WS_KV + (size_t)head * 64 * KVREC;
        if (j == 0) kv_loader<0>(kv, ldsb, lane);
        else if (j == 1) kv_loader<1>(kv, ldsb, lane);
        else retile_loader(wo, w1, w2, ws, lds, b, j, lane, tag);
        __builtin_amdgcn_s_setprio(0);
        wait_vm<0>();
        w1_part(0, 5);
        sfor<0, 28>([&](auto I) {
            constexpr int m = I.value / 7, i = I.value % 7;
            if (m < 3 || j < 3) gld128s<0>(held[I.value], vl, ws + WS_W1 + (size_t)((ft0 + (i == 6 && (c & 3) == 3 ? 5 : i)) * 20 + 5 + j + 4 * m) * 1024);
        });
        lds_bar();
        if (j >= 2) {
            const int n = min(16 * ft0 + 64 * (j - 2) + lane, FF - 1);
#pragma unroll
            for (int i = 0; i < 8; ++i) tcs += __uint_as_float(__hip_atomic_load((gu32*)(ws + WS_CS + (size_t)(i * FF + n) * 4), __ATOMIC_RELAXED, __HIP_MEMORY_SCOPE_AGENT));
            tb1 = bflo(b1[n]);
            pin(tcs); pin(tb1);
        }
        wait_vm<0>();
        sfor<0, 28>([&](auto I) { pin(held[I.value]); });
    }
    u32x2 hk[3];
    wo_phase(x, ws, lds, b, w, lane, hk, held, tag STPASS);
    if (w < 4) {
        up_consumer(ws, lds, b, w, lane, tag STPASS);
        down_phase<0>(b2, ws, out, lds, b, w, lane, hk, held STPASS);
        ST(22); STWAIT; STR(23); STFLUSH(0);
    } else {
        const char* w2s = ws + WS_W2 + (size_t)(b >> 5) * 276480;
        auto hold_w2 = [&] {
            const char* ha = w2s + (j >> 1) * 122880 + 71680 + (j & 1) * 17408;
            sfor<0, 18>([&](auto I) { gld128s<(I.value & 3) * 1024>(held[I.value], vl, ha + (I.value >> 2) * 4096); });
            const char* hb = w2s + ((j & 1) ? 245760 + (j >> 1) * 15360 : (j >> 1) * 122880 + 107520);
            sfor<0, 15>([&](auto I) { gld128s<(I.value & 3) * 1024>(held[18 + I.value], vl, hb + (I.value >> 2) * 4096); });
        };
        lds_bar();
        {
            u32x2 st[2][4];
            const int t0 = 16 * (b & 7) + 8 * ((b >> 3) & 1) + 2 * j;
            sfor<0, 8>([&](auto I) { gld64s<(I.value >> 2) * 2048 + (I.value & 3) * 128>(st[I.value >> 2][I.value & 3], (lane >> 4) * 512 + (lane & 15) * 8, ws + WS_ST + (size_t)t0 * 2048); });
            wait_vm<0>();
            sfor<0, 2>([&](auto T) {
                float s1 = 0.f, s2 = 0.f;
                sfor<0, 4>([&](auto I) { pin(st[T.value][I.value]); const unsigned u0 = st[T.value][I.value][0], u1 = st[T.value][I.value][1]; s1 += __uint_as_float(u0); s2 += __uint_as_float(u1); });
                float m, rs;
                ln_stats(swap32_add(swap16_add(s1)), swap32_add(swap16_add(s2)), m, rs);
                if (lane < 16) *(LAS f32x2*)(lds + L_ST + (32 * j + 16 * T.value + lane) * 8) = f32x2{-m * rs, rs};
            });
        }
        if (j >= 2) { ((LAS float*)(lds + L_X))[64 * (j - 2) + lane] = tcs; ((LAS float*)(lds + L_X))[128 + 64 * (j - 2) + lane] = tb1; }
        if (j == 0 && lane == 0) *(LAS unsigned*)(lds + L_CTR) = 0u;
        lds_bar();
        glds_run(vl, w2s + j * 17408, 1024, ldsb + j * 17408, 1024, j == 3 ? 19 : 17);
        if (j < 2) glds_run(vl, w2s + 122880 + j * 5120, 1024, ldsb + 71680 + j * 5120, 1024, 5);
        lds_bar();
        glds_run(vl, w2s + 133120 + j * 15360, 1024, ldsb + 81920 + j * 15360, 1024, 15);
        hold_w2();
        down_phase<1>(b2, ws, out, lds, b, w, lane, hk, held STPASS);
        ST(22); STWAIT; STR(23); STFLUSH(0);
    }
}

extern "C" void kernel_launch(void* const* d_in, const int* in_sizes, int n_in, void* d_out, int out_size, void* d_ws, size_t ws_size, hipStream_t stream) {
    (void)hipFuncSetAttribute((const void*)k_block, hipFuncAttributeMaxDynamicSharedMemorySize, M_LDS);
    k_block<<<256, 512, M_LDS, stream>>>((const u16*)d_in[0], (const u16*)d_in[1], (const u16*)d_in[2], (const u16*)d_in[4], (const u16*)d_in[5],
                                         (const u16*)d_in[6], (const u16*)d_in[7], (char*)d_ws, (u16*)d_out);
}
```

```cpp
#include <hip/hip_runtime.h>
#include <hip/hip_bf16.h>
#include <stdint.h>

typedef unsigned short u16;
typedef short bf16x8 __attribute__((ext_vector_type(8)));
typedef short s16x4 __attribute__((ext_vector_type(4)));
typedef float f32x16 __attribute__((ext_vector_type(16)));
typedef float f32x8 __attribute__((ext_vector_type(8)));
typedef float f32x4 __attribute__((ext_vector_type(4)));
typedef float f32x2 __attribute__((ext_vector_type(2)));
typedef unsigned u32x8 __attribute__((ext_vector_type(8)));
typedef unsigned u32x4 __attribute__((ext_vector_type(4)));
typedef unsigned u32x2 __attribute__((ext_vector_type(2)));
typedef __bf16 bf2 __attribute__((ext_vector_type(2)));
#define LAS __attribute__((address_space(3)))
#define DI __device__ __forceinline__
typedef LAS char* lptr;

constexpr int SEQ = 2048, DM = 640, NH = 32, FF = 1728;
constexpr int QREC = 1280, KVREC = 2560;

constexpr int FL_E = 0, FL_R = 2048, FL_A = 4096, FL_B = FL_A + 256 * 128, FL_C = FL_B + 256 * 128, FL_D = FL_C + 256 * 128;
constexpr size_t WS_Q = 262144;
constexpr size_t WS_KV = WS_Q + (size_t)NH * 64 * QREC;
constexpr size_t WS_O = WS_KV + (size_t)NH * 64 * KVREC + 4096;
constexpr size_t WS_H = WS_O + (size_t)128 * 20 * 1024;
constexpr size_t WS_G = WS_H + (size_t)128 * 20 * 1024;
constexpr size_t WS_WO = WS_G + (size_t)128 * 54 * 1024;
constexpr size_t WS_W1 = WS_WO + (size_t)8 * 20 * 5 * 1024;
constexpr size_t WS_W2 = WS_W1 + (size_t)108 * 20 * 1024;
constexpr size_t WS_ST = WS_W2 + (size_t)8 * 54 * 5 * 1024;
constexpr size_t WS_CS = WS_ST + (size_t)SEQ * 16 * 8;
constexpr size_t WS_END = WS_CS + (size_t)8 * FF * 4;

template <int V> using IC = std::integral_constant<int, V>;
#define SBAR() __builtin_amdgcn_sched_barrier(0)
template <int I, int N, class F> DI void sfor(F&& f) {
    if constexpr (I < N) { f(std::integral_constant<int, I>{}); sfor<I + 1, N>(f); }
}

DI unsigned pk2(float lo, float hi) { f32x2 v = {lo, hi}; bf2 b = __builtin_convertvector(v, bf2); return __builtin_bit_cast(unsigned, b); }
DI float bflo(unsigned u) { return __uint_as_float(u << 16); }
DI float bfhi(unsigned u) { return __uint_as_float(u & 0xffff0000u); }
DI float rbf(float x) { return bflo(pk2(x, x)); }
DI float dot2(unsigned a, unsigned b, float c) { return __builtin_amdgcn_fdot2_f32_bf16(__builtin_bit_cast(bf2, a), __builtin_bit_cast(bf2, b), c, false); }
DI float swap32_add(float v) { auto r = __builtin_amdgcn_permlane32_swap(__float_as_uint(v), __float_as_uint(v), false, false); return __uint_as_float(r[0]) + __uint_as_float(r[1]); }
DI float swap32_max(float v) { auto r = __builtin_amdgcn_permlane32_swap(__float_as_uint(v), __float_as_uint(v), false, false); return fmaxf(__uint_as_float(r[0]), __uint_as_float(r[1])); }
DI float swap16_add(float v) { auto r = __builtin_amdgcn_permlane16_swap(__float_as_uint(v), __float_as_uint(v), false, false); return __uint_as_float(r[0]) + __uint_as_float(r[1]); }
DI f32x16 mfma32(u32x4 a, u32x4 b, f32x16 c) { return __builtin_amdgcn_mfma_f32_32x32x16_bf16(__builtin_bit_cast(bf16x8, a), __builtin_bit_cast(bf16x8, b), c, 0, 0, 0); }
DI f32x16 mfma32k8(u32x2 a, u32x2 b, f32x16 c) { return __builtin_amdgcn_mfma_f32_32x32x8bf16_1k(__builtin_bit_cast(s16x4, a), __builtin_bit_cast(s16x4, b), c, 0, 0, 0); }
DI f32x4 mfma16(u32x4 a, u32x4 b, f32x4 c) { return __builtin_amdgcn_mfma_f32_16x16x32_bf16(__builtin_bit_cast(bf16x8, a), __builtin_bit_cast(bf16x8, b), c, 0, 0, 0); }
DI u32x4 lds128(lptr p) { return *(LAS u32x4*)p; }
DI u32x2 ldstr(lptr p) { return __builtin_bit_cast(u32x2, __builtin_amdgcn_ds_read_tr16_b64_v4i16((LAS s16x4*)p)); }

template <int OFF> DI void glds(unsigned voff, const void* sbase, unsigned ldsb) {
    asm volatile("s_add_u32 m0, %2, %3\n\ts_nop 0\n\tglobal_load_lds_dwordx4 %0, %1" ::"v"(voff), "s"(sbase), "s"(ldsb), "i"(OFF) : "memory", "scc");
}
template <int IMM> DI void gld128(u32x4& d, unsigned voff, const void* sbase) {
    asm volatile("global_load_dwordx4 %0, %1, %2 offset:%3" : "=v"(d) : "v"(voff), "s"(sbase), "i"(IMM) : "memory");
}
template <int IMM> DI void gld64(u32x2& d, unsigned voff, const void* sbase) {
    asm volatile("global_load_dwordx2 %0, %1, %2 offset:%3" : "=v"(d) : "v"(voff), "s"(sbase), "i"(IMM) : "memory");
}
template <int IMM> DI void gld32(unsigned& d, unsigned voff, const void* sbase) {
    asm volatile("global_load_dword %0, %1, %2 offset:%3" : "=v"(d) : "v"(voff), "s"(sbase), "i"(IMM) : "memory");
}
template <int IMM> DI void gldu16(unsigned& d, unsigned voff, const void* sbase) {
    asm volatile("global_load_ushort %0, %1, %2 offset:%3" : "=v"(d) : "v"(voff), "s"(sbase), "i"(IMM) : "memory");
}
DI void glds_s(unsigned voff, const void* sbase, unsigned ldsa) {
    asm volatile("s_mov_b32 m0, %2\n\ts_nop 0\n\tglobal_load_lds_dwordx4 %0, %1 sc1" ::"v"(voff), "s"(sbase), "s"(ldsa) : "memory");
}
template <int OFF, unsigned LO, unsigned HI> DI void glds_m(unsigned voff, const void* sbase, unsigned ldsb) {
    asm volatile("s_add_u32 m0, %2, %3\n\ts_mov_b32 exec_lo, %4\n\ts_mov_b32 exec_hi, %5\n\tglobal_load_lds_dwordx4 %0, %1 sc1\n\ts_mov_b64 exec, -1"
                 ::"v"(voff), "s"(sbase), "s"(ldsb), "i"(OFF), "i"(LO), "i"(HI) : "memory", "scc");
}
template <int IMM> DI void gld128s(u32x4& d, unsigned voff, const void* sbase) {
    asm volatile("global_load_dwordx4 %0, %1, %2 offset:%3 sc1" : "=v"(d) : "v"(voff), "s"(sbase), "i"(IMM) : "memory");
}
template <int IMM> DI void gld64s(u32x2& d, unsigned voff, const void* sbase) {
    asm volatile("global_load_dwordx2 %0, %1, %2 offset:%3 sc1" : "=v"(d) : "v"(voff), "s"(sbase), "i"(IMM) : "memory");
}
DI void gst128(unsigned voff, const void* sbase, u32x4 v) {
    asm volatile("global_store_dwordx4 %0, %1, %2 sc1\n\ts_nop 1" ::"v"(voff), "v"(v), "s"(sbase) : "memory");
}
DI void gst64(unsigned voff, const void* sbase, u32x2 v) {
    asm volatile("global_store_dwordx2 %0, %1, %2 sc1" ::"v"(voff), "v"(v), "s"(sbase) : "memory");
}
DI void warm64(unsigned voff, const void* sbase, unsigned ldsa) {
    asm volatile("s_mov_b32 m0, %2\n\ts_nop 0\n\tglobal_load_lds_dword %0, %1 sc1" ::"v"(voff), "s"(sbase), "s"(ldsa) : "memory");
}
template <int N> DI void wait_vm() { asm volatile("s_waitcnt vmcnt(%0)" ::"i"(N) : "memory"); }
template <int N> DI void wait_vm_bar() { asm volatile("s_waitcnt vmcnt(%0) lgkmcnt(0)\n\ts_barrier" ::"i"(N) : "memory"); }
DI void lds_bar() { asm volatile("s_waitcnt lgkmcnt(0)\n\ts_barrier" ::: "memory"); }
template <class T> DI void pin(T& v) { asm volatile("" : "+v"(v)); }

#define STARG
#define STPASS
#define STWAIT
#define STDECL
#define ST(i)
#define STR(i)
#define STFLUSH(k)

DI void ln_stats(float s1, float s2, float& m, float& rs) {
    const float mean = s1 * (1.0f / DM);
    const float var = fmaxf(s2 * (1.0f / DM) - mean * mean, 0.0f);
    m = rbf(mean);
    rs = __builtin_amdgcn_rcpf(rbf(__builtin_amdgcn_sqrtf(rbf(rbf(var) + 1e-5f))));
}

#define QKV_LOOP_QK \
    "s_nop 1\n\t" \
    "1:\n\t" \
    "s_waitcnt lgkmcnt(0)\n\t" \
    "v_mfma_f32_32x32x16_bf16 v[64:79], v[40:43], v[32:35], v[64:79]\n\t" \
    "ds_read_b128 v[48:51], %[x1] offset:0\n\t" \
    "ds_read_b64_tr_b16 v[56:57], %[w0] offset:4096\n\t" \
    "ds_read_b64_tr_b16 v[58:59], %[w0] offset:5120\n\t" \
    "v_lshlrev_b32 v14, 16, v32\n\t" \
    "v_and_b32 v15, 0xffff0000, v32\n\t" \
    "v_mfma_f32_32x32x16_bf16 v[80:95], v[44:47], v[32:35], v[80:95]\n\t" \
    "ds_read_b64_tr_b16 v[60:61], %[w1] offset:4096\n\t" \
    "ds_read_b64_tr_b16 v[62:63], %[w1] offset:5120\n\t" \
    "v_fmac_f32 v18, v14, v14\n\t" \
    "v_fmac_f32 v19, v15, v15\n\t" \
    "v_lshlrev_b32 v16, 16, v33\n\t" \
    "v_and_b32 v17, 0xffff0000, v33\n\t" \
    "v_mfma_f32_16x16x32_bf16 v[24:27], v[20:23], v[32:35], v[24:27]\n\t" \
    "ds_read_b128 v[52:55], %[y1] offset:0\n\t" \
    "v_fmac_f32 v18, v16, v16\n\t" \
    "v_mfma_f32_32x32x16_bf16 v[96:111], v[40:43], v[36:39], v[96:111]\n\t" \
    "v_fmac_f32 v19, v17, v17\n\t" \
    "v_lshlrev_b32 v14, 16, v34\n\t" \
    "v_and_b32 v15, 0xffff0000, v34\n\t" \
    "v_fmac_f32 v18, v14, v14\n\t" \
    "v_fmac_f32 v19, v15, v15\n\t" \
    "v_mfma_f32_32x32x16_bf16 v[112:127], v[44:47], v[36:39], v[112:127]\n\t" \
    "v_lshlrev_b32 v16, 16, v35\n\t" \
    "v_and_b32 v17, 0xffff0000, v35\n\t" \
    "v_fmac_f32 v18, v16, v16\n\t" \
    "v_fmac_f32 v19, v17, v17\n\t" \
    "v_mfma_f32_16x16x32_bf16 v[28:31], v[20:23], v[40:43], v[28:31]\n\t" \
    "s_waitcnt lgkmcnt(0)\n\t" \
    "v_mfma_f32_32x32x16_bf16 v[64:79], v[56:59], v[48:51], v[64:79]\n\t" \
    "ds_read_b128 v[32:35], %[x2] offset:0\n\t" \
    "ds_read_b64_tr_b16 v[40:41], %[w0] offset:8192\n\t" \
    "ds_read_b64_tr_b16 v[42:43], %[w0] offset:9216\n\t" \
    "v_lshlrev_b32 v14, 16, v48\n\t" \
    "v_and_b32 v15, 0xffff0000, v48\n\t" \
    "v_mfma_f32_32x32x16_bf16 v[80:95], v[60:63], v[48:51], v[80:95]\n\t" \
    "ds_read_b64_tr_b16 v[44:45], %[w1] offset:8192\n\t" \
    "ds_read_b64_tr_b16 v[46:47], %[w1] offset:9216\n\t" \
    "v_fmac_f32 v18, v14, v14\n\t" \
    "v_fmac_f32 v19, v15, v15\n\t" \
    "v_lshlrev_b32 v16, 16, v49\n\t" \
    "v_and_b32 v17, 0xffff0000, v49\n\t" \
    "v_mfma_f32_16x16x32_bf16 v[24:27], v[20:23], v[48:51], v[24:27]\n\t" \
    "ds_read_b128 v[36:39], %[y2] offset:0\n\t" \
    "v_fmac_f32 v18, v16, v16\n\t" \
    "v_mfma_f32_32x32x16_bf16 v[96:111], v[56:59], v[52:55], v[96:111]\n\t" \
    "v_fmac_f32 v19, v17, v17\n\t" \
    "v_lshlrev_b32 v14, 16, v50\n\t" \
    "v_and_b32 v15, 0xffff0000, v50\n\t" \
    "v_fmac_f32 v18, v14, v14\n\t" \
    "v_fmac_f32 v19, v15, v15\n\t" \
    "v_mfma_f32_32x32x16_bf16 v[112:127], v[60:63], v[52:55], v[112:127]\n\t" \
    "v_lshlrev_b32 v16, 16, v51\n\t" \
    "v_and_b32 v17, 0xffff0000, v51\n\t" \
    "v_fmac_f32 v18, v16, v16\n\t" \
    "v_fmac_f32 v19, v17, v17\n\t" \
    "v_mfma_f32_16x16x32_bf16 v[28:31], v[20:23], v[56:59], v[28:31]\n\t" \
    "s_waitcnt lgkmcnt(0)\n\t" \
    "v_mfma_f32_32x32x16_bf16 v[64:79], v[40:43], v[32:35], v[64:79]\n\t" \
    "ds_read_b128 v[48:51], %[x3] offset:0\n\t" \
    "ds_read_b64_tr_b16 v[56:57], %[w0] offset:12288\n\t" \
    "ds_read_b64_tr_b16 v[58:59], %[w0] offset:13312\n\t" \
    "v_lshlrev_b32 v14, 16, v32\n\t" \
    "v_and_b32 v15, 0xffff0000, v32\n\t" \
    "v_mfma_f32_32x32x16_bf16 v[80:95], v[44:47], v[32:35], v[80:95]\n\t" \
    "ds_read_b64_tr_b16 v[60:61], %[w1] offset:12288\n\t" \
    "ds_read_b64_tr_b16 v[62:63], %[w1] offset:13312\n\t" \
    "v_fmac_f32 v18, v14, v14\n\t" \
    "v_fmac_f32 v19, v15, v15\n\t" \
    "v_lshlrev_b32 v16, 16, v33\n\t" \
    "v_and_b32 v17, 0xffff0000, v33\n\t" \
    "v_mfma_f32_16x16x32_bf16 v[24:27], v[20:23], v[32:35], v[24:27]\n\t" \
    "ds_read_b128 v[52:55], %[y3] offset:0\n\t" \
    "v_fmac_f32 v18, v16, v16\n\t" \
    "v_mfma_f32_32x32x16_bf16 v[96:111], v[40:43], v[36:39], v[96:111]\n\t" \
    "v_fmac_f32 v19, v17, v17\n\t" \
    "v_lshlrev_b32 v14, 16, v34\n\t" \
    "v_and_b32 v15, 0xffff0000, v34\n\t" \
    "v_fmac_f32 v18, v14, v14\n\t" \
    "v_fmac_f32 v19, v15, v15\n\t" \
    "v_mfma_f32_32x32x16_bf16 v[112:127], v[44:47], v[36:39], v[112:127]\n\t" \
    "v_lshlrev_b32 v16, 16, v35\n\t" \
    "v_and_b32 v17, 0xffff0000, v35\n\t" \
    "v_fmac_f32 v18, v16, v16\n\t" \
    "v_fmac_f32 v19, v17, v17\n\t" \
    "v_mfma_f32_16x16x32_bf16 v[28:31], v[20:23], v[40:43], v[28:31]\n\t" \
    "s_waitcnt lgkmcnt(0)\n\t" \
    "s_barrier\n\t" \
    "v_mfma_f32_32x32x16_bf16 v[64:79], v[56:59], v[48:51], v[64:79]\n\t" \
    "ds_read_b128 v[32:35], %[x0] offset:32768\n\t" \
    "ds_read_b64_tr_b16 v[40:41], %[w0] offset:32768\n\t" \
    "ds_read_b64_tr_b16 v[42:43], %[w0] offset:33792\n\t" \
    "v_lshlrev_b32 v14, 16, v48\n\t" \
    "v_and_b32 v15, 0xffff0000, v48\n\t" \
    "v_mfma_f32_32x32x16_bf16 v[80:95], v[60:63], v[48:51], v[80:95]\n\t" \
    "ds_read_b64_tr_b16 v[44:45], %[w1] offset:32768\n\t" \
    "ds_read_b64_tr_b16 v[46:47], %[w1] offset:33792\n\t" \
    "v_fmac_f32 v18, v14, v14\n\t" \
    "v_fmac_f32 v19, v15, v15\n\t" \
    "v_lshlrev_b32 v16, 16, v49\n\t" \
    "v_and_b32 v17, 0xffff0000, v49\n\t" \
    "v_mfma_f32_16x16x32_bf16 v[24:27], v[20:23], v[48:51], v[24:27]\n\t" \
    "ds_read_b128 v[36:39], %[y0] offset:32768\n\t" \
    "v_fmac_f32 v18, v16, v16\n\t" \
    "v_mfma_f32_32x32x16_bf16 v[96:111], v[56:59], v[52:55], v[96:111]\n\t" \
    "v_fmac_f32 v19, v17, v17\n\t" \
    "v_lshlrev_b32 v14, 16, v50\n\t" \
    "v_and_b32 v15, 0xffff0000, v50\n\t" \
    "v_fmac_f32 v18, v14, v14\n\t" \
    "v_fmac_f32 v19, v15, v15\n\t" \
    "v_mfma_f32_32x32x16_bf16 v[112:127], v[60:63], v[52:55], v[112:127]\n\t" \
    "v_lshlrev_b32 v16, 16, v51\n\t" \
    "v_and_b32 v17, 0xffff0000, v51\n\t" \
    "v_fmac_f32 v18, v16, v16\n\t" \
    "v_fmac_f32 v19, v17, v17\n\t" \
    "v_mfma_f32_16x16x32_bf16 v[28:31], v[20:23], v[56:59], v[28:31]\n\t" \
    "s_waitcnt lgkmcnt(0)\n\t" \
    "v_mfma_f32_32x32x16_bf16 v[64:79], v[40:43], v[32:35], v[64:79]\n\t" \
    "ds_read_b128 v[48:51], %[x1] offset:32768\n\t" \
    "ds_read_b64_tr_b16 v[56:57], %[w0] offset:36864\n\t" \
    "ds_read_b64_tr_b16 v[58:59], %[w0] offset:37888\n\t" \
    "v_lshlrev_b32 v14, 16, v32\n\t" \
    "v_and_b32 v15, 0xffff0000, v32\n\t" \
    "v_mfma_f32_32x32x16_bf16 v[80:95], v[44:47], v[32:35], v[80:95]\n\t" \
    "ds_read_b64_tr_b16 v[60:61], %[w1] offset:36864\n\t" \
    "ds_read_b64_tr_b16 v[62:63], %[w1] offset:37888\n\t" \
    "v_fmac_f32 v18, v14, v14\n\t" \
    "v_fmac_f32 v19, v15, v15\n\t" \
    "v_lshlrev_b32 v16, 16, v33\n\t" \
    "v_and_b32 v17, 0xffff0000, v33\n\t" \
    "v_mfma_f32_16x16x32_bf16 v[24:27], v[20:23], v[32:35], v[24:27]\n\t" \
    "ds_read_b128 v[52:55], %[y1] offset:32768\n\t" \
    "v_fmac_f32 v18, v16, v16\n\t" \
    "v_mfma_f32_32x32x16_bf16 v[96:111], v[40:43], v[36:39], v[96:111]\n\t" \
    "v_fmac_f32 v19, v17, v17\n\t" \
    "v_lshlrev_b32 v14, 16, v34\n\t" \
    "v_and_b32 v15, 0xffff0000, v34\n\t" \
    "v_fmac_f32 v18, v14, v14\n\t" \
    "v_fmac_f32 v19, v15, v15\n\t" \
    "v_mfma_f32_32x32x16_bf16 v[112:127], v[44:47], v[36:39], v[112:127]\n\t" \
    "v_lshlrev_b32 v16, 16, v35\n\t" \
    "v_and_b32 v17, 0xffff0000, v35\n\t" \
    "v_fmac_f32 v18, v16, v16\n\t" \
    "v_fmac_f32 v19, v17, v17\n\t" \
    "v_mfma_f32_16x16x32_bf16 v[28:31], v[20:23], v[40:43], v[28:31]\n\t" \
    "v_xor_b32 %[x0], 0x10000, %[x0]\n\t" \
    "v_xor_b32 %[y0], 0x10000, %[y0]\n\t" \
    "s_waitcnt lgkmcnt(0)\n\t" \
    "v_mfma_f32_32x32x16_bf16 v[64:79], v[56:59], v[48:51], v[64:79]\n\t" \
    "ds_read_b128 v[32:35], %[x2] offset:32768\n\t" \
    "ds_read_b64_tr_b16 v[40:41], %[w0] offset:40960\n\t" \
    "ds_read_b64_tr_b16 v[42:43], %[w0] offset:41984\n\t" \
    "v_lshlrev_b32 v14, 16, v48\n\t" \
    "v_and_b32 v15, 0xffff0000, v48\n\t" \
    "v_mfma_f32_32x32x16_bf16 v[80:95], v[60:63], v[48:51], v[80:95]\n\t" \
    "ds_read_b64_tr_b16 v[44:45], %[w1] offset:40960\n\t" \
    "ds_read_b64_tr_b16 v[46:47], %[w1] offset:41984\n\t" \
    "v_fmac_f32 v18, v14, v14\n\t" \
    "v_fmac_f32 v19, v15, v15\n\t" \
    "v_lshlrev_b32 v16, 16, v49\n\t" \
    "v_and_b32 v17, 0xffff0000, v49\n\t" \
    "v_mfma_f32_16x16x32_bf16 v[24:27], v[20:23], v[48:51], v[24:27]\n\t" \
    "ds_read_b128 v[36:39], %[y2] offset:32768\n\t" \
    "v_fmac_f32 v18, v16, v16\n\t" \
    "v_mfma_f32_32x32x16_bf16 v[96:111], v[56:59], v[52:55], v[96:111]\n\t" \
    "v_fmac_f32 v19, v17, v17\n\t" \
    "v_lshlrev_b32 v14, 16, v50\n\t" \
    "v_and_b32 v15, 0xffff0000, v50\n\t" \
    "v_fmac_f32 v18, v14, v14\n\t" \
    "v_fmac_f32 v19, v15, v15\n\t" \
    "v_mfma_f32_32x32x16_bf16 v[112:127], v[60:63], v[52:55], v[112:127]\n\t" \
    "v_lshlrev_b32 v16, 16, v51\n\t" \
    "v_and_b32 v17, 0xffff0000, v51\n\t" \
    "v_fmac_f32 v18, v16, v16\n\t" \
    "v_fmac_f32 v19, v17, v17\n\t" \
    "v_mfma_f32_16x16x32_bf16 v[28:31], v[20:23], v[56:59], v[28:31]\n\t" \
    "v_xor_b32 %[x1], 0x10000, %[x1]\n\t" \
    "v_xor_b32 %[y1], 0x10000, %[y1]\n\t" \
    "s_waitcnt lgkmcnt(0)\n\t" \
    "v_mfma_f32_32x32x16_bf16 v[64:79], v[40:43], v[32:35], v[64:79]\n\t" \
    "ds_read_b128 v[48:51], %[x3] offset:32768\n\t" \
    "ds_read_b64_tr_b16 v[56:57], %[w0] offset:45056\n\t" \
    "ds_read_b64_tr_b16 v[58:59], %[w0] offset:46080\n\t" \
    "v_lshlrev_b32 v14, 16, v32\n\t" \
    "v_and_b32 v15, 0xffff0000, v32\n\t" \
    "v_mfma_f32_32x32x16_bf16 v[80:95], v[44:47], v[32:35], v[80:95]\n\t" \
    "ds_read_b64_tr_b16 v[60:61], %[w1] offset:45056\n\t" \
    "ds_read_b64_tr_b16 v[62:63], %[w1] offset:46080\n\t" \
    "v_fmac_f32 v18, v14, v14\n\t" \
    "v_fmac_f32 v19, v15, v15\n\t" \
    "v_lshlrev_b32 v16, 16, v33\n\t" \
    "v_and_b32 v17, 0xffff0000, v33\n\t" \
    "v_mfma_f32_16x16x32_bf16 v[24:27], v[20:23], v[32:35], v[24:27]\n\t" \
    "ds_read_b128 v[52:55], %[y3] offset:32768\n\t" \
    "v_fmac_f32 v18, v16, v16\n\t" \
    "v_mfma_f32_32x32x16_bf16 v[96:111], v[40:43], v[36:39], v[96:111]\n\t" \
    "v_fmac_f32 v19, v17, v17\n\t" \
    "v_lshlrev_b32 v14, 16, v34\n\t" \
    "v_and_b32 v15, 0xffff0000, v34\n\t" \
    "v_fmac_f32 v18, v14, v14\n\t" \
    "v_fmac_f32 v19, v15, v15\n\t" \
    "v_mfma_f32_32x32x16_bf16 v[112:127], v[44:47], v[36:39], v[112:127]\n\t" \
    "v_lshlrev_b32 v16, 16, v35\n\t" \
    "v_and_b32 v17, 0xffff0000, v35\n\t" \
    "v_fmac_f32 v18, v16, v16\n\t" \
    "v_fmac_f32 v19, v17, v17\n\t" \
    "v_mfma_f32_16x16x32_bf16 v[28:31], v[20:23], v[40:43], v[28:31]\n\t" \
    "v_xor_b32 %[w0], 0x10000, %[w0]\n\t" \
    "v_xor_b32 %[w1], 0x10000, %[w1]\n\t" \
    "s_waitcnt lgkmcnt(0)\n\t" \
    "s_barrier\n\t" \
    "v_mfma_f32_32x32x16_bf16 v[64:79], v[56:59], v[48:51], v[64:79]\n\t" \
    "ds_read_b128 v[32:35], %[x0] offset:0\n\t" \
    "ds_read_b64_tr_b16 v[40:41], %[w0] offset:0\n\t" \
    "ds_read_b64_tr_b16 v[42:43], %[w0] offset:1024\n\t" \
    "v_lshlrev_b32 v14, 16, v48\n\t" \
    "v_and_b32 v15, 0xffff0000, v48\n\t" \
    "v_mfma_f32_32x32x16_bf16 v[80:95], v[60:63], v[48:51], v[80:95]\n\t" \
    "ds_read_b64_tr_b16 v[44:45], %[w1] offset:0\n\t" \
    "ds_read_b64_tr_b16 v[46:47], %[w1] offset:1024\n\t" \
    "v_fmac_f32 v18, v14, v14\n\t" \
    "v_fmac_f32 v19, v15, v15\n\t" \
    "v_lshlrev_b32 v16, 16, v49\n\t" \
    "v_and_b32 v17, 0xffff0000, v49\n\t" \
    "v_mfma_f32_16x16x32_bf16 v[24:27], v[20:23], v[48:51], v[24:27]\n\t" \
    "ds_read_b128 v[36:39], %[y0] offset:0\n\t" \
    "v_fmac_f32 v18, v16, v16\n\t" \
    "v_mfma_f32_32x32x16_bf16 v[96:111], v[56:59], v[52:55], v[96:111]\n\t" \
    "v_fmac_f32 v19, v17, v17\n\t" \
    "v_lshlrev_b32 v14, 16, v50\n\t" \
    "v_and_b32 v15, 0xffff0000, v50\n\t" \
    "v_fmac_f32 v18, v14, v14\n\t" \
    "v_fmac_f32 v19, v15, v15\n\t" \
    "v_mfma_f32_32x32x16_bf16 v[112:127], v[60:63], v[52:55], v[112:127]\n\t" \
    "v_lshlrev_b32 v16, 16, v51\n\t" \
    "v_and_b32 v17, 0xffff0000, v51\n\t" \
    "v_fmac_f32 v18, v16, v16\n\t" \
    "v_fmac_f32 v19, v17, v17\n\t" \
    "v_mfma_f32_16x16x32_bf16 v[28:31], v[20:23], v[56:59], v[28:31]\n\t" \
    "v_xor_b32 %[x2], 0x10000, %[x2]\n\t" \
    "v_xor_b32 %[y2], 0x10000, %[y2]\n\t" \
    "v_xor_b32 %[x3], 0x10000, %[x3]\n\t" \
    "v_xor_b32 %[y3], 0x10000, %[y3]\n\t" \
    "s_sub_u32 %[cnt], %[cnt], 1\n\t" \
    "s_cbranch_scc0 1b\n\t" \
    "s_waitcnt lgkmcnt(0)\n\t" \
    "s_nop 15\n\t"
#define QKV_LOOP_V \
    "s_nop 1\n\t" \
    "1:\n\t" \
    "s_waitcnt lgkmcnt(0)\n\t" \
    "v_mfma_f32_32x32x16_bf16 v[64:79], v[32:35], v[40:43], v[64:79]\n\t" \
    "ds_read_b128 v[48:51], %[x1] offset:0\n\t" \
    "ds_read_b64_tr_b16 v[56:57], %[w0] offset:4096\n\t" \
    "ds_read_b64_tr_b16 v[58:59], %[w0] offset:5120\n\t" \
    "v_lshlrev_b32 v14, 16, v32\n\t" \
    "v_and_b32 v15, 0xffff0000, v32\n\t" \
    "v_mfma_f32_32x32x16_bf16 v[80:95], v[32:35], v[44:47], v[80:95]\n\t" \
    "ds_read_b64_tr_b16 v[60:61], %[w1] offset:4096\n\t" \
    "ds_read_b64_tr_b16 v[62:63], %[w1] offset:5120\n\t" \
    "v_fmac_f32 v18, v14, v14\n\t" \
    "v_fmac_f32 v19, v15, v15\n\t" \
    "v_lshlrev_b32 v16, 16, v33\n\t" \
    "v_and_b32 v17, 0xffff0000, v33\n\t" \
    "v_mfma_f32_16x16x32_bf16 v[24:27], v[20:23], v[32:35], v[24:27]\n\t" \
    "ds_read_b128 v[52:55], %[y1] offset:0\n\t" \
    "v_fmac_f32 v18, v16, v16\n\t" \
    "v_mfma_f32_32x32x16_bf16 v[96:111], v[36:39], v[40:43], v[96:111]\n\t" \
    "v_fmac_f32 v19, v17, v17\n\t" \
    "v_lshlrev_b32 v14, 16, v34\n\t" \
    "v_and_b32 v15, 0xffff0000, v34\n\t" \
    "v_fmac_f32 v18, v14, v14\n\t" \
    "v_fmac_f32 v19, v15, v15\n\t" \
    "v_mfma_f32_32x32x16_bf16 v[112:127], v[36:39], v[44:47], v[112:127]\n\t" \
    "v_lshlrev_b32 v16, 16, v35\n\t" \
    "v_and_b32 v17, 0xffff0000, v35\n\t" \
    "v_fmac_f32 v18, v16, v16\n\t" \
    "v_fmac_f32 v19, v17, v17\n\t" \
    "v_mfma_f32_16x16x32_bf16 v[28:31], v[20:23], v[40:43], v[28:31]\n\t" \
    "s_waitcnt lgkmcnt(0)\n\t" \
    "v_mfma_f32_32x32x16_bf16 v[64:79], v[48:51], v[56:59], v[64:79]\n\t" \
    "ds_read_b128 v[32:35], %[x2] offset:0\n\t" \
    "ds_read_b64_tr_b16 v[40:41], %[w0] offset:8192\n\t" \
    "ds_read_b64_tr_b16 v[42:43], %[w0] offset:9216\n\t" \
    "v_lshlrev_b32 v14, 16, v48\n\t" \
    "v_and_b32 v15, 0xffff0000, v48\n\t" \
    "v_mfma_f32_32x32x16_bf16 v[80:95], v[48:51], v[60:63], v[80:95]\n\t" \
    "ds_read_b64_tr_b16 v[44:45], %[w1] offset:8192\n\t" \
    "ds_read_b64_tr_b16 v[46:47], %[w1] offset:9216\n\t" \
    "v_fmac_f32 v18, v14, v14\n\t" \
    "v_fmac_f32 v19, v15, v15\n\t" \
    "v_lshlrev_b32 v16, 16, v49\n\t" \
    "v_and_b32 v17, 0xffff0000, v49\n\t" \
    "v_mfma_f32_16x16x32_bf16 v[24:27], v[20:23], v[48:51], v[24:27]\n\t" \
    "ds_read_b128 v[36:39], %[y2] offset:0\n\t" \
    "v_fmac_f32 v18, v16, v16\n\t" \
    "v_mfma_f32_32x32x16_bf16 v[96:111], v[52:55], v[56:59], v[96:111]\n\t" \
    "v_fmac_f32 v19, v17, v17\n\t" \
    "v_lshlrev_b32 v14, 16, v50\n\t" \
    "v_and_b32 v15, 0xffff0000, v50\n\t" \
    "v_fmac_f32 v18, v14, v14\n\t" \
    "v_fmac_f32 v19, v15, v15\n\t" \
    "v_mfma_f32_32x32x16_bf16 v[112:127], v[52:55], v[60:63], v[112:127]\n\t" \
    "v_lshlrev_b32 v16, 16, v51\n\t" \
    "v_and_b32 v17, 0xffff0000, v51\n\t" \
    "v_fmac_f32 v18, v16, v16\n\t" \
    "v_fmac_f32 v19, v17, v17\n\t" \
    "v_mfma_f32_16x16x32_bf16 v[28:31], v[20:23], v[56:59], v[28:31]\n\t" \
    "s_waitcnt lgkmcnt(0)\n\t" \
    "v_mfma_f32_32x32x16_bf16 v[64:79], v[32:35], v[40:43], v[64:79]\n\t" \
    "ds_read_b128 v[48:51], %[x3] offset:0\n\t" \
    "ds_read_b64_tr_b16 v[56:57], %[w0] offset:12288\n\t" \
    "ds_read_b64_tr_b16 v[58:59], %[w0] offset:13312\n\t" \
    "v_lshlrev_b32 v14, 16, v32\n\t" \
    "v_and_b32 v15, 0xffff0000, v32\n\t" \
    "v_mfma_f32_32x32x16_bf16 v[80:95], v[32:35], v[44:47], v[80:95]\n\t" \
    "ds_read_b64_tr_b16 v[60:61], %[w1] offset:12288\n\t" \
    "ds_read_b64_tr_b16 v[62:63], %[w1] offset:13312\n\t" \
    "v_fmac_f32 v18, v14, v14\n\t" \
    "v_fmac_f32 v19, v15, v15\n\t" \
    "v_lshlrev_b32 v16, 16, v33\n\t" \
    "v_and_b32 v17, 0xffff0000, v33\n\t" \
    "v_mfma_f32_16x16x32_bf16 v[24:27], v[20:23], v[32:35], v[24:27]\n\t" \
    "ds_read_b128 v[52:55], %[y3] offset:0\n\t" \
    "v_fmac_f32 v18, v16, v16\n\t" \
    "v_mfma_f32_32x32x16_bf16 v[96:111], v[36:39], v[40:43], v[96:111]\n\t" \
    "v_fmac_f32 v19, v17, v17\n\t" \
    "v_lshlrev_b32 v14, 16, v34\n\t" \
    "v_and_b32 v15, 0xffff0000, v34\n\t" \
    "v_fmac_f32 v18, v14, v14\n\t" \
    "v_fmac_f32 v19, v15, v15\n\t" \
    "v_mfma_f32_32x32x16_bf16 v[112:127], v[36:39], v[44:47], v[112:127]\n\t" \
    "v_lshlrev_b32 v16, 16, v35\n\t" \
    "v_and_b32 v17, 0xffff0000, v35\n\t" \
    "v_fmac_f32 v18, v16, v16\n\t" \
    "v_fmac_f32 v19, v17, v17\n\t" \
    "v_mfma_f32_16x16x32_bf16 v[28:31], v[20:23], v[40:43], v[28:31]\n\t" \
    "s_waitcnt lgkmcnt(0)\n\t" \
    "s_barrier\n\t" \
    "v_mfma_f32_32x32x16_bf16 v[64:79], v[48:51], v[56:59], v[64:79]\n\t" \
    "ds_read_b128 v[32:35], %[x0] offset:32768\n\t" \
    "ds_read_b64_tr_b16 v[40:41], %[w0] offset:32768\n\t" \
    "ds_read_b64_tr_b16 v[42:43], %[w0] offset:33792\n\t" \
    "v_lshlrev_b32 v14, 16, v48\n\t" \
    "v_and_b32 v15, 0xffff0000, v48\n\t" \
    "v_mfma_f32_32x32x16_bf16 v[80:95], v[48:51], v[60:63], v[80:95]\n\t" \
    "ds_read_b64_tr_b16 v[44:45], %[w1] offset:32768\n\t" \
    "ds_read_b64_tr_b16 v[46:47], %[w1] offset:33792\n\t" \
    "v_fmac_f32 v18, v14, v14\n\t" \
    "v_fmac_f32 v19, v15, v15\n\t" \
    "v_lshlrev_b32 v16, 16, v49\n\t" \
    "v_and_b32 v17, 0xffff0000, v49\n\t" \
    "v_mfma_f32_16x16x32_bf16 v[24:27], v[20:23], v[48:51], v[24:27]\n\t" \
    "ds_read_b128 v[36:39], %[y0] offset:32768\n\t" \
    "v_fmac_f32 v18, v16, v16\n\t" \
    "v_mfma_f32_32x32x16_bf16 v[96:111], v[52:55], v[56:59], v[96:111]\n\t" \
    "v_fmac_f32 v19, v17, v17\n\t" \
    "v_lshlrev_b32 v14, 16, v50\n\t" \
    "v_and_b32 v15, 0xffff0000, v50\n\t" \
    "v_fmac_f32 v18, v14, v14\n\t" \
    "v_fmac_f32 v19, v15, v15\n\t" \
    "v_mfma_f32_32x32x16_bf16 v[112:127], v[52:55], v[60:63], v[112:127]\n\t" \
    "v_lshlrev_b32 v16, 16, v51\n\t" \
    "v_and_b32 v17, 0xffff0000, v51\n\t" \
    "v_fmac_f32 v18, v16, v16\n\t" \
    "v_fmac_f32 v19, v17, v17\n\t" \
    "v_mfma_f32_16x16x32_bf16 v[28:31], v[20:23], v[56:59], v[28:31]\n\t" \
    "s_waitcnt lgkmcnt(0)\n\t" \
    "v_mfma_f32_32x32x16_bf16 v[64:79], v[32:35], v[40:43], v[64:79]\n\t" \
    "ds_read_b128 v[48:51], %[x1] offset:32768\n\t" \
    "ds_read_b64_tr_b16 v[56:57], %[w0] offset:36864\n\t" \
    "ds_read_b64_tr_b16 v[58:59], %[w0] offset:37888\n\t" \
    "v_lshlrev_b32 v14, 16, v32\n\t" \
    "v_and_b32 v15, 0xffff0000, v32\n\t" \
    "v_mfma_f32_32x32x16_bf16 v[80:95], v[32:35], v[44:47], v[80:95]\n\t" \
    "ds_read_b64_tr_b16 v[60:61], %[w1] offset:36864\n\t" \
    "ds_read_b64_tr_b16 v[62:63], %[w1] offset:37888\n\t" \
    "v_fmac_f32 v18, v14, v14\n\t" \
    "v_fmac_f32 v19, v15, v15\n\t" \
    "v_lshlrev_b32 v16, 16, v33\n\t" \
    "v_and_b32 v17, 0xffff0000, v33\n\t" \
    "v_mfma_f32_16x16x32_bf16 v[24:27], v[20:23], v[32:35], v[24:27]\n\t" \
    "ds_read_b128 v[52:55], %[y1] offset:32768\n\t" \
    "v_fmac_f32 v18, v16, v16\n\t" \
    "v_mfma_f32_32x32x16_bf16 v[96:111], v[36:39], v[40:43], v[96:111]\n\t" \
    "v_fmac_f32 v19, v17, v17\n\t" \
    "v_lshlrev_b32 v14, 16, v34\n\t" \
    "v_and_b32 v15, 0xffff0000, v34\n\t" \
    "v_fmac_f32 v18, v14, v14\n\t" \
    "v_fmac_f32 v19, v15, v15\n\t" \
    "v_mfma_f32_32x32x16_bf16 v[112:127], v[36:39], v[44:47], v[112:127]\n\t" \
    "v_lshlrev_b32 v16, 16, v35\n\t" \
    "v_and_b32 v17, 0xffff0000, v35\n\t" \
    "v_fmac_f32 v18, v16, v16\n\t" \
    "v_fmac_f32 v19, v17, v17\n\t" \
    "v_mfma_f32_16x16x32_bf16 v[28:31], v[20:23], v[40:43], v[28:31]\n\t" \
    "v_xor_b32 %[x0], 0x10000, %[x0]\n\t" \
    "v_xor_b32 %[y0], 0x10000, %[y0]\n\t" \
    "s_waitcnt lgkmcnt(0)\n\t" \
    "v_mfma_f32_32x32x16_bf16 v[64:79], v[48:51], v[56:59], v[64:79]\n\t" \
    "ds_read_b128 v[32:35], %[x2] offset:32768\n\t" \
    "ds_read_b64_tr_b16 v[40:41], %[w0] offset:40960\n\t" \
    "ds_read_b64_tr_b16 v[42:43], %[w0] offset:41984\n\t" \
    "v_lshlrev_b32 v14, 16, v48\n\t" \
    "v_and_b32 v15, 0xffff0000, v48\n\t" \
    "v_mfma_f32_32x32x16_bf16 v[80:95], v[48:51], v[60:63], v[80:95]\n\t" \
    "ds_read_b64_tr_b16 v[44:45], %[w1] offset:40960\n\t" \
    "ds_read_b64_tr_b16 v[46:47], %[w1] offset:41984\n\t" \
    "v_fmac_f32 v18, v14, v14\n\t" \
    "v_fmac_f32 v19, v15, v15\n\t" \
    "v_lshlrev_b32 v16, 16, v49\n\t" \
    "v_and_b32 v17, 0xffff0000, v49\n\t" \
    "v_mfma_f32_16x16x32_bf16 v[24:27], v[20:23], v[48:51], v[24:27]\n\t" \
    "ds_read_b128 v[36:39], %[y2] offset:32768\n\t" \
    "v_fmac_f32 v18, v16, v16\n\t" \
    "v_mfma_f32_32x32x16_bf16 v[96:111], v[52:55], v[56:59], v[96:111]\n\t" \
    "v_fmac_f32 v19, v17, v17\n\t" \
    "v_lshlrev_b32 v14, 16, v50\n\t" \
    "v_and_b32 v15, 0xffff0000, v50\n\t" \
    "v_fmac_f32 v18, v14, v14\n\t" \
    "v_fmac_f32 v19, v15, v15\n\t" \
    "v_mfma_f32_32x32x16_bf16 v[112:127], v[52:55], v[60:63], v[112:127]\n\t" \
    "v_lshlrev_b32 v16, 16, v51\n\t" \
    "v_and_b32 v17, 0xffff0000, v51\n\t" \
    "v_fmac_f32 v18, v16, v16\n\t" \
    "v_fmac_f32 v19, v17, v17\n\t" \
    "v_mfma_f32_16x16x32_bf16 v[28:31], v[20:23], v[56:59], v[28:31]\n\t" \
    "v_xor_b32 %[x1], 0x10000, %[x1]\n\t" \
    "v_xor_b32 %[y1], 0x10000, %[y1]\n\t" \
    "s_waitcnt lgkmcnt(0)\n\t" \
    "v_mfma_f32_32x32x16_bf16 v[64:79], v[32:35], v[40:43], v[64:79]\n\t" \
    "ds_read_b128 v[48:51], %[x3] offset:32768\n\t" \
    "ds_read_b64_tr_b16 v[56:57], %[w0] offset:45056\n\t" \
    "ds_read_b64_tr_b16 v[58:59], %[w0] offset:46080\n\t" \
    "v_lshlrev_b32 v14, 16, v32\n\t" \
    "v_and_b32 v15, 0xffff0000, v32\n\t" \
    "v_mfma_f32_32x32x16_bf16 v[80:95], v[32:35], v[44:47], v[80:95]\n\t" \
    "ds_read_b64_tr_b16 v[60:61], %[w1] offset:45056\n\t" \
    "ds_read_b64_tr_b16 v[62:63], %[w1] offset:46080\n\t" \
    "v_fmac_f32 v18, v14, v14\n\t" \
    "v_fmac_f32 v19, v15, v15\n\t" \
    "v_lshlrev_b32 v16, 16, v33\n\t" \
    "v_and_b32 v17, 0xffff0000, v33\n\t" \
    "v_mfma_f32_16x16x32_bf16 v[24:27], v[20:23], v[32:35], v[24:27]\n\t" \
    "ds_read_b128 v[52:55], %[y3] offset:32768\n\t" \
    "v_fmac_f32 v18, v16, v16\n\t" \
    "v_mfma_f32_32x32x16_bf16 v[96:111], v[36:39], v[40:43], v[96:111]\n\t" \
    "v_fmac_f32 v19, v17, v17\n\t" \
    "v_lshlrev_b32 v14, 16, v34\n\t" \
    "v_and_b32 v15, 0xffff0000, v34\n\t" \
    "v_fmac_f32 v18, v14, v14\n\t" \
    "v_fmac_f32 v19, v15, v15\n\t" \
    "v_mfma_f32_32x32x16_bf16 v[112:127], v[36:39], v[44:47], v[112:127]\n\t" \
    "v_lshlrev_b32 v16, 16, v35\n\t" \
    "v_and_b32 v17, 0xffff0000, v35\n\t" \
    "v_fmac_f32 v18, v16, v16\n\t" \
    "v_fmac_f32 v19, v17, v17\n\t" \
    "v_mfma_f32_16x16x32_bf16 v[28:31], v[20:23], v[40:43], v[28:31]\n\t" \
    "v_xor_b32 %[w0], 0x10000, %[w0]\n\t" \
    "v_xor_b32 %[w1], 0x10000, %[w1]\n\t" \
    "s_waitcnt lgkmcnt(0)\n\t" \
    "s_barrier\n\t" \
    "v_mfma_f32_32x32x16_bf16 v[64:79], v[48:51], v[56:59], v[64:79]\n\t" \
    "ds_read_b128 v[32:35], %[x0] offset:0\n\t" \
    "ds_read_b64_tr_b16 v[40:41], %[w0] offset:0\n\t" \
    "ds_read_b64_tr_b16 v[42:43], %[w0] offset:1024\n\t" \
    "v_lshlrev_b32 v14, 16, v48\n\t" \
    "v_and_b32 v15, 0xffff0000, v48\n\t" \
    "v_mfma_f32_32x32x16_bf16 v[80:95], v[48:51], v[60:63], v[80:95]\n\t" \
    "ds_read_b64_tr_b16 v[44:45], %[w1] offset:0\n\t" \
    "ds_read_b64_tr_b16 v[46:47], %[w1] offset:1024\n\t" \
    "v_fmac_f32 v18, v14, v14\n\t" \
    "v_fmac_f32 v19, v15, v15\n\t" \
    "v_lshlrev_b32 v16, 16, v49\n\t" \
    "v_and_b32 v17, 0xffff0000, v49\n\t" \
    "v_mfma_f32_16x16x32_bf16 v[24:27], v[20:23], v[48:51], v[24:27]\n\t" \
    "ds_read_b128 v[36:39], %[y0] offset:0\n\t" \
    "v_fmac_f32 v18, v16, v16\n\t" \
    "v_mfma_f32_32x32x16_bf16 v[96:111], v[52:55], v[56:59], v[96:111]\n\t" \
    "v_fmac_f32 v19, v17, v17\n\t" \
    "v_lshlrev_b32 v14, 16, v50\n\t" \
    "v_and_b32 v15, 0xffff0000, v50\n\t" \
    "v_fmac_f32 v18, v14, v14\n\t" \
    "v_fmac_f32 v19, v15, v15\n\t" \
    "v_mfma_f32_32x32x16_bf16 v[112:127], v[52:55], v[60:63], v[112:127]\n\t" \
    "v_lshlrev_b32 v16, 16, v51\n\t" \
    "v_and_b32 v17, 0xffff0000, v51\n\t" \
    "v_fmac_f32 v18, v16, v16\n\t" \
    "v_fmac_f32 v19, v17, v17\n\t" \
    "v_mfma_f32_16x16x32_bf16 v[28:31], v[20:23], v[56:59], v[28:31]\n\t" \
    "v_xor_b32 %[x2], 0x10000, %[x2]\n\t" \
    "v_xor_b32 %[y2], 0x10000, %[y2]\n\t" \
    "v_xor_b32 %[x3], 0x10000, %[x3]\n\t" \
    "v_xor_b32 %[y3], 0x10000, %[y3]\n\t" \
    "s_sub_u32 %[cnt], %[cnt], 1\n\t" \
    "s_cbranch_scc0 1b\n\t" \
    "s_waitcnt lgkmcnt(0)\n\t" \
    "s_nop 15\n\t"

constexpr int A_STAGE = 32768, A_IMG = 2 * A_STAGE, A_TAB = 4 * A_STAGE, A_END = A_TAB + 4096;

constexpr int quad_role(int head, int dq) { return head == 19 ? (dq == 0 ? 4 : dq - 1) : head == 25 ? (dq == 2 ? 4 : dq > 2 ? dq - 1 : dq) : dq; }
constexpr unsigned role_mask(int cgm, bool single) {
    unsigned m = 0;
    for (int n = 0; n < 32; ++n) { const int q = 32 * cgm + n, r = quad_role(q / 5, q % 5); m |= (single ? r == 4 : (r & 1) != 0) ? 1u << n : 0u; }
    return m;
}
template <bool S> DI unsigned role_bits(int cgm) {
    return cgm == 0 ? role_mask(0, S) : cgm == 1 ? role_mask(1, S) : cgm == 2 ? role_mask(2, S) : cgm == 3 ? role_mask(3, S) : role_mask(4, S);
}

template <bool ISV>
DI void qkv_consumer(char* ws, lptr lds, int tb, int cg, int w, int lane STARG) {
    const int th = w >> 1, fh = w & 1, hi = lane >> 5, l31 = lane & 31;
    unsigned xp[2][4], wp[2];
    const unsigned ldsb = (unsigned)(size_t)lds;
#pragma unroll
    for (int ks = 0; ks < 4; ++ks) {
        const int c = (((2 * ks + hi) ^ ((l31 >> 1) & 7)) << 4);
        xp[0][ks] = ldsb + (64 * th + 32 * fh + l31) * 128 + c;
        xp[1][ks] = ldsb + (64 * th + 32 * (1 - fh) + l31) * 128 + c;
    }
    {
        const int q = (lane >> 2) & 3, p = lane & 3, cb = (lane >> 4) & 1;
#pragma unroll
        for (int f = 0; f < 2; ++f) {
            const int ft = 2 * fh + (f == 0 ? th : 1 - th);
            wp[f] = ldsb + 16384 + (8 * hi + q) * 256 + (((4 * (ft ^ q)) + 2 * cb + (p >> 1)) << 4) + 8 * (p & 1);
        }
    }
    int oa[2][4];
    if constexpr (!ISV) {
        const int cgm = cg < 5 ? cg : cg - 5;
        const unsigned ms = role_bits<true>(cgm) >> (16 * fh + hi), mo = role_bits<false>(cgm) >> (16 * fh + hi);
        const int o0 = A_IMG + (2 * th + fh) * 8192 + (16 * fh + hi) * 256 + l31 * 16;
#pragma unroll
        for (int f = 0; f < 2; ++f)
#pragma unroll
            for (int g = 0; g < 4; ++g) {
                const int n = 8 * (f == 0 ? th : 1 - th) + 2 * g;
                oa[f][g] = o0 + n * 256 - ((ms >> n) & 1) * (l31 * 8) - ((mo >> n) & 1) * 248;
                pin(oa[f][g]);
            }
    } else {
#pragma unroll
        for (int f = 0; f < 2; ++f) {
            const int col = 128 * (cg - 10) + 64 * fh + 32 * (f == 0 ? th : 1 - th) + l31, head = (col * 3277) >> 16;
            oa[f][0] = (head * 64 + 4 * tb + 2 * th) * KVREC + 1280 + hi * 320 + (col - 20 * head) * 16;
            pin(oa[f][0]);
        }
    }
    const unsigned selv = (lane & 15) == ((lane >> 4) & 1) ? 0x3f803f80u : 0u;
    u32x4 sel = {selv, selv, selv, selv};
    f32x16 acc[2][2] = {};
    f32x4 sx = {}, sw = {};
    float s2a = 0.f, s2b = 0.f;
    u32x4 x00, x01, w00, w01, x10, x11, w10, w11;
    unsigned cnt = 4;
    asm volatile("" : "+{v[64:79]}"(acc[0][0]), "+{v[80:95]}"(acc[0][1]), "+{v[96:111]}"(acc[1][0]), "+{v[112:127]}"(acc[1][1]), "+{v[24:27]}"(sx), "+{v[28:31]}"(sw), "+{v[20:23]}"(sel));
    ST(1);
    lds_bar();
    ST(2);
    {
        x00 = lds128((lptr)(size_t)xp[0][0]); x01 = lds128((lptr)(size_t)xp[1][0]);
        const u32x2 a = ldstr((lptr)(size_t)wp[0]), b = ldstr((lptr)(size_t)(wp[0] + 1024)), c = ldstr((lptr)(size_t)wp[1]), d = ldstr((lptr)(size_t)(wp[1] + 1024));
        w00 = u32x4{a[0], a[1], b[0], b[1]}; w01 = u32x4{c[0], c[1], d[0], d[1]};
    }
#define QKV_OPERANDS \
        : "+{v[64:79]}"(acc[0][0]), "+{v[80:95]}"(acc[0][1]), "+{v[96:111]}"(acc[1][0]), "+{v[112:127]}"(acc[1][1]), "+{v[24:27]}"(sx), "+{v[28:31]}"(sw), \
          "+{v18}"(s2a), "+{v19}"(s2b), "+{v[32:35]}"(x00), "+{v[36:39]}"(x01), "+{v[40:43]}"(w00), "+{v[44:47]}"(w01), \
          [x0] "+v"(xp[0][0]), [x1] "+v"(xp[0][1]), [x2] "+v"(xp[0][2]), [x3] "+v"(xp[0][3]), \
          [y0] "+v"(xp[1][0]), [y1] "+v"(xp[1][1]), [y2] "+v"(xp[1][2]), [y3] "+v"(xp[1][3]), [w0] "+v"(wp[0]), [w1] "+v"(wp[1]), [cnt] "+s"(cnt) \
        : "{v[20:23]}"(sel) \
        : "memory", "scc", "v14", "v15", "v16", "v17", "v48", "v49", "v50", "v51", "v52", "v53", "v54", "v55", "v56", "v57", "v58", "v59", "v60", "v61", "v62", "v63"
    if constexpr (ISV) asm volatile(QKV_LOOP_V QKV_OPERANDS);
    else asm volatile(QKV_LOOP_QK QKV_OPERANDS);
    ST(3);
    LAS float* T1 = (LAS float*)(lds + A_TAB);
    LAS float* T2 = T1 + 128;
    LAS float* TC = T1 + 256;
    LAS f32x2* TS = (LAS f32x2*)(T1 + 384) + w * 64;
    const float s2 = swap32_add(s2a + s2b);
    if (hi == 0) T2[64 * th + 32 * fh + l31] = s2;
    if (lane < 16) {
        T1[64 * th + 32 * fh + lane] = sx[0]; T1[64 * th + 32 * fh + 16 + lane] = sx[1];
        TC[64 * fh + 32 * th + lane] = sw[0]; TC[64 * fh + 32 * th + 16 + lane] = sw[1];
    }
    lds_bar();
    if constexpr (!ISV) {
        f32x4 c4[2][4];
        float t1[2], t2[2];
#pragma unroll
        for (int t = 0; t < 2; ++t) { t1[t] = T1[64 * th + 32 * (t == 0 ? fh : 1 - fh) + l31]; t2[t] = T2[64 * th + 32 * (t == 0 ? fh : 1 - fh) + l31]; }
#pragma unroll
        for (int f = 0; f < 2; ++f)
#pragma unroll
            for (int g = 0; g < 4; ++g) c4[f][g] = *(LAS f32x4*)(TC + 64 * fh + 32 * (f == 0 ? th : 1 - th) + 8 * g + 4 * hi);
        const float sc = cg < 5 ? 0.22360679774997896f * 1.4426950408889634f : 1.0f;
        const int flip = fh ? -8192 : 8192;
#pragma unroll
        for (int t = 0; t < 2; ++t) {
            float m, rs;
            ln_stats(t1[t], t2[t], m, rs);
            rs *= sc;
            const float mr = -m * rs;
#pragma unroll
            for (int f = 0; f < 2; ++f)
#pragma unroll
                for (int g = 0; g < 4; ++g) {
                    const f32x4 v = f32x4{acc[t][f][4 * g], acc[t][f][4 * g + 1], acc[t][f][4 * g + 2], acc[t][f][4 * g + 3]} * rs + c4[f][g] * mr;
                    *(LAS u32x2*)(lds + oa[f][g] + t * flip) = u32x2{pk2(v[0], v[1]), pk2(v[2], v[3])};
                }
        }
    } else {
        {
            float m, rs;
            ln_stats(T1[64 * th + lane], T2[64 * th + lane], m, rs);
            TS[lane] = f32x2{-m * rs, rs};
        }
        const float c0 = TC[64 * fh + 32 * th + l31], c1 = TC[64 * fh + 32 * (1 - th) + l31];
        asm volatile("s_waitcnt lgkmcnt(0)" ::: "memory");
#pragma unroll
        for (int t = 0; t < 2; ++t) {
            const int ta = t == 0 ? fh : 1 - fh;
            f32x4 sa[4], sb[4];
#pragma unroll
            for (int g = 0; g < 4; ++g) { sa[g] = *(LAS f32x4*)(TS + 32 * ta + 8 * g + 4 * hi); sb[g] = *(LAS f32x4*)(TS + 32 * ta + 8 * g + 4 * hi + 2); }
#pragma unroll
            for (int f = 0; f < 2; ++f) {
                const float c = f == 0 ? c0 : c1;
#pragma unroll
                for (int s = 0; s < 2; ++s) {
                    u32x4 o;
#pragma unroll
                    for (int g2 = 0; g2 < 2; ++g2) {
                        const int g = 2 * s + g2;
                        o[2 * g2] = pk2(fmaf(acc[t][f][4 * g], sa[g][1], sa[g][0] * c), fmaf(acc[t][f][4 * g + 1], sa[g][3], sa[g][2] * c));
                        o[2 * g2 + 1] = pk2(fmaf(acc[t][f][4 * g + 2], sb[g][1], sb[g][0] * c), fmaf(acc[t][f][4 * g + 3], sb[g][3], sb[g][2] * c));
                    }
                    gst128(oa[f][0], ws + WS_KV + ta * KVREC + s * 640, o);
                }
            }
        }
    }
}

DI void qkv_loader(const u16* x, const u16* wqkv, lptr lds, int tb, int cg, int j, int lane) {
    unsigned vx[4];
#pragma unroll
    for (int i = 0; i < 4; ++i) {
        const int row = 32 * j + 8 * i + (lane >> 3);
        vx[i] = row * 1280 + (((lane & 7) ^ ((row >> 1) & 7)) << 4);
    }
    unsigned vw[4];
#pragma unroll
    for (int i = 0; i < 4; ++i) vw[i] = (16 * j + 4 * i + (lane >> 4)) * 3840 + (((lane & 15) ^ ((lane >> 4) << 2)) << 4);
    const char* xs = (const char*)x + (size_t)tb * 128 * 1280;
    const char* wsrc = (const char*)wqkv + cg * 256;
    const unsigned ldw = (unsigned)(size_t)lds + j * 4096;
#define A_ROT 0
#define A_DEPTH 2
    const int rot = A_ROT ? (cg + 3 * tb) % 10 : 0;
    auto issue = [&](int s) {
        const unsigned dst = ldw + (s & 3) * A_STAGE;
        const int k = s + rot >= 10 ? s + rot - 10 : s + rot;
        sfor<0, 4>([&](auto I) { glds<I.value * 1024>(vx[I.value], xs + k * 128, dst); glds<16384 + I.value * 1024>(vw[I.value], wsrc + (size_t)k * 64 * 3840, dst); });
    };
    issue(0); issue(1);
    wait_vm_bar<8>();
#pragma unroll 1
    for (int s = 2; s < A_DEPTH; ++s) issue(s);
#pragma unroll 1
    for (int s = A_DEPTH; s < 10; ++s) { issue(s); wait_vm_bar<8 * (A_DEPTH - 1)>(); }
    sfor<0, A_DEPTH - 1>([&](auto I) { wait_vm_bar<8 * (A_DEPTH - 2 - I.value)>(); });
    lds_bar();
    lds_bar();
}

DI void qkv_phase(const u16* x, const u16* wqkv, char* ws, lptr lds, int tb, int cg, int w, int lane STARG) {
    if (w >= 4) qkv_loader(x, wqkv, lds, tb, cg, w - 4, lane);
    else if (cg >= 10) qkv_consumer<true>(ws, lds, tb, cg, w, lane STPASS);
    else qkv_consumer<false>(ws, lds, tb, cg, w, lane STPASS);
    if (cg < 10) {
        const int cgm = cg < 5 ? cg : cg - 5, rec = cg < 5 ? QREC : KVREC;
        const char* dst = ws + (cg < 5 ? WS_Q : WS_KV) + (size_t)4 * tb * rec;
        const int nq = 4 * w + (lane >> 4), gq = 32 * cgm + nq, head = (gq * 205) >> 10, role = quad_role(head, gq - 5 * head);
        const unsigned off = head * 64 * rec + role * 256 + (lane & 15) * 16;
        lds_bar();
        u32x4 v[4];
#pragma unroll
        for (int t = 0; t < 4; ++t) v[t] = lds128(lds + A_IMG + t * 8192 + w * 1024 + lane * 16);
#pragma unroll
        for (int t = 0; t < 4; ++t) gst128(off, dst + t * rec, v[t]);
    }
}

#define ATTN_LOOP_ASM \
    "s_nop 4\n\t" \
    "1:\n\t" \
    "v_mfma_f32_32x32x16_bf16 v[64:79], v[156:159], v[144:147], 0\n\t" \
    "ds_read_b128 v[160:163], %[va] offset:0\n\t" \
    "v_exp_f32 v80, v80\n\t" \
    "v_exp_f32 v81, v81\n\t" \
    "v_exp_f32 v82, v82\n\t" \
    "v_cvt_pk_bf16_f32 v136, v80, v81\n\t" \
    "v_exp_f32 v83, v83\n\t" \
    "v_mfma_f32_32x32x8_bf16 v[64:79], v[164:165], v[152:153], v[64:79]\n\t" \
    "v_cvt_pk_bf16_f32 v137, v82, v83\n\t" \
    "v_exp_f32 v84, v84\n\t" \
    "v_exp_f32 v85, v85\n\t" \
    "v_exp_f32 v86, v86\n\t" \
    "v_cvt_pk_bf16_f32 v138, v84, v85\n\t" \
    "v_exp_f32 v87, v87\n\t" \
    "v_mfma_f32_32x32x16_bf16 v[96:111], v[176:179], v[128:131], v[96:111]\n\t" \
    "ds_read_b64 v[166:167], %[vb] offset:1024\n\t" \
    "v_cvt_pk_bf16_f32 v139, v86, v87\n\t" \
    "v_exp_f32 v88, v88\n\t" \
    "v_exp_f32 v89, v89\n\t" \
    "v_exp_f32 v90, v90\n\t" \
    "v_cvt_pk_bf16_f32 v140, v88, v89\n\t" \
    "v_exp_f32 v91, v91\n\t" \
    "v_mfma_f32_32x32x16_bf16 v[96:111], v[180:183], v[132:135], v[96:111]\n\t" \
    "ds_read_b128 v[168:171], %[va] offset:1280\n\t" \
    "v_cvt_pk_bf16_f32 v141, v90, v91\n\t" \
    "v_exp_f32 v92, v92\n\t" \
    "v_exp_f32 v93, v93\n\t" \
    "v_exp_f32 v94, v94\n\t" \
    "v_exp_f32 v95, v95\n\t" \
    "v_cvt_pk_bf16_f32 v142, v92, v93\n\t" \
    "v_cvt_pk_bf16_f32 v143, v94, v95\n\t" \
    "v_mfma_f32_32x32x16_bf16 v[80:95], v[156:159], v[148:151], 0\n\t" \
    "ds_read_b128 v[172:175], %[va] offset:2304\n\t" \
    "v_exp_f32 v64, v64\n\t" \
    "v_exp_f32 v65, v65\n\t" \
    "v_exp_f32 v66, v66\n\t" \
    "v_cvt_pk_bf16_f32 v128, v64, v65\n\t" \
    "v_exp_f32 v67, v67\n\t" \
    "v_mfma_f32_32x32x8_bf16 v[80:95], v[164:165], v[154:155], v[80:95]\n\t" \
    "v_cvt_pk_bf16_f32 v129, v66, v67\n\t" \
    "v_exp_f32 v68, v68\n\t" \
    "v_exp_f32 v69, v69\n\t" \
    "v_exp_f32 v70, v70\n\t" \
    "v_cvt_pk_bf16_f32 v130, v68, v69\n\t" \
    "v_exp_f32 v71, v71\n\t" \
    "v_mfma_f32_32x32x16_bf16 v[112:127], v[176:179], v[136:139], v[112:127]\n\t" \
    "v_cvt_pk_bf16_f32 v131, v70, v71\n\t" \
    "v_exp_f32 v72, v72\n\t" \
    "v_exp_f32 v73, v73\n\t" \
    "v_exp_f32 v74, v74\n\t" \
    "v_cvt_pk_bf16_f32 v132, v72, v73\n\t" \
    "v_exp_f32 v75, v75\n\t" \
    "v_mfma_f32_32x32x16_bf16 v[112:127], v[180:183], v[140:143], v[112:127]\n\t" \
    "s_waitcnt lgkmcnt(0)\n\t" \
    "v_cvt_pk_bf16_f32 v133, v74, v75\n\t" \
    "v_exp_f32 v76, v76\n\t" \
    "v_exp_f32 v77, v77\n\t" \
    "v_exp_f32 v78, v78\n\t" \
    "v_exp_f32 v79, v79\n\t" \
    "v_cvt_pk_bf16_f32 v134, v76, v77\n\t" \
    "v_cvt_pk_bf16_f32 v135, v78, v79\n\t" \
    "v_mfma_f32_32x32x16_bf16 v[64:79], v[160:163], v[144:147], 0\n\t" \
    "ds_read_b128 v[156:159], %[va] offset:3328\n\t" \
    "v_exp_f32 v80, v80\n\t" \
    "v_exp_f32 v81, v81\n\t" \
    "v_exp_f32 v82, v82\n\t" \
    "v_cvt_pk_bf16_f32 v136, v80, v81\n\t" \
    "v_exp_f32 v83, v83\n\t" \
    "v_mfma_f32_32x32x8_bf16 v[64:79], v[166:167], v[152:153], v[64:79]\n\t" \
    "v_cvt_pk_bf16_f32 v137, v82, v83\n\t" \
    "v_exp_f32 v84, v84\n\t" \
    "v_exp_f32 v85, v85\n\t" \
    "v_exp_f32 v86, v86\n\t" \
    "v_cvt_pk_bf16_f32 v138, v84, v85\n\t" \
    "v_exp_f32 v87, v87\n\t" \
    "v_mfma_f32_32x32x16_bf16 v[96:111], v[168:171], v[128:131], v[96:111]\n\t" \
    "ds_read_b64 v[164:165], %[vb] offset:4352\n\t" \
    "v_cvt_pk_bf16_f32 v139, v86, v87\n\t" \
    "v_exp_f32 v88, v88\n\t" \
    "v_exp_f32 v89, v89\n\t" \
    "v_exp_f32 v90, v90\n\t" \
    "v_cvt_pk_bf16_f32 v140, v88, v89\n\t" \
    "v_exp_f32 v91, v91\n\t" \
    "v_mfma_f32_32x32x16_bf16 v[96:111], v[172:175], v[132:135], v[96:111]\n\t" \
    "ds_read_b128 v[176:179], %[va] offset:4608\n\t" \
    "v_cvt_pk_bf16_f32 v141, v90, v91\n\t" \
    "v_exp_f32 v92, v92\n\t" \
    "v_exp_f32 v93, v93\n\t" \
    "v_exp_f32 v94, v94\n\t" \
    "v_exp_f32 v95, v95\n\t" \
    "v_cvt_pk_bf16_f32 v142, v92, v93\n\t" \
    "v_cvt_pk_bf16_f32 v143, v94, v95\n\t" \
    "v_mfma_f32_32x32x16_bf16 v[80:95], v[160:163], v[148:151], 0\n\t" \
    "ds_read_b128 v[180:183], %[va] offset:5632\n\t" \
    "v_exp_f32 v64, v64\n\t" \
    "v_exp_f32 v65, v65\n\t" \
    "v_exp_f32 v66, v66\n\t" \
    "v_cvt_pk_bf16_f32 v128, v64, v65\n\t" \
    "v_exp_f32 v67, v67\n\t" \
    "v_mfma_f32_32x32x8_bf16 v[80:95], v[166:167], v[154:155], v[80:95]\n\t" \
    "v_cvt_pk_bf16_f32 v129, v66, v67\n\t" \
    "v_exp_f32 v68, v68\n\t" \
    "v_exp_f32 v69, v69\n\t" \
    "v_exp_f32 v70, v70\n\t" \
    "v_cvt_pk_bf16_f32 v130, v68, v69\n\t" \
    "v_exp_f32 v71, v71\n\t" \
    "v_mfma_f32_32x32x16_bf16 v[112:127], v[168:171], v[136:139], v[112:127]\n\t" \
    "v_cvt_pk_bf16_f32 v131, v70, v71\n\t" \
    "v_exp_f32 v72, v72\n\t" \
    "v_exp_f32 v73, v73\n\t" \
    "v_exp_f32 v74, v74\n\t" \
    "v_cvt_pk_bf16_f32 v132, v72, v73\n\t" \
    "v_exp_f32 v75, v75\n\t" \
    "v_mfma_f32_32x32x16_bf16 v[112:127], v[172:175], v[140:143], v[112:127]\n\t" \
    "s_waitcnt lgkmcnt(0)\n\t" \
    "v_cvt_pk_bf16_f32 v133, v74, v75\n\t" \
    "v_exp_f32 v76, v76\n\t" \
    "v_exp_f32 v77, v77\n\t" \
    "v_exp_f32 v78, v78\n\t" \
    "v_exp_f32 v79, v79\n\t" \
    "v_cvt_pk_bf16_f32 v134, v76, v77\n\t" \
    "v_cvt_pk_bf16_f32 v135, v78, v79\n\t" \
    "v_mfma_f32_32x32x16_bf16 v[64:79], v[156:159], v[144:147], 0\n\t" \
    "ds_read_b128 v[160:163], %[va] offset:6656\n\t" \
    "v_exp_f32 v80, v80\n\t" \
    "v_exp_f32 v81, v81\n\t" \
    "v_exp_f32 v82, v82\n\t" \
    "v_cvt_pk_bf16_f32 v136, v80, v81\n\t" \
    "v_exp_f32 v83, v83\n\t" \
    "v_mfma_f32_32x32x8_bf16 v[64:79], v[164:165], v[152:153], v[64:79]\n\t" \
    "v_cvt_pk_bf16_f32 v137, v82, v83\n\t" \
    "v_exp_f32 v84, v84\n\t" \
    "v_exp_f32 v85, v85\n\t" \
    "v_exp_f32 v86, v86\n\t" \
    "v_cvt_pk_bf16_f32 v138, v84, v85\n\t" \
    "v_exp_f32 v87, v87\n\t" \
    "v_mfma_f32_32x32x16_bf16 v[96:111], v[176:179], v[128:131], v[96:111]\n\t" \
    "ds_read_b64 v[166:167], %[vb] offset:7680\n\t" \
    "v_cvt_pk_bf16_f32 v139, v86, v87\n\t" \
    "v_exp_f32 v88, v88\n\t" \
    "v_exp_f32 v89, v89\n\t" \
    "v_exp_f32 v90, v90\n\t" \
    "v_cvt_pk_bf16_f32 v140, v88, v89\n\t" \
    "v_exp_f32 v91, v91\n\t" \
    "v_mfma_f32_32x32x16_bf16 v[96:111], v[180:183], v[132:135], v[96:111]\n\t" \
    "ds_read_b128 v[168:171], %[va] offset:7936\n\t" \
    "v_cvt_pk_bf16_f32 v141, v90, v91\n\t" \
    "v_exp_f32 v92, v92\n\t" \
    "v_exp_f32 v93, v93\n\t" \
    "v_exp_f32 v94, v94\n\t" \
    "v_exp_f32 v95, v95\n\t" \
    "v_cvt_pk_bf16_f32 v142, v92, v93\n\t" \
    "v_cvt_pk_bf16_f32 v143, v94, v95\n\t" \
    "v_mfma_f32_32x32x16_bf16 v[80:95], v[156:159], v[148:151], 0\n\t" \
    "ds_read_b128 v[172:175], %[va] offset:8960\n\t" \
    "v_exp_f32 v64, v64\n\t" \
    "v_exp_f32 v65, v65\n\t" \
    "v_exp_f32 v66, v66\n\t" \
    "v_cvt_pk_bf16_f32 v128, v64, v65\n\t" \
    "v_exp_f32 v67, v67\n\t" \
    "v_mfma_f32_32x32x8_bf16 v[80:95], v[164:165], v[154:155], v[80:95]\n\t" \
    "v_cvt_pk_bf16_f32 v129, v66, v67\n\t" \
    "v_exp_f32 v68, v68\n\t" \
    "v_exp_f32 v69, v69\n\t" \
    "v_exp_f32 v70, v70\n\t" \
    "v_cvt_pk_bf16_f32 v130, v68, v69\n\t" \
    "v_exp_f32 v71, v71\n\t" \
    "v_mfma_f32_32x32x16_bf16 v[112:127], v[176:179], v[136:139], v[112:127]\n\t" \
    "v_cvt_pk_bf16_f32 v131, v70, v71\n\t" \
    "v_exp_f32 v72, v72\n\t" \
    "v_exp_f32 v73, v73\n\t" \
    "v_exp_f32 v74, v74\n\t" \
    "v_cvt_pk_bf16_f32 v132, v72, v73\n\t" \
    "v_exp_f32 v75, v75\n\t" \
    "v_mfma_f32_32x32x16_bf16 v[112:127], v[180:183], v[140:143], v[112:127]\n\t" \
    "s_waitcnt lgkmcnt(0)\n\t" \
    "v_cvt_pk_bf16_f32 v133, v74, v75\n\t" \
    "v_exp_f32 v76, v76\n\t" \
    "v_exp_f32 v77, v77\n\t" \
    "v_exp_f32 v78, v78\n\t" \
    "v_exp_f32 v79, v79\n\t" \
    "v_cvt_pk_bf16_f32 v134, v76, v77\n\t" \
    "v_cvt_pk_bf16_f32 v135, v78, v79\n\t" \
    "v_mfma_f32_32x32x16_bf16 v[64:79], v[160:163], v[144:147], 0\n\t" \
    "ds_read_b128 v[156:159], %[va] offset:9984\n\t" \
    "v_exp_f32 v80, v80\n\t" \
    "v_exp_f32 v81, v81\n\t" \
    "v_exp_f32 v82, v82\n\t" \
    "v_cvt_pk_bf16_f32 v136, v80, v81\n\t" \
    "v_exp_f32 v83, v83\n\t" \
    "v_mfma_f32_32x32x8_bf16 v[64:79], v[166:167], v[152:153], v[64:79]\n\t" \
    "v_cvt_pk_bf16_f32 v137, v82, v83\n\t" \
    "v_exp_f32 v84, v84\n\t" \
    "v_exp_f32 v85, v85\n\t" \
    "v_exp_f32 v86, v86\n\t" \
    "v_cvt_pk_bf16_f32 v138, v84, v85\n\t" \
    "v_exp_f32 v87, v87\n\t" \
    "v_mfma_f32_32x32x16_bf16 v[96:111], v[168:171], v[128:131], v[96:111]\n\t" \
    "ds_read_b64 v[164:165], %[vb] offset:11008\n\t" \
    "v_cvt_pk_bf16_f32 v139, v86, v87\n\t" \
    "v_exp_f32 v88, v88\n\t" \
    "v_exp_f32 v89, v89\n\t" \
    "v_exp_f32 v90, v90\n\t" \
    "v_cvt_pk_bf16_f32 v140, v88, v89\n\t" \
    "v_exp_f32 v91, v91\n\t" \
    "v_mfma_f32_32x32x16_bf16 v[96:111], v[172:175], v[132:135], v[96:111]\n\t" \
    "ds_read_b128 v[176:179], %[va] offset:11264\n\t" \
    "v_cvt_pk_bf16_f32 v141, v90, v91\n\t" \
    "v_exp_f32 v92, v92\n\t" \
    "v_exp_f32 v93, v93\n\t" \
    "v_exp_f32 v94, v94\n\t" \
    "v_exp_f32 v95, v95\n\t" \
    "v_cvt_pk_bf16_f32 v142, v92, v93\n\t" \
    "v_cvt_pk_bf16_f32 v143, v94, v95\n\t" \
    "v_mfma_f32_32x32x16_bf16 v[80:95], v[160:163], v[148:151], 0\n\t" \
    "ds_read_b128 v[180:183], %[va] offset:12288\n\t" \
    "v_exp_f32 v64, v64\n\t" \
    "v_exp_f32 v65, v65\n\t" \
    "v_exp_f32 v66, v66\n\t" \
    "v_cvt_pk_bf16_f32 v128, v64, v65\n\t" \
    "v_exp_f32 v67, v67\n\t" \
    "v_mfma_f32_32x32x8_bf16 v[80:95], v[166:167], v[154:155], v[80:95]\n\t" \
    "v_cvt_pk_bf16_f32 v129, v66, v67\n\t" \
    "v_exp_f32 v68, v68\n\t" \
    "v_exp_f32 v69, v69\n\t" \
    "v_exp_f32 v70, v70\n\t" \
    "v_cvt_pk_bf16_f32 v130, v68, v69\n\t" \
    "v_exp_f32 v71, v71\n\t" \
    "v_mfma_f32_32x32x16_bf16 v[112:127], v[168:171], v[136:139], v[112:127]\n\t" \
    "s_sub_u32 %[cnt], %[cnt], 1\n\t" \
    "v_cvt_pk_bf16_f32 v131, v70, v71\n\t" \
    "v_exp_f32 v72, v72\n\t" \
    "v_exp_f32 v73, v73\n\t" \
    "v_exp_f32 v74, v74\n\t" \
    "v_cvt_pk_bf16_f32 v132, v72, v73\n\t" \
    "v_exp_f32 v75, v75\n\t" \
    "v_mfma_f32_32x32x16_bf16 v[112:127], v[172:175], v[140:143], v[112:127]\n\t" \
    "s_waitcnt lgkmcnt(0)\n\t" \
    "s_barrier\n\t" \
    "v_cvt_pk_bf16_f32 v133, v74, v75\n\t" \
    "v_exp_f32 v76, v76\n\t" \
    "v_exp_f32 v77, v77\n\t" \
    "v_exp_f32 v78, v78\n\t" \
    "v_exp_f32 v79, v79\n\t" \
    "v_cvt_pk_bf16_f32 v134, v76, v77\n\t" \
    "v_cvt_pk_bf16_f32 v135, v78, v79\n\t" \
    "v_xor_b32 %[va], 0x4000, %[va]\n\t" \
    "v_xor_b32 %[vb], 0x4000, %[vb]\n\t" \
    "s_cbranch_scc0 1b\n\t" \
    "v_exp_f32 v80, v80\n\t" \
    "v_exp_f32 v81, v81\n\t" \
    "v_exp_f32 v82, v82\n\t" \
    "v_cvt_pk_bf16_f32 v136, v80, v81\n\t" \
    "v_exp_f32 v83, v83\n\t" \
    "s_nop 0\n\t" \
    "v_cvt_pk_bf16_f32 v137, v82, v83\n\t" \
    "v_exp_f32 v84, v84\n\t" \
    "v_exp_f32 v85, v85\n\t" \
    "v_exp_f32 v86, v86\n\t" \
    "v_cvt_pk_bf16_f32 v138, v84, v85\n\t" \
    "v_exp_f32 v87, v87\n\t" \
    "v_mfma_f32_32x32x16_bf16 v[96:111], v[176:179], v[128:131], v[96:111]\n\t" \
    "v_cvt_pk_bf16_f32 v139, v86, v87\n\t" \
    "v_exp_f32 v88, v88\n\t" \
    "v_exp_f32 v89, v89\n\t" \
    "v_exp_f32 v90, v90\n\t" \
    "v_cvt_pk_bf16_f32 v140, v88, v89\n\t" \
    "v_exp_f32 v91, v91\n\t" \
    "v_mfma_f32_32x32x16_bf16 v[96:111], v[180:183], v[132:135], v[96:111]\n\t" \
    "v_cvt_pk_bf16_f32 v141, v90, v91\n\t" \
    "v_exp_f32 v92, v92\n\t" \
    "v_exp_f32 v93, v93\n\t" \
    "v_exp_f32 v94, v94\n\t" \
    "v_exp_f32 v95, v95\n\t" \
    "v_cvt_pk_bf16_f32 v142, v92, v93\n\t" \
    "v_cvt_pk_bf16_f32 v143, v94, v95\n\t" \
    "s_nop 1\n\t" \
    "v_mfma_f32_32x32x16_bf16 v[112:127], v[176:179], v[136:139], v[112:127]\n\t" \
    "v_mfma_f32_32x32x16_bf16 v[112:127], v[180:183], v[140:143], v[112:127]\n\t" \
    "s_nop 15\n\t"

#define WARM1 1
#define WARM2 1
constexpr int L_WO = 40960, L_X = 143360, M_LDS = 163840;
constexpr int KV_SLOT = 16384, KV_LREC = 3328;

DI int up_start(int c) { return 27 * (c >> 2) + 7 * (c & 3); }

DI void attn_slow(const char* kv, int lane, u32x4 q16, u32x2 q8, f32x16& o) {
    const int hi = lane >> 5, l31 = lane & 31;
    const unsigned fill = l31 == 20 ? 0x3f803f80u : 0u;
    o = f32x16{};
    float m = -3.0e38f;
#pragma unroll 1
    for (int t = 0; t < 64; ++t) {
        const char* r = kv + t * KVREC;
        const u32x4 k16 = *(const u32x4*)(r + lane * 16);
        const u32x2 k8 = *(const u32x2*)(r + 1024 + l31 * 8);
        u32x4 v0 = {fill, fill, fill, fill}, v1 = v0;
        if (l31 < 20) { v0 = *(const u32x4*)(r + 1280 + hi * 320 + l31 * 16); v1 = *(const u32x4*)(r + 1920 + hi * 320 + l31 * 16); }
        f32x16 s = mfma32k8(k8, q8, mfma32(k16, q16, f32x16{}));
        float mx = s[0];
#pragma unroll
        for (int i = 1; i < 16; ++i) mx = fmaxf(mx, s[i]);
        const float mn = fmaxf(m, swap32_max(mx));
        const float al = __builtin_amdgcn_exp2f(m - mn);
        m = mn;
        u32x4 p0, p1;
#pragma unroll
        for (int i = 0; i < 4; ++i) {
            p0[i] = pk2(__builtin_amdgcn_exp2f(s[2 * i] - mn), __builtin_amdgcn_exp2f(s[2 * i + 1] - mn));
            p1[i] = pk2(__builtin_amdgcn_exp2f(s[8 + 2 * i] - mn), __builtin_amdgcn_exp2f(s[9 + 2 * i] - mn));
        }
#pragma unroll
        for (int i = 0; i < 16; ++i) o[i] *= al;
        o = mfma32(v1, p1, mfma32(v0, p0, o));
    }
}

constexpr float GELU_C0 = -2.0f * 0.7978845608028654f * 1.4426950408889634f, GELU_C1 = 0.044715f * GELU_C0;

typedef __attribute__((address_space(1))) unsigned gu32;
typedef __attribute__((address_space(1))) unsigned long long gu64;
typedef unsigned long long u64;
DI void flag_set(char* ws, int off, u64 tag) { __hip_atomic_store((gu64*)(ws + off), tag, __ATOMIC_RELAXED, __HIP_MEMORY_SCOPE_AGENT); }
DI u64 flag_get(char* ws, int off) { return __hip_atomic_load((gu64*)(ws + off), __ATOMIC_RELAXED, __HIP_MEMORY_SCOPE_AGENT); }
DI void flag_wait(char* ws, int off, u64 tag) {
#pragma unroll 1
    for (int spin = 0; spin < (1 << 20); ++spin) {
        if (__all(flag_get(ws, off) == tag)) break;
        __builtin_amdgcn_s_sleep(2);
    }
}
DI void flag_wait2(char* ws, int off0, int off1, u64 tag) {
#pragma unroll 1
    for (int spin = 0; spin < (1 << 20); ++spin) {
        const u64 u = flag_get(ws, off0), v = flag_get(ws, off1);
        if (__all(u == tag && v == tag)) break;
        __builtin_amdgcn_s_sleep(2);
    }
}

constexpr int nld(int k, int first2, int per) { return k >= first2 ? 2 * per : per; }
constexpr int nld_o(int k) { return k % 5 == 4 ? 2 : 1; }

DI void attn_consumer(char* ws, lptr lds, int b, int w, int lane, u64 tag STARG) {
    const int hi = lane >> 5, l31 = lane & 31, tk = lane & 15;
    const unsigned vl = lane * 16;
    const int head = 4 * (b & 7) + ((b >> 3) & 3), tta = 8 * (b >> 5) + 2 * w;
    const char* qa = ws + WS_Q + (size_t)(head * 64 + tta) * QREC;
    const char* kv = ws + WS_KV + (size_t)head * 64 * KVREC;
    const unsigned v8 = 1024 + l31 * 8;
    u32x4 QA16, QB16, K16a, V0a = {}, V1a = {}, V0b = {}, V1b = {};
    u32x2 QA8, QB8, K8a;
    f32x16 SB, OA = {}, OB = {};
    u32x8 PA = {}, PB = {};
#pragma unroll
    for (int i = 0; i < 16; ++i) SB[i] = -1.0e30f;
    asm volatile("" : "+{v[80:95]}"(SB), "+{v[96:111]}"(OA), "+{v[112:127]}"(OB), "+{v[128:135]}"(PA), "+{v[136:143]}"(PB),
                      "+{v[168:171]}"(V0a), "+{v[172:175]}"(V1a), "+{v[176:179]}"(V0b), "+{v[180:183]}"(V1b));
    if (w == 0) {
        const int ca = (5 * head) >> 5, cb = (5 * head + 4) >> 5, cs = (lane & 16) ? cb : ca;
        flag_wait2(ws, FL_A + ((5 + 5 * hi + cs) * 16 + tk) * 128, FL_A + (cs * 16 + 2 * (b >> 5) + (lane & 1)) * 128, tag);
    }
    ST(5);
    lds_bar();
    ST(6);
    gld128s<0>(QA16, vl, qa); gld64s<0>(QA8, v8, qa);
    gld128s<QREC>(QB16, vl, qa); gld64s<QREC>(QB8, v8, qa);
    gld128s<0>(K16a, vl, kv); gld64s<0>(K8a, v8, kv);
    asm volatile("s_waitcnt vmcnt(0)" : "+v"(QA16), "+v"(QA8), "+v"(QB16), "+v"(QB8), "+v"(K16a), "+v"(K8a)::"memory");
    ST(7);
    if (hi) { QA8 = u32x2{0u, 0u}; QB8 = u32x2{0u, 0u}; }
    f32x16 SA = mfma32k8(K8a, QA8, mfma32(K16a, QA16, f32x16{}));
    f32x16 ST0 = mfma32k8(K8a, QB8, mfma32(K16a, QB16, f32x16{}));
    float ma = SA[0], mb = ST0[0];
#pragma unroll
    for (int i = 1; i < 16; ++i) { ma = fmaxf(ma, SA[i]); mb = fmaxf(mb, ST0[i]); }
    ma = swap32_max(ma); mb = swap32_max(mb);
    u32x2 RA8 = QA8, RB8 = QB8;
    if (hi) { RA8[0] = pk2(-ma, rbf(ma) - ma); RB8[0] = pk2(-mb, rbf(mb) - mb); K8a = u32x2{0x3f803f80u, 0x3f803f80u}; }
    unsigned va = (unsigned)(size_t)lds + vl, vb = (unsigned)(size_t)lds + (hi ? 576 : l31 * 8), cnt = 15;
    lds_bar();
    ST(8);
    asm volatile(ATTN_LOOP_ASM
                 : "+{v[64:79]}"(SA), "+{v[80:95]}"(SB), "+{v[96:111]}"(OA), "+{v[112:127]}"(OB), "+{v[128:135]}"(PA), "+{v[136:143]}"(PB),
                   "+{v[156:159]}"(K16a), "+{v[164:165]}"(K8a), "+{v[168:171]}"(V0a), "+{v[172:175]}"(V1a), "+{v[176:179]}"(V0b), "+{v[180:183]}"(V1b),
                   [va] "+v"(va), [vb] "+v"(vb), [cnt] "+s"(cnt)
                 : "{v[144:147]}"(QA16), "{v[148:151]}"(QB16), "{v[152:153]}"(RA8), "{v[154:155]}"(RB8)
                 : "memory", "scc", "v160", "v161", "v162", "v163", "v166", "v167");
    ST(9);
    bool ok = !hi || (OA[8] <= 1.8e19f && OB[8] <= 1.8e19f && OA[8] >= 1.0e-9f && OB[8] >= 1.0e-9f);
#pragma unroll
    for (int i = 0; i < 12; ++i) ok = ok && fabsf(OA[i]) <= 3.0e38f && fabsf(OB[i]) <= 3.0e38f;
    if (__builtin_expect(__any(!ok), 0)) {
        __builtin_amdgcn_fence(__ATOMIC_ACQUIRE, "agent");
        attn_slow(kv, lane, QA16, QA8, OA);
        attn_slow(kv, lane, QB16, QB8, OB);
    }
    const int u = 2 * (head & 7) + hi;
    const unsigned vo = (l31 >> 4) * 20480 + (4 * (head >> 3) + (u >> 2)) * 1024 + ((u & 3) * 16 + tk) * 16;
    const unsigned vs = (l31 >> 4) * 20480 + 16384 + head * 128 + tk * 8;
    sfor<0, 2>([&](auto T) {
        const f32x16& O = T.value == 0 ? OA : OB;
        auto r = __builtin_amdgcn_permlane32_swap(__float_as_uint(O[8]), __float_as_uint(O[8]), false, false);
        const float inv = __builtin_amdgcn_rcpf(__uint_as_float(r[1]));
        const char* ob = ws + WS_O + (size_t)(2 * tta + 2 * T.value) * 20480;
        gst128(vo, ob, u32x4{pk2(O[0] * inv, O[1] * inv), pk2(O[2] * inv, O[3] * inv), pk2(O[4] * inv, O[5] * inv), pk2(O[6] * inv, O[7] * inv)});
        if (hi == 0) gst64(vs, ob, u32x2{pk2(O[8] * inv, O[9] * inv), pk2(O[10] * inv, O[11] * inv)});
    });
}

#define MF16(d, a, b) "v_mfma_f32_16x16x32_bf16 %[" #d "], %[" #a "], %[" #b "], %[" #d "]\n\t"
#define DSR(d, p, o) "ds_read_b128 %[" #d "], %[" #p "] offset:%c[off]+" #o "\n\t"
template <int OFF, int VM>
DI void step5(f32x4 (&acc)[5], const u32x4 (&c)[5], u32x4 (&n)[5], u32x4 a, unsigned pa, unsigned pb, unsigned pc) {
    asm volatile("s_waitcnt vmcnt(%c[vm]) lgkmcnt(2)\n\t"
                 MF16(d0, c0, a) DSR(n0, pa, 0) DSR(n1, pa, 1024) MF16(d1, c1, a) DSR(n2, pb, 0) DSR(n3, pb, 1024) MF16(d2, c2, a) DSR(n4, pc, 0)
                 "s_waitcnt lgkmcnt(5)\n\t" MF16(d3, c3, a) MF16(d4, c4, a)
                 : [d0] "+v"(acc[0]), [d1] "+v"(acc[1]), [d2] "+v"(acc[2]), [d3] "+v"(acc[3]), [d4] "+v"(acc[4]),
                   [n0] "=&v"(n[0]), [n1] "=&v"(n[1]), [n2] "=&v"(n[2]), [n3] "=&v"(n[3]), [n4] "=&v"(n[4])
                 : [c0] "v"(c[0]), [c1] "v"(c[1]), [c2] "v"(c[2]), [c3] "v"(c[3]), [c4] "v"(c[4]), [a] "v"(a),
                   [pa] "v"(pa), [pb] "v"(pb), [pc] "v"(pc), [off] "i"(OFF), [vm] "i"(VM)
                 : "memory");
}
template <int VM>
DI void last5(f32x4 (&acc)[5], const u32x4 (&c)[5], u32x4 a) {
    asm volatile("s_waitcnt vmcnt(%c[vm]) lgkmcnt(0)\n\t" MF16(d0, c0, a) MF16(d1, c1, a) MF16(d2, c2, a) MF16(d3, c3, a) MF16(d4, c4, a) "s_nop 7"
                 : [d0] "+v"(acc[0]), [d1] "+v"(acc[1]), [d2] "+v"(acc[2]), [d3] "+v"(acc[3]), [d4] "+v"(acc[4])
                 : [c0] "v"(c[0]), [c1] "v"(c[1]), [c2] "v"(c[2]), [c3] "v"(c[3]), [c4] "v"(c[4]), [a] "v"(a), [vm] "i"(VM)
                 : "memory");
}
template <int OFF> DI void first5(u32x4 (&n)[5], unsigned pa, unsigned pb, unsigned pc) {
    asm volatile(DSR(n0, pa, 0) DSR(n1, pa, 1024) DSR(n2, pb, 0) DSR(n3, pb, 1024) DSR(n4, pc, 0)
                 : [n0] "=&v"(n[0]), [n1] "=&v"(n[1]), [n2] "=&v"(n[2]), [n3] "=&v"(n[3]), [n4] "=&v"(n[4])
                 : [pa] "v"(pa), [pb] "v"(pb), [pc] "v"(pc), [off] "i"(OFF) : "memory");
}
template <int OFF, int VM, bool RD>
DI void step7(f32x4 (&acc)[2][7], const u32x4 (&c)[7], u32x4 (&n)[7], u32x4 a, u32x4 b, unsigned p) {
    if constexpr (RD) {
        asm volatile("s_waitcnt vmcnt(%c[vm]) lgkmcnt(3)\n\t"
                     MF16(d0, c0, a) DSR(n0, p, 0) MF16(e0, c0, b) MF16(d1, c1, a) DSR(n1, p, 1024) MF16(e1, c1, b)
                     MF16(d2, c2, a) DSR(n2, p, 2048) MF16(e2, c2, b) MF16(d3, c3, a) DSR(n3, p, 3072) MF16(e3, c3, b)
                     : [d0] "+v"(acc[0][0]), [d1] "+v"(acc[0][1]), [d2] "+v"(acc[0][2]), [d3] "+v"(acc[0][3]),
                       [e0] "+v"(acc[1][0]), [e1] "+v"(acc[1][1]), [e2] "+v"(acc[1][2]), [e3] "+v"(acc[1][3]),
                       [n0] "=&v"(n[0]), [n1] "=&v"(n[1]), [n2] "=&v"(n[2]), [n3] "=&v"(n[3])
                     : [c0] "v"(c[0]), [c1] "v"(c[1]), [c2] "v"(c[2]), [c3] "v"(c[3]), [a] "v"(a), [b] "v"(b), [p] "v"(p), [off] "i"(OFF), [vm] "i"(VM)
                     : "memory");
        asm volatile("s_waitcnt lgkmcnt(4)\n\t"
                     MF16(d0, c0, a) DSR(n0, p, 4096) MF16(e0, c0, b) MF16(d1, c1, a) DSR(n1, p, 5120) MF16(e1, c1, b)
                     MF16(d2, c2, a) DSR(n2, p, 6144) MF16(e2, c2, b)
                     : [d0] "+v"(acc[0][4]), [d1] "+v"(acc[0][5]), [d2] "+v"(acc[0][6]), [e0] "+v"(acc[1][4]), [e1] "+v"(acc[1][5]), [e2] "+v"(acc[1][6]),
                       [n0] "=&v"(n[4]), [n1] "=&v"(n[5]), [n2] "=&v"(n[6])
                     : [c0] "v"(c[4]), [c1] "v"(c[5]), [c2] "v"(c[6]), [a] "v"(a), [b] "v"(b), [p] "v"(p), [off] "i"(OFF)
                     : "memory");
    } else {
        asm volatile("s_waitcnt vmcnt(%c[vm]) lgkmcnt(0)\n\t"
                     MF16(d0, c0, a) MF16(e0, c0, b) MF16(d1, c1, a) MF16(e1, c1, b) MF16(d2, c2, a) MF16(e2, c2, b) MF16(d3, c3, a) MF16(e3, c3, b)
                     : [d0] "+v"(acc[0][0]), [d1] "+v"(acc[0][1]), [d2] "+v"(acc[0][2]), [d3] "+v"(acc[0][3]),
                       [e0] "+v"(acc[1][0]), [e1] "+v"(acc[1][1]), [e2] "+v"(acc[1][2]), [e3] "+v"(acc[1][3])
                     : [c0] "v"(c[0]), [c1] "v"(c[1]), [c2] "v"(c[2]), [c3] "v"(c[3]), [a] "v"(a), [b] "v"(b), [vm] "i"(VM)
                     : "memory");
        asm volatile(MF16(d0, c0, a) MF16(e0, c0, b) MF16(d1, c1, a) MF16(e1, c1, b) MF16(d2, c2, a) MF16(e2, c2, b) "s_nop 7"
                     : [d0] "+v"(acc[0][4]), [d1] "+v"(acc[0][5]), [d2] "+v"(acc[0][6]), [e0] "+v"(acc[1][4]), [e1] "+v"(acc[1][5]), [e2] "+v"(acc[1][6])
                     : [c0] "v"(c[4]), [c1] "v"(c[5]), [c2] "v"(c[6]), [a] "v"(a), [b] "v"(b)
                     : "memory");
    }
}
template <int VM> DI u32x4 join2(u32x2& p, u32x2& q) {
    asm volatile("s_waitcnt vmcnt(%c2)" : "+v"(p), "+v"(q) : "i"(VM) : "memory");
    u32x4 a = {p[0], p[1], q[0], q[1]};
    asm volatile("s_nop 0" : "+v"(a));
    return a;
}

constexpr int L_CTR = L_X + 1024, L_ST = L_X + 2048;
#define H2_AT 2
#define UP_P 5
#define DN_P 6

DI void swap_halves(lptr lds, int w, int lane, f32x4 (&acc)[5]) {
    const int kh = w >> 2, tw = w & 3;
    const lptr mine = lds + L_X + tw * 5120 + kh * 2048 + lane * 16, other = lds + L_X + tw * 5120 + (1 - kh) * 2048 + lane * 16;
    *(LAS f32x4*)mine = acc[2];
    *(LAS f32x4*)(mine + 1024) = acc[3];
    if (kh) *(LAS f32x4*)(mine + 2048) = acc[4];
    lds_bar();
    acc[0] += *(LAS f32x4*)other;
    acc[1] += *(LAS f32x4*)(other + 1024);
    if (!kh) acc[4] += *(LAS f32x4*)(other + 2048);
}

DI void wo_phase(const u16* x, char* ws, lptr lds, int b, int w, int lane, u32x2 (&hk)[3], u32x4 (&held)[33], u64 tag STARG) {
    const int kh = w >> 2, tk = lane & 15, g = lane >> 4, cg = b >> 5;
    const int tt16 = 16 * (b & 7) + 4 * ((b >> 3) & 3) + (w & 3);
    const unsigned vl = lane * 16;
    u32x2 xr[3];
    const unsigned vxr = (16 * tt16 + tk) * 1280 + 8 * g;
    gld64<0>(xr[0], vxr, (const char*)x + cg * 160 + kh * 64); gld64<32>(xr[1], vxr, (const char*)x + cg * 160 + kh * 64);
    gld64<128>(xr[2], vxr, (const char*)x + cg * 160);
    ST(11);
    lds_bar();
    ST(12);
    const char* osrc = ws + WS_O + (size_t)tt16 * 20480;
    const unsigned vs = g * 256 + tk * 8;
    constexpr int P = 5;
    u32x4 of[8]; u32x2 os[2][2];
    auto ld = [&](auto K_) {
        constexpr int K = K_.value, G = K / 5;
        if constexpr (K % 5 < 4) gld128s<(K % 5) * 1024>(of[K - G], vl, osrc + (2 * kh + G) * 4096);
        else { gld64s<G * 1024>(os[G][0], vs, osrc + 16384 + kh * 2048); gld64s<G * 1024 + 128>(os[G][1], vs, osrc + 16384 + kh * 2048); }
    };
    sfor<0, P>(ld);
    const unsigned wb = (unsigned)(size_t)lds + L_WO + kh * 51200 + vl, pa = wb + kh * 2048, pb = wb + (1 - kh) * 2048, pc = wb + 4096;
    f32x4 acc[5] = {};
    u32x4 wf[2][5];
    sfor<0, 5>([&](auto I) { pin(acc[I.value]); });
    first5<0>(wf[0], pa, pb, pc);
    sfor<0, 10>([&](auto K_) {
        constexpr int K = K_.value;
        constexpr int N = [] { int n = 0; for (int j = K + 1; j < K + P && j < 10; ++j) n += nld_o(j); return n; }();
        u32x4 a;
        if constexpr (K % 5 < 4) a = of[K - K / 5]; else a = join2<N>(os[K / 5][0], os[K / 5][1]);
        if constexpr (K < 9) step5<(K + 1) * 5120, N>(acc, wf[K & 1], wf[(K + 1) & 1], a, pa, pb, pc);
        else last5<N>(acc, wf[K & 1], a);
        if constexpr (K + P < 10) ld(IC<K + P>{});
    });
    ST(13);
    swap_halves(lds, w, lane, acc);
    float s1 = 0.f, s2 = 0.f;
#pragma unroll
    for (int i = 0; i < 3; ++i) {
        pin(xr[i]);
        const f32x4 v = acc[i == 2 ? 4 : i];
        const unsigned a0 = pk2(v[0], v[1]), a1 = pk2(v[2], v[3]), x0 = xr[i][0], x1 = xr[i][1];
        unsigned h0 = pk2(bflo(x0) + bflo(a0), bfhi(x0) + bfhi(a0)), h1 = pk2(bflo(x1) + bflo(a1), bfhi(x1) + bfhi(a1));
        if (i == 2 && kh) { h0 = 0u; h1 = 0u; }
        hk[i] = u32x2{h0, h1};
        s1 = dot2(h0, 0x3f803f80u, dot2(h1, 0x3f803f80u, s1));
        s2 = dot2(h0, h0, dot2(h1, h1, s2));
    }
    const char* hb = ws + WS_H + (size_t)tt16 * 20480;
    gst128(vl, hb + (2 * cg + kh) * 1024, u32x4{hk[0][0], hk[0][1], hk[1][0], hk[1][1]});
    if (!kh) gst64(lane * 8, hb + 16384 + cg * 512, hk[2]);
    s1 = swap32_add(swap16_add(s1)); s2 = swap32_add(swap16_add(s2));
    if (g == 0) gst64(tk * 8, ws + WS_ST + (size_t)(tt16 * 16 + 2 * cg + kh) * 128, u32x2{__float_as_uint(s1), __float_as_uint(s2)});
    if (kh) {
        const lptr d = lds + (7 * (1 + w)) * 1024 + vl;
        sfor<0, 21>([&](auto I) { *(LAS u32x4*)(d + (28 * (I.value / 7) + I.value % 7) * 1024) = held[I.value]; });
        if (w < 7) sfor<21, 28>([&](auto I) { *(LAS u32x4*)(d + (84 + I.value % 7) * 1024) = held[I.value]; });
    }
    ST(14);
    wait_vm_bar<0>();
    if (w == 0) {
        if (lane == 0) flag_set(ws, FL_C + ((2 * (b & 7) + ((b >> 4) & 1)) * 16 + ((b >> 3) & 1) * 8 + cg) * 128, tag);
        flag_wait(ws, FL_C + ((2 * (b & 7) + ((b >> 3) & 1)) * 16 + tk) * 128, tag);
    }
}

DI void up_consumer(char* ws, lptr lds, int b, int w, int lane, u64 tag STARG) {
    const int tk = lane & 15, g = lane >> 4;
    const unsigned vl = lane * 16;
    ST(15);
    lds_bar();
    ST(16);
    const int t0 = 16 * (b & 7) + 8 * ((b >> 3) & 1) + 2 * w, c = b >> 4;
    const char* hsrc = ws + WS_H + (size_t)t0 * 20480;
    constexpr int P = UP_P;
    u32x4 hf[16][2]; u32x2 hs[4][2][2];
    auto ld = [&](auto K_) {
        constexpr int K = K_.value;
        sfor<0, 2>([&](auto T) {
            if constexpr (K < 16) gld128s<(K & 3) * 1024>(hf[K][T.value], vl, hsrc + T.value * 20480 + (K >> 2) * 4096);
            else { gld64s<(K - 16) * 1024>(hs[K - 16][T.value][0], lane * 8, hsrc + T.value * 20480 + 16384); gld64s<(K - 16) * 1024 + 512>(hs[K - 16][T.value][1], lane * 8, hsrc + T.value * 20480 + 16384); }
        });
    };
    sfor<0, P>(ld);
    f32x4 acc[2][7] = {};
    u32x4 wf[2][7];
    const unsigned p0 = (unsigned)(size_t)lds + vl;
    sfor<0, 7>([&](auto I) { wf[0][I.value] = lds128(lds + I.value * 1024 + vl); pin(acc[0][I.value]); pin(acc[1][I.value]); });
    sfor<0, 7>([&](auto I) { pin(wf[0][I.value]); });
    sfor<0, 20>([&](auto K_) {
        constexpr int K = K_.value, NX = K + 1, SEG = NX / 8;
        constexpr int N = [] { int n = 0; for (int j = K + 1; j < K + P && j < 20; ++j) n += nld(j, 16, 2); return n; }();
        u32x4 a0, a1;
        if constexpr (K < 16) { a0 = hf[K][0]; a1 = hf[K][1]; }
        else { a0 = join2<N>(hs[K - 16][0][0], hs[K - 16][0][1]); a1 = join2<N>(hs[K - 16][1][0], hs[K - 16][1][1]); }
        step7<(NX - 8 * SEG) * 7168, N, (K < 19)>(acc, wf[K & 1], wf[NX & 1], a0, a1, p0 + SEG * 57344);
        if constexpr (K + P < 20) ld(IC<K + P>{});
        if constexpr (K == 10 || K == 18) lds_bar();
    });
    ST(17);
    f32x4 cs[7], bv[7];
#pragma unroll
    for (int i = 0; i < 7; ++i) { cs[i] = *(LAS f32x4*)(lds + L_X + (16 * i + 4 * g) * 4); bv[i] = *(LAS f32x4*)(lds + L_X + 512 + (16 * i + 4 * g) * 4); }
    float mr[2], rs[2];
#pragma unroll
    for (int t = 0; t < 2; ++t) { const f32x2 v = *(LAS f32x2*)(lds + L_ST + (32 * w + 16 * t + tk) * 8); mr[t] = v[0]; rs[t] = v[1]; }
    u32x2 o[2][7];
    sfor<0, 7>([&](auto I) {
        constexpr int i = I.value;
        f32x2 a[4], z[4];
#pragma unroll
        for (int q = 0; q < 4; ++q) {
            const int t = q >> 1, e = 2 * (q & 1);
            a[q] = f32x2{acc[t][i][e], acc[t][i][e + 1]} * rs[t] + (f32x2{cs[i][e], cs[i][e + 1]} * mr[t] + f32x2{bv[i][e], bv[i][e + 1]});
        }
#pragma unroll
        for (int q = 0; q < 4; ++q) z[q] = a[q] * (a[q] * a[q] * GELU_C1 + GELU_C0);
#pragma unroll
        for (int q = 0; q < 4; ++q) z[q] = f32x2{__builtin_amdgcn_exp2f(z[q][0]), __builtin_amdgcn_exp2f(z[q][1])};
#pragma unroll
        for (int q = 0; q < 4; ++q) z[q] += 1.0f;
#pragma unroll
        for (int q = 0; q < 4; ++q) z[q] = f32x2{__builtin_amdgcn_rcpf(z[q][0]), __builtin_amdgcn_rcpf(z[q][1])};
#pragma unroll
        for (int q = 0; q < 4; ++q) { a[q] *= z[q]; o[q >> 1][i][q & 1] = pk2(a[q][0], a[q][1]); }
        pin(o[0][i]); pin(o[1][i]);
        SBAR();
        if constexpr (i & 1) sfor<0, 2>([&](auto T) {
            gst128(vl, ws + WS_G + (size_t)((t0 + T.value) * 54 + 3 * c + (i >> 1)) * 1024, u32x4{o[T.value][i - 1][0], o[T.value][i - 1][1], o[T.value][i][0], o[T.value][i][1]});
        });
    });
    if ((c & 3) != 3) sfor<0, 2>([&](auto T) { gst64(lane * 8, ws + WS_G + (size_t)((t0 + T.value) * 54 + 48) * 1024 + (3 * (c >> 2) + (c & 3)) * 512, o[T.value][6]); });
    ST(18);
    wait_vm<0>();
    unsigned old = 0;
    if (lane == 0) old = __hip_atomic_fetch_add((LAS unsigned*)(lds + L_CTR), 1u, __ATOMIC_RELAXED, __HIP_MEMORY_SCOPE_WORKGROUP);
    if (__builtin_amdgcn_readfirstlane(old) == 3u) {
        if (lane == 0) flag_set(ws, FL_D + ((2 * (b & 7) + ((b >> 3) & 1)) * 16 + c) * 128, tag);
        flag_wait(ws, FL_D + ((2 * (b & 7) + ((b >> 4) & 1)) * 16 + tk) * 128, tag);
    }
}

template <int KH>
DI void down_phase(const u16* b2, char* ws, u16* out, lptr lds, int b, int w, int lane, const u32x2 (&hk)[3], u32x4 (&held)[33] STARG) {
    const int tk = lane & 15, g = lane >> 4, cg = b >> 5;
    const int tt16 = 16 * (b & 7) + 4 * ((b >> 3) & 3) + (w & 3);
    const unsigned vl = lane * 16;
    u32x2 bv[3];
    gld64<KH * 64>(bv[0], 8 * g, (const char*)b2 + cg * 160); gld64<KH * 64 + 32>(bv[1], 8 * g, (const char*)b2 + cg * 160); gld64<128>(bv[2], 8 * g, (const char*)b2 + cg * 160);
    ST(19);
    if constexpr (KH) { wait_vm_bar<0>(); sfor<0, 33>([&](auto I) { pin(held[I.value]); }); } else lds_bar();
    ST(20);
    const char* gsrc = ws + WS_G + (size_t)tt16 * 54 * 1024;
    constexpr int P = DN_P;
    u32x4 gf[24]; u32x2 gs[3][2];
    auto ld = [&](auto S_) {
        constexpr int S = S_.value;
        if constexpr (S < 24) gld128s<(S & 3) * 1024>(gf[S], vl, gsrc + (6 * KH + (S >> 2)) * 4096);
        else { gld64s<(S - 24) * 1024>(gs[S - 24][0], lane * 8, gsrc + (48 + 3 * KH) * 1024); gld64s<(S - 24) * 1024 + 512>(gs[S - 24][1], lane * 8, gsrc + (48 + 3 * KH) * 1024); }
    };
    sfor<0, P>(ld);
    f32x4 acc[5] = {};
    u32x4 wf[2][5];
    const unsigned wb = (unsigned)(size_t)lds + KH * 71680 + vl, pa = wb + KH * 2048, pb = wb + (1 - KH) * 2048, pc = wb + 4096;
    sfor<0, 5>([&](auto I) { pin(acc[I.value]); });
    first5<0>(wf[0], pa, pb, pc);
    sfor<0, 27>([&](auto S_) {
        constexpr int S = S_.value, SL = (S + 1) % 14, UP = SL >= 12 ? 61440 : 0;
        constexpr int N = [] { int n = 0; for (int j = S + 1; j < S + P && j < 27; ++j) n += nld(j, 24, 1); return n; }();
        if constexpr (S == 6 || S == 13 || S == 20) lds_bar();
        if constexpr (KH && S == 6) {
            const lptr d = lds + ((w >> 1) & 1) * 71680 + (w & 1) * 17408 + vl;
            sfor<0, 18>([&](auto I) { *(LAS u32x4*)(d + I.value * 1024) = held[I.value]; });
        }
        if constexpr (KH && S == 13) {
            const lptr d = lds + ((w >> 1) & 1) * 71680 + 35840 + (w & 1) * 15360 + vl;
            sfor<0, 15>([&](auto I) { *(LAS u32x4*)(d + I.value * 1024) = held[18 + I.value]; });
        }
        u32x4 a;
        if constexpr (S < 24) a = gf[S]; else a = join2<N>(gs[S - 24][0], gs[S - 24][1]);
        if constexpr (S < 26) step5<SL * 5120 - UP, N>(acc, wf[S & 1], wf[(S + 1) & 1], a, pa + UP, pb + UP, pc + UP);
        else last5<N>(acc, wf[S & 1], a);
        if constexpr (S + P < 27) ld(IC<S + P>{});
    });
    ST(21);
    swap_halves(lds, w, lane, acc);
    char* ob = (char*)out + (size_t)(16 * tt16 + tk) * 1280 + cg * 160 + 8 * g;
#pragma unroll
    for (int i = 0; i < 3 - KH; ++i) {
        pin(bv[i]);
        const f32x4 v = acc[i == 2 ? 4 : i];
        const unsigned h0 = hk[i][0], h1 = hk[i][1];
        const unsigned y0 = pk2(v[0], v[1]), y1 = pk2(v[2], v[3]), b0 = bv[i][0], b1 = bv[i][1];
        const unsigned z0 = pk2(bflo(y0) + bflo(b0), bfhi(y0) + bfhi(b0)), z1 = pk2(bflo(y1) + bflo(b1), bfhi(y1) + bfhi(b1));
        *(u32x2*)(ob + (i == 2 ? 128 : KH * 64 + 32 * i)) = u32x2{pk2(bflo(h0) + bflo(z0), bfhi(h0) + bfhi(z0)), pk2(bflo(h1) + bflo(z1), bfhi(h1) + bfhi(z1))};
    }
}

DI void glds_run(unsigned vl, const char* src, int sstep, unsigned dst, int dstep, int n) {
#pragma unroll 1
    for (int i = 0; i < n; ++i) { glds_s(vl, src, dst); src += sstep; dst += dstep; }
}

constexpr bool kv_live(int i) { const int e = i % 208; return e < 80 || ((e - 80) & 31) < 20; }
constexpr unsigned kv_mask(int piece, int half) { unsigned m = 0; for (int l = 0; l < 32; ++l) m |= kv_live(piece * 64 + half * 32 + l) ? 1u << l : 0u; return m; }

template <int J>
DI void kv_loader(const char* kv, unsigned ldsb, int lane) {
    constexpr int P0 = J == 0 ? 0 : 7, NP = J == 0 ? 7 : 6;
    unsigned vo[NP];
#pragma unroll
    for (int p = 0; p < NP; ++p) {
        const int i = (P0 + p) * 64 + lane, r = i / 208, e = i - 208 * r, v = e - 80;
        vo[p] = r * KVREC + (e < 80 ? KVREC + e * 16 : 1280 + (v >> 5) * 320 + (v & 31) * 16);
    }
    auto issue = [&](int c) {
        const char* src = kv + c * 4 * KVREC;
        const unsigned dst = ldsb + (c & 1) * KV_SLOT;
        sfor<0, NP>([&](auto Pp) { glds_m<(P0 + Pp.value) * 1024, kv_mask(P0 + Pp.value, 0), kv_mask(P0 + Pp.value, 1)>(vo[Pp.value], src, dst); });
    };
    lds_bar();
    issue(0);
#pragma unroll 1
    for (int c = 0; c < 15; ++c) { wait_vm_bar<0>(); issue(c + 1); }
    wait_vm_bar<0>();
    lds_bar();
}

DI void retile_loader(const u16* wo, const u16* w1, const u16* w2, char* ws, lptr lds, int b, int j, int lane, u64 tag) {
    const unsigned ldsb = (unsigned)(size_t)lds, vl = lane * 16;
    const int first = j == 2 ? 0 : 3, nt = j == 2 ? 3 : 2;
    const int kind = b < 108 ? 1 : b < 148 ? 0 : 2;
    const char* src = kind == 0 ? (const char*)wo : kind == 1 ? (const char*)w1 : (const char*)w2;
    const int pitch = kind == 1 ? FF * 2 : DM * 2;
    const int rel = kind == 1 ? b : kind == 0 ? b - 108 : 5 * (b - 148), div = kind == 1 ? 27 : 10, quo = rel / div;
    auto task = [&](int i, int& kap, int& col64) {
        const int c = rel - quo * div + (kind == 2 ? i : 0), wrap = c >= 10 && kind == 2;
        col64 = wrap ? c - 10 : c;
        kap = kind == 2 ? quo + wrap : 5 * quo + i;
    };
    {
        const int l31 = lane & 31;
        const unsigned fill = l31 == 20 ? 0x3f803f80u : 0u;
        if (l31 >= 20) {
#pragma unroll
            for (int i = 0; i < 8; ++i) *(LAS u32x4*)(lds + (j - 2) * KV_SLOT + (i >> 1) * KV_LREC + 1280 + (i & 1) * 1024 + vl) = u32x4{fill, fill, fill, fill};
        }
    }
    {
        const int gl = lane >> 5, q = (lane >> 3) & 3, swz = ((lane & 7) ^ (2 * ((lane >> 4) & 3))) << 4;
#pragma unroll 1
        for (int i = first; i < first + nt; ++i) {
            int kap, col64;
            task(i, kap, col64);
            const int G = kap / 5, r = kap - 5 * G, c3 = kap / 3;
            int k0[2];
#pragma unroll
            for (int t = 0; t < 2; ++t) {
                const int lam = 2 * (kap - 48) + t, l3 = lam / 3;
                const int f1 = kap < 16 ? 5 * (kap >> 1) + 2 * (kap & 1) + t : 10 * (kap - 16) + 5 * t + 4;
                const int f2 = kap < 48 ? up_start(c3) + 2 * (kap - 3 * c3) + t : 27 * l3 + 7 * (lam - 3 * l3) + 6;
                const int r0 = r < 4 ? 160 * G + 40 * r + 8 * t : 160 * G + 20 * t + 16;
                k0[t] = kind == 0 ? r0 : 16 * (kind == 1 ? f1 : f2);
            }
            const int slo = kind == 0 && r == 4 ? 40 : 4, shi = kind == 0 ? (r == 4 ? 80 : 20) : 8;
            const unsigned v0 = (slo * gl + q) * pitch + swz;
            sfor<0, 4>([&](auto J) { glds<L_X + J.value * 1024>(v0, src + col64 * 128 + (k0[J.value >> 1] + (J.value & 1) * shi) * pitch, ldsb + i * 4096); });
        }
    }
    const int g = lane >> 4, q = (lane >> 2) & 3, p = lane & 3;
    lptr tp[4];
#pragma unroll
    for (int jj = 0; jj < 4; ++jj) { tp[jj] = lds + L_X + (4 * g + q) * 128 + 8 * (p & 1) + (((2 * jj + (p >> 1)) ^ (2 * (((4 * g + q) >> 1) & 3))) << 4); pin(tp[jj]); }
    lds_bar(); lds_bar(); lds_bar(); lds_bar();
    wait_vm<0>();
    f32x4 cs[4] = {};
#pragma unroll 1
    for (int i = first; i < first + 3; ++i) {
        if (i < 5) {
            int kap, col64;
            task(i, kap, col64);
            u32x4 f[4];
#pragma unroll
            for (int jj = 0; jj < 4; ++jj) {
                const u32x2 a = ldstr(tp[jj] + i * 4096), c = ldstr(tp[jj] + i * 4096 + 2048);
                f[jj] = u32x4{a[0], a[1], c[0], c[1]};
            }
#pragma unroll
            for (int jj = 0; jj < 4; ++jj) {
                const int ft = 4 * col64 + jj, f5 = ft / 5;
                const size_t piece = (kind == 0 ? WS_WO : kind == 1 ? WS_W1 : WS_W2) +
                                     (size_t)(kind == 1 ? ft * 20 + kap : (f5 * (kind == 0 ? 20 : 54) + kap) * 5 + ft - 5 * f5) * 1024;
                gst128(vl, ws + piece, f[jj]);
            }
            if (kind == 1) {
#pragma unroll
                for (int jj = 0; jj < 4; ++jj) cs[jj] = mfma16(f[jj], u32x4{0x3f803f80u, 0x3f803f80u, 0x3f803f80u, 0x3f803f80u}, cs[jj]);
            }
        }
        lds_bar();
    }
    asm volatile("s_nop 7" : "+v"(cs[0]), "+v"(cs[1]), "+v"(cs[2]), "+v"(cs[3]));
    if (kind == 1 && (lane & 15) == 0) {
#pragma unroll
        for (int jj = 0; jj < 4; ++jj)
            gst128(g * 16 + jj * 64, ws + WS_CS + (size_t)((2 * (b / 27) + j - 2) * FF + 64 * (b % 27)) * 4,
                   u32x4{__float_as_uint(cs[jj][0]), __float_as_uint(cs[jj][1]), __float_as_uint(cs[jj][2]), __float_as_uint(cs[jj][3])});
    }
    wait_vm_bar<0>();
    if (j == 3 && lane == 0) flag_set(ws, FL_R + b * 8, tag);
    lds_bar(); lds_bar();
    if (j == 2) {
        flag_wait2(ws, FL_R + lane * 8, FL_R + 512 + lane * 8, tag);
        flag_wait2(ws, FL_R + 1024 + lane * 8, FL_R + 1536 + lane * 8, tag);
        if (b == 0 && lane == 0) flag_set(ws, FL_E, tag);
    }
    lds_bar();
    const char* wsrc = ws + WS_WO + (size_t)(b >> 5) * 102400 + (j - 2) * 51200;
#pragma unroll 1
    for (int i = 0; i < 5; ++i) {
        glds_run(vl, wsrc + i * 10240, 1024, ldsb + L_WO + (j - 2) * 51200 + i * 10240, 1024, 10);
        const int ch = (b >> 3) + 32 * (2 * i + j - 2);
        if (WARM1 && ch < 270) warm64(lane * 128, ws + WS_W1 + (size_t)ch * 8192, ldsb + L_X + 4096);
        lds_bar();
    }
    lds_bar(); lds_bar();
}

__global__ __launch_bounds__(512, 2) void k_block(const u16* x, const u16* wqkv, const u16* wo, const u16* w1, const u16* b1, const u16* w2, const u16* b2, char* ws, u16* out) {
    extern __shared__ __attribute__((aligned(16))) char smem[];
    const lptr lds = (lptr)smem;
    const unsigned ldsb = (unsigned)(size_t)lds;
    const int b = blockIdx.x, w = __builtin_amdgcn_readfirstlane(threadIdx.x >> 6), lane = threadIdx.x & 63, j = w - 4;
    const unsigned vl = lane * 16;
    const int head = 4 * (b & 7) + ((b >> 3) & 3), c = b >> 4, ft0 = up_start(c);
    STDECL; STR(0);
    u32x2 seed;
    gld64s<0>(seed, 0, ws + FL_E);
    const int tbq = 4 * ((b & 7) >> 1) + ((b >> 3) & 3), cgq = (b & 1) * 8 + (b >> 5);
    if (cgq < 15) qkv_phase(x, wqkv, ws, lds, tbq, cgq, w, lane STPASS);
    ST(4);
    wait_vm_bar<0>();
    pin(seed);
    const u64 tag = (((u64)(unsigned)__builtin_amdgcn_readfirstlane(seed[1]) << 32) | (u64)(unsigned)__builtin_amdgcn_readfirstlane(seed[0])) * 6364136223846793005ull + 1442695040888963407ull;
    if (cgq < 15 && threadIdx.x == 0) flag_set(ws, FL_A + (cgq * 16 + tbq) * 128, tag);
    auto w1_part = [&](int k0, int n) {
#pragma unroll 1
        for (int i = j; i < 7; i += 4)
            glds_run(vl, ws + WS_W1 + (size_t)((ft0 + (i == 6 && (c & 3) == 3 ? 5 : i)) * 20 + k0) * 1024, 1024, ldsb + (7 * k0 + i) * 1024, 7168, n);
    };
    float tcs = 0.f, tb1 = 0.f;
    u32x4 held[33];
    if (w < 4) {
        attn_consumer(ws, lds, b, w, lane, tag STPASS);
        ST(10);
        wait_vm_bar<0>();
        if (w == 0) {
            if (lane == 0) flag_set(ws, FL_B + ((b >> 5) * 32 + head) * 128, tag);
            flag_wait(ws, FL_B + ((b & 7) * 32 + (lane & 31)) * 128, tag);
        }
    } else {
        __builtin_amdgcn_s_setprio(3);
        const char* kv = ws + WS_KV + (size_t)head * 64 * KVREC;
        if (j == 0) kv_loader<0>(kv, ldsb, lane);
        else if (j == 1) kv_loader<1>(kv, ldsb, lane);
        else retile_loader(wo, w1, w2, ws, lds, b, j, lane, tag);
        __builtin_amdgcn_s_setprio(0);
        wait_vm<0>();
        w1_part(0, 5);
        sfor<0, 28>([&](auto I) {
            constexpr int m = I.value / 7, i = I.value % 7;
            if (m < 3 || j < 3) gld128s<0>(held[I.value], vl, ws + WS_W1 + (size_t)((ft0 + (i == 6 && (c & 3) == 3 ? 5 : i)) * 20 + 5 + j + 4 * m) * 1024);
        });
        lds_bar();
        if (j >= 2) {
            const int n = min(16 * ft0 + 64 * (j - 2) + lane, FF - 1);
#pragma unroll
            for (int i = 0; i < 8; ++i) tcs += __uint_as_float(__hip_atomic_load((gu32*)(ws + WS_CS + (size_t)(i * FF + n) * 4), __ATOMIC_RELAXED, __HIP_MEMORY_SCOPE_AGENT));
            tb1 = bflo(b1[n]);
            pin(tcs); pin(tb1);
        }
        wait_vm<0>();
        sfor<0, 28>([&](auto I) { pin(held[I.value]); });
    }
    u32x2 hk[3];
    wo_phase(x, ws, lds, b, w, lane, hk, held, tag STPASS);
    if (w < 4) {
        up_consumer(ws, lds, b, w, lane, tag STPASS);
        down_phase<0>(b2, ws, out, lds, b, w, lane, hk, held STPASS);
        ST(22); STWAIT; STR(23); STFLUSH(0);
    } else {
        const char* w2s = ws + WS_W2 + (size_t)(b >> 5) * 276480;
        auto hold_w2 = [&] {
            const char* ha = w2s + (j >> 1) * 122880 + 71680 + (j & 1) * 17408;
            sfor<0, 18>([&](auto I) { gld128s<(I.value & 3) * 1024>(held[I.value], vl, ha + (I.value >> 2) * 4096); });
            const char* hb = w2s + ((j & 1) ? 245760 + (j >> 1) * 15360 : (j >> 1) * 122880 + 107520);
            sfor<0, 15>([&](auto I) { gld128s<(I.value & 3) * 1024>(held[18 + I.value], vl, hb + (I.value >> 2) * 4096); });
        };
        lds_bar();
        {
            u32x2 st[2][4];
            const int t0 = 16 * (b & 7) + 8 * ((b >> 3) & 1) + 2 * j;
            sfor<0, 8>([&](auto I) { gld64s<(I.value >> 2) * 2048 + (I.value & 3) * 128>(st[I.value >> 2][I.value & 3], (lane >> 4) * 512 + (lane & 15) * 8, ws + WS_ST + (size_t)t0 * 2048); });
            wait_vm<0>();
            sfor<0, 2>([&](auto T) {
                float s1 = 0.f, s2 = 0.f;
                sfor<0, 4>([&](auto I) { pin(st[T.value][I.value]); const unsigned u0 = st[T.value][I.value][0], u1 = st[T.value][I.value][1]; s1 += __uint_as_float(u0); s2 += __uint_as_float(u1); });
                float m, rs;
                ln_stats(swap32_add(swap16_add(s1)), swap32_add(swap16_add(s2)), m, rs);
                if (lane < 16) *(LAS f32x2*)(lds + L_ST + (32 * j + 16 * T.value + lane) * 8) = f32x2{-m * rs, rs};
            });
        }
        if (j >= 2) { ((LAS float*)(lds + L_X))[64 * (j - 2) + lane] = tcs; ((LAS float*)(lds + L_X))[128 + 64 * (j - 2) + lane] = tb1; }
        if (j == 0 && lane == 0) *(LAS unsigned*)(lds + L_CTR) = 0u;
        lds_bar();
        glds_run(vl, w2s + j * 17408, 1024, ldsb + j * 17408, 1024, j == 3 ? 19 : 17);
        if (j < 2) glds_run(vl, w2s + 122880 + j * 5120, 1024, ldsb + 71680 + j * 5120, 1024, 5);
        lds_bar();
        glds_run(vl, w2s + 133120 + j * 15360, 1024, ldsb + 81920 + j * 15360, 1024, 15);
        hold_w2();
        down_phase<1>(b2, ws, out, lds, b, w, lane, hk, held STPASS);
        ST(22); STWAIT; STR(23); STFLUSH(0);
    }
}

extern "C" void kernel_launch(void* const* d_in, const int* in_sizes, int n_in, void* d_out, int out_size, void* d_ws, size_t ws_size, hipStream_t stream) {
    (void)hipFuncSetAttribute((const void*)k_block, hipFuncAttributeMaxDynamicSharedMemorySize, M_LDS);
    k_block<<<256, 512, M_LDS, stream>>>((const u16*)d_in[0], (const u16*)d_in[1], (const u16*)d_in[2], (const u16*)d_in[4], (const u16*)d_in[5],
                                         (const u16*)d_in[6], (const u16*)d_in[7], (char*)d_ws, (u16*)d_out);
}
```
